```python
import torch

def _softmax(x, axis=-1):
    return torch.softmax(x, dim=axis)
def _gelu(x):
    return torch.nn.functional.gelu(x, approximate="tanh")
def _silu(x):
    return torch.nn.functional.silu(x)
def _sigmoid(x):
    return torch.sigmoid(x)
def _layernorm(x, axis=-1, eps=1e-5):
    m = x.mean(dim=axis, keepdim=True)
    v = x.var(dim=axis, keepdim=True, unbiased=False)
    return (x - m) / torch.sqrt(v + eps)
def _scatter_add(tgt, idx, src, axis=0):
    return tgt.clone().index_add_(axis, idx, src)
def _segment_sum(src, seg, n):
    out = torch.zeros((n,) + src.shape[1:], dtype=src.dtype)
    return out.index_add_(0, seg, src)
def _pad(x, pw):
    flat = tuple(v for pair in reversed(pw) for v in pair)
    return torch.nn.functional.pad(x, flat)

kernel_name = 'parallel_sum__ssm_conv_s5__mixer_channel_mlp__prompt'

def setup_inputs():

    _w = (lambda a: (lambda t: t.to(torch.bfloat16) if t.is_floating_point() else t)(torch.as_tensor(a)))
    def _A_setup():
        B, S, D = 1, 2048, 640
        P = D
        Lambda_re = -0.5 * (lambda _x: torch.exp(torch.as_tensor(_x)))((lambda *s: torch.randn(*s, dtype=torch.bfloat16) * 0.2)(P))
        Lambda_im = (lambda *s: torch.randn(*s, dtype=torch.bfloat16) * 0.8)(P) * 1.0
        B_re = (lambda *s: torch.randn(*s, dtype=torch.bfloat16) * 0.2)(P, D) * (D ** -0.5)
        B_im = (lambda *s: torch.randn(*s, dtype=torch.bfloat16) * 0.2)(P, D) * (D ** -0.5)
        C_re = (lambda *s: torch.randn(*s, dtype=torch.bfloat16) * 0.2)(D, P) * (P ** -0.5)
        C_im = (lambda *s: torch.randn(*s, dtype=torch.bfloat16) * 0.2)(D, P) * (P ** -0.5)
        log_dt = (lambda *s: torch.randn(*s, dtype=torch.bfloat16) * 0.2)(P) * 0.3 - 2.0
        Dskip = (lambda *s: torch.randn(*s, dtype=torch.bfloat16) * 0.05)(D)
        return {
            "x": _w((lambda *s: torch.randn(*s, dtype=torch.bfloat16) * 0.05)(B, S, D)),
            "Lambda_re": _w(Lambda_re),
            "Lambda_im": _w(Lambda_im),
            "B_re": _w(B_re),
            "B_im": _w(B_im),
            "C_re": _w(C_re),
            "C_im": _w(C_im),
            "log_dt": _w(log_dt),
            "Dskip": _w(Dskip),
        }
    _A_in = _A_setup()

    def _B_setup():
        B, S, D, F = 1, 2048, 640, 1536
        return {
            "x": _w((lambda *s: torch.randn(*s, dtype=torch.bfloat16) * 0.05)(B, S, D)),
            "w1": _w((lambda *s: torch.randn(*s, dtype=torch.bfloat16) * 1.6)(D, F) * (D ** -0.5)),
            "b1": _w((lambda *s: torch.randn(*s, dtype=torch.bfloat16) * 0.8)(F)),
            "w2": _w((lambda *s: torch.randn(*s, dtype=torch.bfloat16) * 0.05)(F, D) * (F ** -0.5)),
            "b2": _w((lambda *s: torch.randn(*s, dtype=torch.bfloat16) * 0.05)(D)),
        }
    _B_in = _B_setup()

    return {
        "A_x": _A_in["x"],
        "A_Lambda_re": _A_in["Lambda_re"],
        "A_Lambda_im": _A_in["Lambda_im"],
        "A_B_re": _A_in["B_re"],
        "A_B_im": _A_in["B_im"],
        "A_C_re": _A_in["C_re"],
        "A_C_im": _A_in["C_im"],
        "A_log_dt": _A_in["log_dt"],
        "A_Dskip": _A_in["Dskip"],
        "B_x": _B_in["x"],
        "B_w1": _B_in["w1"],
        "B_b1": _B_in["b1"],
        "B_w2": _B_in["w2"],
        "B_b2": _B_in["b2"],
    }

def reference(A_x, A_Lambda_re, A_Lambda_im, A_B_re, A_B_im, A_C_re, A_C_im, A_log_dt, A_Dskip, B_x, B_w1, B_b1, B_w2, B_b2):
    def _A_ref(x, Lambda_re, Lambda_im, B_re, B_im, C_re, C_im, log_dt, Dskip):
        Bsz, L, H = x.shape
        Lam = Lambda_re + 1j * Lambda_im
        Bmat = B_re + 1j * B_im
        Cmat = C_re + 1j * C_im
        dt = torch.exp(log_dt)
        Lam_bar = torch.exp(dt * Lam)
        B_bar = ((Lam_bar - 1.0) / Lam)[:, None] * Bmat
        Bu = torch.einsum("bld,pd->blp", x + 0j, B_bar)
        h = torch.zeros_like(Bu[:, 0, :])
        hs = []
        for t in range(L):
            h = Lam_bar[None, :] * h + Bu[:, t, :]
            hs.append(h)
        hseq = torch.stack(hs, 1)
        y = 2.0 * torch.einsum("blp,dp->bld", hseq, Cmat).real + x * Dskip[None, None, :]
        return y

    def _B_ref(x, w1, b1, w2, b2):
        y = _layernorm(x, axis=-1)
        h = _gelu(torch.einsum("bsd,df->bsf", y, w1) + b1)
        return torch.einsum("bsf,fd->bsd", h, w2) + b2

    x = A_x
    xn = _layernorm(x, axis=-1)
    return x + _A_ref(xn, A_Lambda_re, A_Lambda_im, A_B_re, A_B_im, A_C_re, A_C_im, A_log_dt, A_Dskip) + _B_ref(xn, B_w1, B_b1, B_w2, B_b2)
```

```cpp
#include <hip/hip_runtime.h>
#include <hip/hip_bf16.h>
typedef unsigned short u16;
constexpr int L=2048, D=640, P=640, F=1536;
__device__ __forceinline__ float bf2f(u16 v){ return __uint_as_float(((unsigned)v)<<16); }
__device__ __forceinline__ u16 f2bf(float f){ unsigned u=__float_as_uint(f); u += 0x7fff + ((u>>16)&1); return (u16)(u>>16); }
__device__ __forceinline__ float rb(float f){ return bf2f(f2bf(f)); }

__global__ void k_ln(const u16* x, float* xn, float* y){
  int t=blockIdx.x; int lane=threadIdx.x;
  float v[10]; float s=0;
  for(int i=0;i<10;i++){ v[i]=bf2f(x[t*D+lane+64*i]); s+=v[i]; }
  for(int o=32;o>=1;o>>=1) s+=__shfl_xor(s,o);
  float m=s/D; float q=0;
  for(int i=0;i<10;i++){ float d=v[i]-m; q+=d*d; }
  for(int o=32;o>=1;o>>=1) q+=__shfl_xor(q,o);
  float var=rb(q/D); m=rb(m);
  float den=rb(sqrtf(rb(var+1e-5f)));
  s=0;
  for(int i=0;i<10;i++){ v[i]=rb(rb(v[i]-m)/den); xn[t*D+lane+64*i]=v[i]; s+=v[i]; }
  for(int o=32;o>=1;o>>=1) s+=__shfl_xor(s,o);
  m=s/D; q=0;
  for(int i=0;i<10;i++){ float d=v[i]-m; q+=d*d; }
  for(int o=32;o>=1;o>>=1) q+=__shfl_xor(q,o);
  var=rb(q/D); m=rb(m);
  den=rb(sqrtf(rb(var+1e-5f)));
  for(int i=0;i<10;i++){ y[t*D+lane+64*i]=rb(rb(v[i]-m)/den); }
}
__global__ void k_bu(const float* xn, const u16* Bre, const u16* Bim, float* ure, float* uim){
  int t=blockIdx.x; __shared__ float xs[D];
  for(int i=threadIdx.x;i<D;i+=blockDim.x) xs[i]=xn[t*D+i];
  __syncthreads();
  for(int p=threadIdx.x;p<P;p+=blockDim.x){ float a=0,b=0;
    for(int d=0;d<D;d++){ a+=xs[d]*bf2f(Bre[p*D+d]); b+=xs[d]*bf2f(Bim[p*D+d]); }
    ure[t*P+p]=a; uim[t*P+p]=b; }
}
__global__ void k_scan(const u16* lre,const u16* lim,const u16* ldt,float* ure,float* uim){
  int p=blockIdx.x*64+threadIdx.x;
  float dt=rb(expf(bf2f(ldt[p]))); float a=bf2f(lre[p]), b=bf2f(lim[p]);
  float e=expf(dt*a); float lr=e*cosf(dt*b), li=e*sinf(dt*b);
  float nr=lr-1.f, ni=li; float den=a*a+b*b; float cr=(nr*a+ni*b)/den, ci=(ni*a-nr*b)/den;
  float hr=0,hi=0;
  for(int t=0;t<L;t++){ float ur=ure[t*P+p], ui=uim[t*P+p]; float br=cr*ur-ci*ui, bi=cr*ui+ci*ur;
    float nhr=lr*hr-li*hi+br, nhi=lr*hi+li*hr+bi; hr=nhr; hi=nhi; ure[t*P+p]=hr; uim[t*P+p]=hi; }
}
__global__ void k_h1(const float* y,const u16* w1,const u16* b1,float* h1){
  int t=blockIdx.x; __shared__ float xs[D];
  for(int i=threadIdx.x;i<D;i+=blockDim.x) xs[i]=y[t*D+i];
  __syncthreads();
  for(int f=threadIdx.x;f<F;f+=blockDim.x){ float a=0;
    for(int d=0;d<D;d++) a+=xs[d]*bf2f(w1[d*F+f]);
    a=rb(rb(a)+bf2f(b1[f]));
    float g=0.5f*a*(1.f+tanhf(0.7978845608f*(a+0.044715f*a*a*a)));
    h1[t*F+f]=rb(g); }
}
__global__ void k_out(const u16* x,const float* xn,const float* hre,const float* him,const float* h1,const u16* Cre,const u16* Cim,const u16* Dsk,const u16* w2,const u16* b2,float* out){
  int t=blockIdx.x; __shared__ float hr[P],hi[P],hh[F];
  for(int i=threadIdx.x;i<P;i+=blockDim.x){ hr[i]=hre[t*P+i]; hi[i]=him[t*P+i]; }
  for(int i=threadIdx.x;i<F;i+=blockDim.x) hh[i]=h1[t*F+i];
  __syncthreads();
  for(int d=threadIdx.x;d<D;d+=blockDim.x){ float a=0,b=0;
    for(int p=0;p<P;p++) a+=hr[p]*bf2f(Cre[d*P+p])-hi[p]*bf2f(Cim[d*P+p]);
    for(int f=0;f<F;f++) b+=hh[f]*bf2f(w2[f*D+d]);
    float A=2.f*a+rb(xn[t*D+d]*bf2f(Dsk[d]));
    float B=rb(rb(b)+bf2f(b2[d]));
    out[t*D+d]=bf2f(x[t*D+d])+A+B; }
}
extern "C" void kernel_launch(void* const* d_in, const int* in_sizes, int n_in, void* d_out, int out_size, void* d_ws, size_t ws_size, hipStream_t stream){
  const u16* x=(const u16*)d_in[0]; const u16* lre=(const u16*)d_in[1]; const u16* lim=(const u16*)d_in[2];
  const u16* Bre=(const u16*)d_in[3]; const u16* Bim=(const u16*)d_in[4]; const u16* Cre=(const u16*)d_in[5]; const u16* Cim=(const u16*)d_in[6];
  const u16* ldt=(const u16*)d_in[7]; const u16* Dsk=(const u16*)d_in[8];
  const u16* w1=(const u16*)d_in[10]; const u16* b1=(const u16*)d_in[11]; const u16* w2=(const u16*)d_in[12]; const u16* b2=(const u16*)d_in[13];
  float* ws=(float*)d_ws; float* xn=ws; float* y=xn+L*D; float* ure=y+L*D; float* uim=ure+L*P; float* h1=uim+L*P;
  k_ln<<<L,64,0,stream>>>(x,xn,y);
  k_bu<<<L,256,0,stream>>>(xn,Bre,Bim,ure,uim);
  k_scan<<<P/64,64,0,stream>>>(lre,lim,ldt,ure,uim);
  k_h1<<<L,256,0,stream>>>(y,w1,b1,h1);
  k_out<<<L,256,0,stream>>>(x,xn,ure,uim,h1,Cre,Cim,Dsk,w2,b2,(float*)d_out);
}
```

```cpp
#include <hip/hip_runtime.h>
#include <hip/hip_bf16.h>
typedef unsigned short u16;
constexpr int L=2048, D=640, P=640, F=1536;
__device__ __forceinline__ float bf2f(u16 v){ return __uint_as_float(((unsigned)v)<<16); }
__device__ __forceinline__ u16 f2bf(float f){ unsigned u=__float_as_uint(f); u += 0x7fff + ((u>>16)&1); return (u16)(u>>16); }
__device__ __forceinline__ float rb(float f){ return bf2f(f2bf(f)); }

__global__ void k_ln(const u16* x, float* xn, float* y){
  int t=blockIdx.x; int lane=threadIdx.x;
  float v[10]; float s=0;
  for(int i=0;i<10;i++){ v[i]=bf2f(x[t*D+lane+64*i]); s+=v[i]; }
  for(int o=32;o>=1;o>>=1) s+=__shfl_xor(s,o);
  float m=s/D; float q=0;
  for(int i=0;i<10;i++){ float d=v[i]-m; q+=d*d; }
  for(int o=32;o>=1;o>>=1) q+=__shfl_xor(q,o);
  float var=rb(q/D); m=rb(m);
  float den=rb(sqrtf(rb(var+1e-5f)));
  s=0;
  for(int i=0;i<10;i++){ v[i]=rb(rb(v[i]-m)/den); xn[t*D+lane+64*i]=v[i]; s+=v[i]; }
  for(int o=32;o>=1;o>>=1) s+=__shfl_xor(s,o);
  m=s/D; q=0;
  for(int i=0;i<10;i++){ float d=v[i]-m; q+=d*d; }
  for(int o=32;o>=1;o>>=1) q+=__shfl_xor(q,o);
  var=rb(q/D); m=rb(m);
  den=rb(sqrtf(rb(var+1e-5f)));
  for(int i=0;i<10;i++){ y[t*D+lane+64*i]=rb(rb(v[i]-m)/den); }
}
__global__ void k_bu(const float* xn, const u16* Bre, const u16* Bim, float* ure, float* uim){
  int t=blockIdx.x; __shared__ float xs[D];
  for(int i=threadIdx.x;i<D;i+=blockDim.x) xs[i]=xn[t*D+i];
  __syncthreads();
  for(int p=threadIdx.x;p<P;p+=blockDim.x){ float a=0,b=0;
    for(int d=0;d<D;d++){ a+=xs[d]*bf2f(Bre[p*D+d]); b+=xs[d]*bf2f(Bim[p*D+d]); }
    ure[t*P+p]=a; uim[t*P+p]=b; }
}
__global__ void k_scan(const u16* lre,const u16* lim,const u16* ldt,float* ure,float* uim){
  int p=blockIdx.x*64+threadIdx.x;
  float dt=rb(expf(bf2f(ldt[p]))); float a=bf2f(lre[p]), b=bf2f(lim[p]);
  float e=expf(dt*a); float lr=e*cosf(dt*b), li=e*sinf(dt*b);
  float nr=lr-1.f, ni=li; float den=a*a+b*b; float cr=(nr*a+ni*b)/den, ci=(ni*a-nr*b)/den;
  float hr=0,hi=0;
  for(int t=0;t<L;t++){ float ur=ure[t*P+p], ui=uim[t*P+p]; float br=cr*ur-ci*ui, bi=cr*ui+ci*ur;
    float nhr=lr*hr-li*hi+br, nhi=lr*hi+li*hr+bi; hr=nhr; hi=nhi; ure[t*P+p]=hr; uim[t*P+p]=hi; }
}
__global__ void k_h1(const float* y,const u16* w1,const u16* b1,float* h1){
  int t=blockIdx.x; __shared__ float xs[D];
  for(int i=threadIdx.x;i<D;i+=blockDim.x) xs[i]=y[t*D+i];
  __syncthreads();
  for(int f=threadIdx.x;f<F;f+=blockDim.x){ float a=0;
    for(int d=0;d<D;d++) a+=xs[d]*bf2f(w1[d*F+f]);
    a=rb(rb(a)+bf2f(b1[f]));
    float g=0.5f*a*(1.f+tanhf(0.7978845608f*(a+0.044715f*a*a*a)));
    h1[t*F+f]=rb(g); }
}
__global__ void k_out(const u16* x,const float* xn,const float* hre,const float* him,const float* h1,const u16* Cre,const u16* Cim,const u16* Dsk,const u16* w2,const u16* b2,float* out){
  int t=blockIdx.x; __shared__ float hr[P],hi[P],hh[F];
  for(int i=threadIdx.x;i<P;i+=blockDim.x){ hr[i]=hre[t*P+i]; hi[i]=him[t*P+i]; }
  for(int i=threadIdx.x;i<F;i+=blockDim.x) hh[i]=h1[t*F+i];
  __syncthreads();
  for(int d=threadIdx.x;d<D;d+=blockDim.x){ float a=0,b=0;
    for(int p=0;p<P;p++) a+=hr[p]*bf2f(Cre[d*P+p])-hi[p]*bf2f(Cim[d*P+p]);
    for(int f=0;f<F;f++) b+=hh[f]*bf2f(w2[f*D+d]);
    float A=2.f*a+rb(xn[t*D+d]*bf2f(Dsk[d]));
    float B=rb(rb(b)+bf2f(b2[d]));
    out[t*D+d]=bf2f(x[t*D+d])+A+B; }
}

typedef unsigned v4u __attribute__((ext_vector_type(4)));
__global__ __launch_bounds__(256) void k_probe(const char* p, int iters, unsigned* sink){
  v4u acc={0,0,0,0};
  const char* b=p+(size_t)blockIdx.x*iters*32768+threadIdx.x*16;
  for(int i=0;i<iters;i++){
    v4u v[8];
    #pragma unroll
    for(int d=0;d<8;d++) v[d]=*(const v4u*)(b+i*32768+d*4096);
    #pragma unroll
    for(int d=0;d<8;d++) acc^=v[d];
  }
  if(acc.x==0x12345 && acc.y==0x54321) sink[0]=acc.z^acc.w;
}
extern "C" void kernel_launch(void* const* d_in, const int* in_sizes, int n_in, void* d_out, int out_size, void* d_ws, size_t ws_size, hipStream_t stream){
  const u16* x=(const u16*)d_in[0]; const u16* lre=(const u16*)d_in[1]; const u16* lim=(const u16*)d_in[2];
  const u16* Bre=(const u16*)d_in[3]; const u16* Bim=(const u16*)d_in[4]; const u16* Cre=(const u16*)d_in[5]; const u16* Cim=(const u16*)d_in[6];
  const u16* ldt=(const u16*)d_in[7]; const u16* Dsk=(const u16*)d_in[8];
  const u16* w1=(const u16*)d_in[10]; const u16* b1=(const u16*)d_in[11]; const u16* w2=(const u16*)d_in[12]; const u16* b2=(const u16*)d_in[13];
  float* ws=(float*)d_ws; float* xn=ws; float* y=xn+L*D; float* ure=y+L*D; float* uim=ure+L*P; float* h1=uim+L*P;
  k_probe<<<1024,256,0,stream>>>((const char*)d_ws+(60u<<20),1,(unsigned*)((char*)d_ws+(59u<<20)));
  k_ln<<<L,64,0,stream>>>(x,xn,y);
  k_bu<<<L,256,0,stream>>>(xn,Bre,Bim,ure,uim);
  k_scan<<<P/64,64,0,stream>>>(lre,lim,ldt,ure,uim);
  k_h1<<<L,256,0,stream>>>(y,w1,b1,h1);
  k_out<<<L,256,0,stream>>>(x,xn,ure,uim,h1,Cre,Cim,Dsk,w2,b2,(float*)d_out);
}
```

```cpp
#include <hip/hip_runtime.h>
#include <hip/hip_bf16.h>

typedef unsigned short u16;
typedef float    f2 __attribute__((ext_vector_type(2)));
typedef float    f4 __attribute__((ext_vector_type(4)));
typedef unsigned u2 __attribute__((ext_vector_type(2)));
typedef unsigned u4 __attribute__((ext_vector_type(4)));
typedef __bf16   bf2 __attribute__((ext_vector_type(2)));
typedef __amdgpu_buffer_rsrc_t rsrc_t;
#define LAS __attribute__((address_space(3)))
#define LDS(T, off) (*(LAS T*)(smem + (off)))
#define DI __device__ __forceinline__
constexpr int SC1 = 16;
constexpr unsigned OOB = 0x7ffffff0u;
constexpr int SPIN_CAP = 50000;

DI rsrc_t mk(const void* p, unsigned n) { return __builtin_amdgcn_make_buffer_rsrc((void*)p, (short)0, (int)n, 0x00020000); }
DI f2 unpk(unsigned v) { return f2{__uint_as_float(v << 16), __uint_as_float(v & 0xffff0000u)}; }
DI f4 unpk4(u2 v) { const f2 a = unpk(v.x), b = unpk(v.y); return f4{a.x, a.y, b.x, b.y}; }
DI unsigned cvtpk(float lo, float hi) { return __builtin_bit_cast(unsigned, __builtin_convertvector(f2{lo, hi}, bf2)); }
DI unsigned ard(unsigned a) { unsigned v; asm volatile("v_accvgpr_read_b32 %0, %1" : "=v"(v) : "a"(a)); return v; }
DI float rbf(float v) { return __uint_as_float(cvtpk(v, v) << 16); }
DI float ln_rstd(float var) { return __builtin_amdgcn_rcpf(rbf(__builtin_amdgcn_sqrtf(rbf(rbf(var) + 1e-5f)))); }
DI u4 cat(u2 a, u2 b) { return __builtin_shufflevector(a, b, 0, 1, 2, 3); }
template <int C> DI float dpp(float v) { return __builtin_bit_cast(float, __builtin_amdgcn_update_dpp(0, __builtin_bit_cast(int, v), C, 0xf, 0xf, true)); }
template <int I> DI float qb(float v) { return dpp<I*0x55>(v); }
DI float shfl(int a, float v) { return __builtin_bit_cast(float, __builtin_amdgcn_ds_bpermute(a, __builtin_bit_cast(int, v))); }
DI float red32(float v) {
  v += dpp<0xB1>(v); v += dpp<0x4E>(v); v += dpp<0x141>(v); v += dpp<0x140>(v);
  const auto s = __builtin_amdgcn_permlane16_swap(__float_as_uint(v), __float_as_uint(v), false, false);
  return __uint_as_float(s[0]) + __uint_as_float(s[1]);
}
DI f2 csq(f2 a) { return f2{a.x*a.x - a.y*a.y, 2.f*a.x*a.y}; }
DI f2 cmul(f2 l, f2 h) { return f2{l.x*h.x - l.y*h.y, l.x*h.y + l.y*h.x}; }
DI f2 cfma(f2 l, f2 h, f2 u) { return f2{l.x*h.x - l.y*h.y + u.x, l.x*h.y + l.y*h.x + u.y}; }
DI void lam_bar(unsigned re, unsigned im, unsigned ld, float& a, float& b, float& lr, float& li) {
  a = __uint_as_float(re << 16); b = __uint_as_float(im << 16);
  const float dt = __uint_as_float(cvtpk(__expf(__uint_as_float(ld << 16)), 0.f) << 16);
  const float e = __expf(dt*a); float th = dt*b*0.15915494309189535f; th -= __builtin_rintf(th);
  lr = e*__builtin_amdgcn_cosf(th); li = e*__builtin_amdgcn_sinf(th);
}
DI f4 gelu_z(f4 a) { return a*(a*a*-0.10294324f + -2.3022082f); }
DI f4 exp4(f4 z) { return f4{__builtin_amdgcn_exp2f(z.x), __builtin_amdgcn_exp2f(z.y), __builtin_amdgcn_exp2f(z.z), __builtin_amdgcn_exp2f(z.w)}; }
DI f4 rcp4(f4 d) { return f4{__builtin_amdgcn_rcpf(d.x), __builtin_amdgcn_rcpf(d.y), __builtin_amdgcn_rcpf(d.z), __builtin_amdgcn_rcpf(d.w)}; }
template <int AUX> DI void st128_(u4 v, rsrc_t r, unsigned vo, unsigned so) {
  if constexpr (AUX == SC1) asm volatile("buffer_store_dwordx4 %0, %1, %2, %3 offen sc1\n\ts_nop 1" :: "v"(v), "v"(vo), "s"(r), "s"(so) : "memory");
  else asm volatile("buffer_store_dwordx4 %0, %1, %2, %3 offen\n\ts_nop 1" :: "v"(v), "v"(vo), "s"(r), "s"(so) : "memory");
}
#define st128(v, r, vo, so, aux) st128_<aux>((v), r, vo, so)
DI void spin1(rsrc_t r, unsigned off, bool skip, unsigned tag) {
  _Pragma("nounroll") for (int spin = 0; spin < SPIN_CAP; ++spin) { unsigned v;
    asm volatile("buffer_load_dword %0, %1, %2, 0 offen sc1\n\ts_waitcnt vmcnt(0)" : "=&v"(v) : "v"(off), "s"(r) : "memory");
    if (__all(skip || v == tag)) break;
    __builtin_amdgcn_s_sleep(2); }
}

__global__ __launch_bounds__(256, 1)
void s5_mixer_fused(const u16* __restrict__ x, char* __restrict__ ws, const u16* __restrict__ Bre, const u16* __restrict__ Bim,
                    const u16* __restrict__ w1, const u16* __restrict__ w2, const u16* __restrict__ b1, const u16* __restrict__ Dsk,
                    const u16* __restrict__ b2, const u16* __restrict__ lre, const u16* __restrict__ lim, const u16* __restrict__ ldt,
                    const u16* __restrict__ Cre, const u16* __restrict__ Cim, float* __restrict__ out) {
  __shared__ __attribute__((aligned(1024))) char smem_[163840];
  LAS char* const smem = (LAS char*)smem_;
  const unsigned lds0 = (unsigned)(size_t)smem;
  const unsigned tid = threadIdx.x, lane = tid & 63, r16 = lane & 15, g = lane >> 4;
  const unsigned b = blockIdx.x, xc = b & 7, idx = b >> 3;

  unsigned pfd;
  { unsigned long long pc; unsigned n0, n1;
    asm volatile(".Lfk_start:\n\ts_getpc_b64 %0\n\ts_mov_b32 %1, .Lfk_end0-.Lfk_start\n\ts_mov_b32 %2, .Lfk_end1-.Lfk_start" : "=s"(pc), "=s"(n0), "=s"(n1));
    const unsigned n = (n0 > n1 ? n0 : n1) - 8, o = (idx*256 + tid)*8;
    asm volatile("global_load_dword %0, %1, %2" : "=v"(pfd) : "v"(o < n ? o : n), "s"(pc) : "memory"); }
  asm volatile("" :: "s"(x), "s"(ws), "s"(Bre), "s"(Bim), "s"(w1), "s"(w2), "s"(b1), "s"(Dsk), "s"(b2), "s"(lre), "s"(lim), "s"(ldt), "s"(Cre), "s"(Cim), "s"(out));
  const unsigned w = __builtin_amdgcn_readfirstlane(tid >> 6), wm = w >> 1, wn = w & 1;
  const unsigned c = 8*(xc & 1) + (idx & 7), j = 2*(xc >> 1) + ((idx >> 3) & 1);
  const bool ssm = idx < 16;
  const unsigned kst = (unsigned)(0x109876543210ull >> (4*(5*((idx ^ (idx >> 3)) & 1) + ((0x4310u >> (2*(xc & 6))) & 15) + 2*(xc & 1)))) & 15;
  const unsigned kq = xc >> 1, cq = idx >> 3;
  const rsrc_t rX = mk(x, 2048*640*2), rS = mk(ws, 23592960);
  unsigned ep; asm volatile("s_load_dword %0, %1, 0x0" : "=s"(ep) : "s"(ws) : "memory");
  const unsigned l3 = lane >> 3, sl = lane & 7, xr = 8*(w & 1) + l3;
  unsigned xv[4];
  xv[0] = (128*c + (ssm ? 16*(xr >> 2) + 4*(w >> 1) + (xr & 3) : 8*w + l3))*1280 + ((sl ^ (xr >> 1)) << 4);
  xv[1] = xv[0] + (ssm ? 10240 : 40960); xv[2] = xv[0] + 81920; xv[3] = xv[1] + 81920;
  unsigned kx = kst*128;
  const unsigned lw = lds0 + w*1024;
  asm volatile("s_add_u32 m0, %0, 12288" :: "s"(lw) : "scc", "memory");
  asm volatile("s_nop 0");
  asm volatile("buffer_load_dwordx4 %0, %1, %2 offen lds" :: "v"(xv[0]), "s"(rX), "s"(kx) : "memory");
  asm volatile("s_addk_i32 m0, 0x1000" ::: "scc", "memory");
  asm volatile("s_nop 0");
  asm volatile("buffer_load_dwordx4 %0, %1, %2 offen lds" :: "v"(xv[1]), "s"(rX), "s"(kx) : "memory");
  asm volatile("s_addk_i32 m0, 0x1000" ::: "scc", "memory");
  asm volatile("s_nop 0");
  asm volatile("buffer_load_dwordx4 %0, %1, %2 offen lds" :: "v"(xv[2]), "s"(rX), "s"(kx) : "memory");
  asm volatile("s_addk_i32 m0, 0x1000" ::: "scc", "memory");
  asm volatile("s_nop 0");
  asm volatile("buffer_load_dwordx4 %0, %1, %2 offen lds" :: "v"(xv[3]), "s"(rX), "s"(kx) : "memory");
  asm volatile("s_add_u32 %0, %0, 128\n\ts_cmp_eq_u32 %0, 1280\n\ts_cselect_b32 %0, 0, %0" : "+s"(kx) :: "scc");
  __builtin_amdgcn_sched_barrier(0);
  unsigned wv[6];
  if (ssm) { wv[0] = (80*j + 8*(w >> 1) + l3)*1280 + ((sl ^ ((l3 & 6) | (w & 1))) << 4);
    wv[1] = wv[0] + 20480; wv[2] = wv[0] + 40960; wv[3] = wv[0] + 61440; wv[4] = wv[0] + 81920; wv[5] = 0;
  } else {
    _Pragma("unroll") for (int i = 0; i < 3; ++i) { const unsigned pos = tid + 256*i, k = (pos*43691u) >> 20, s = pos - 24*k;
      wv[i] = k*3072 + 384*j + ((s ^ ((k & 2) | ((k >> 1) & 4))) << 4); wv[i + 3] = wv[i] + 32*3072; }
  }
  const rsrc_t rW = mk(ssm ? ((w & 1) ? Bim : Bre) : w1, ssm ? 640*640*2 : 640*1536*2);
  const unsigned kws = ssm ? 128 : 196608, kwe = 10*kws; unsigned kw = kst*kws;
  asm volatile("s_add_u32 m0, %0, 61440" :: "s"(lw) : "scc", "memory");
  asm volatile("s_nop 0");
  asm volatile("buffer_load_dwordx4 %0, %1, %2 offen lds" :: "v"(wv[0]), "s"(rW), "s"(kw) : "memory");
  asm volatile("s_addk_i32 m0, 0x1000" ::: "scc", "memory");
  asm volatile("s_nop 0");
  asm volatile("buffer_load_dwordx4 %0, %1, %2 offen lds" :: "v"(wv[1]), "s"(rW), "s"(kw) : "memory");
  asm volatile("s_addk_i32 m0, 0x1000" ::: "scc", "memory");
  asm volatile("s_nop 0");
  asm volatile("buffer_load_dwordx4 %0, %1, %2 offen lds" :: "v"(wv[2]), "s"(rW), "s"(kw) : "memory");
  asm volatile("s_addk_i32 m0, 0x1000" ::: "scc", "memory");
  asm volatile("s_nop 0");
  asm volatile("buffer_load_dwordx4 %0, %1, %2 offen lds" :: "v"(wv[3]), "s"(rW), "s"(kw) : "memory");
  asm volatile("s_addk_i32 m0, 0x1000" ::: "scc", "memory");
  asm volatile("s_nop 0");
  asm volatile("buffer_load_dwordx4 %0, %1, %2 offen lds" :: "v"(wv[4]), "s"(rW), "s"(kw) : "memory");
  asm volatile("s_addk_i32 m0, 0x1000" ::: "scc", "memory");
  asm volatile("s_nop 0");
  if (!ssm) asm volatile("buffer_load_dwordx4 %0, %1, %2 offen lds" :: "v"(wv[5]), "s"(rW), "s"(kw) : "memory");
  asm volatile("s_add_u32 %0, %0, %1\n\ts_cmp_eq_u32 %0, %2\n\ts_cselect_b32 %0, 0, %0" : "+s"(kw) : "s"(kws), "s"(kwe) : "scc");
  __builtin_amdgcn_sched_barrier(0);
  u4 sx[3]; unsigned q_re, q_im, q_dt;
  const unsigned u1 = b < 224 ? b + 256 : b - 224, k0 = b/10, k1 = u1/10, d0 = 4*(b - 10*k0), d1 = 4*(u1 - 10*k1);
  const unsigned uc = b < 200 ? b : b - 200, cpb = uc/20, cdt = 2*(uc - 20*cpb);
  { const unsigned o = (8*w + l3)*1280 + sl*16; const rsrc_t rW2 = mk(w2, 1536*640*2), rC = mk((w & 1) ? Cim : Cre, 640*640*2);
    asm volatile("s_add_u32 m0, %0, 135168" :: "s"(lw) : "scc", "memory");
  asm volatile("s_nop 0");
    asm volatile("buffer_load_dwordx4 %0, %1, %2 offen lds" :: "v"(o), "s"(rW2), "s"(k0*40960 + d0*32) : "memory");
  asm volatile("s_addk_i32 m0, 0x1000" ::: "scc", "memory");
  asm volatile("s_nop 0");
    asm volatile("buffer_load_dwordx4 %0, %1, %2 offen lds" :: "v"(o), "s"(rW2), "s"(k1*40960 + d1*32) : "memory");
    const unsigned oc = (16*(w >> 1) + l3)*1280 + sl*16;
    asm volatile("s_add_u32 m0, %0, 143360" :: "s"(lw + (w >> 1)*2048) : "scc", "memory");
  asm volatile("s_nop 0");
    asm volatile("buffer_load_dwordx4 %0, %1, %2 offen lds" :: "v"(oc), "s"(rC), "s"(cdt*20480 + cpb*128) : "memory");
  asm volatile("s_addk_i32 m0, 0x800" ::: "scc", "memory");
  asm volatile("s_nop 0");
    asm volatile("buffer_load_dwordx4 %0, %1, %2 offen lds" :: "v"(oc), "s"(rC), "s"(cdt*20480 + cpb*128 + 10240) : "memory"); }
  { const unsigned o = (64*cpb + 16*w + 4*g + (lane & 3))*2;
    asm volatile("global_load_ushort %0, %1, %2" : "=a"(q_re) : "v"(o), "s"(lre) : "memory");
    asm volatile("global_load_ushort %0, %1, %2" : "=a"(q_im) : "v"(o), "s"(lim) : "memory");
    asm volatile("global_load_ushort %0, %1, %2" : "=a"(q_dt) : "v"(o), "s"(ldt) : "memory");
  }
  { const rsrc_t rV = mk(w == 0 ? b1 + 192*j : (w == 1 ? Dsk : b2) + 160*cq, w == 0 ? 384 : 320);
    asm volatile("s_add_u32 m0, %0, 9728" :: "s"(lds0 + (w < 2 ? w : 2)*384) : "scc", "memory");
    asm volatile("s_mov_b64 exec, 0xffffff\n\tbuffer_load_dwordx4 %0, %1, 0 offen lds\n\ts_mov_b64 exec, -1" :: "v"(lane*16), "s"(rV) : "memory"); }
  unsigned wtd;
  { const unsigned o = lane < 3 ? 3964928 + ((((idx << 3) | (xc & 1)) + 2*((kq + 1 + lane) & 3))*4 + w)*128 : lane == 3 ? 3932160 + b*64 : lane == 4 ? 4096 + (c*16 + (ssm ? 0 : 8) + j)*128 :
      (lane < 8 && ssm) ? 65536 + (j*4 + w)*6144 + (15 - c)*384 + (lane - 5)*128 : lane == 8 ? 40960 + (128*(8*(xc & 1) + (xc & 6) + (idx >> 4)) + 8*(idx & 15))*8 : (lane >= 16 && lane < 28 && (idx & 6) == 0) ? 4096000 + ((ssm ? 0 : 8) + j)*6144 + (12*w + lane - 16)*128 : OOB;
    asm volatile("buffer_load_dword %0, %1, %2, 0 offen sc1" : "=a"(wtd) : "v"(o), "s"(rS) : "memory"); }
  const unsigned srow = 128*(8*(xc & 1) + (xc & 6) + (idx >> 4)) + 8*(idx & 15) + 2*w;
  { const unsigned o = (srow + (lane >> 5))*1280 + (lane & 31)*16;
    asm volatile("buffer_load_dwordx4 %0, %1, %2, 0 offen" : "=a"(sx[0]) : "v"(o), "s"(rX) : "memory");
    asm volatile("buffer_load_dwordx4 %0, %1, %2, 0 offen offset:512" : "=a"(sx[1]) : "v"(o), "s"(rX) : "memory");
    asm volatile("buffer_load_dwordx4 %0, %1, %2, 0 offen offset:1024" : "=a"(sx[2]) : "v"((lane & 16) ? OOB : o), "s"(rX) : "memory"); }
  __builtin_amdgcn_sched_barrier(0);
  asm volatile("s_waitcnt lgkmcnt(0)" : "+s"(ep) :: "memory");
  const unsigned tag = (ep + 2u) | 1u;
  unsigned tid2 = tid, mlp = idx >> 4; asm volatile("" : "+s"(mlp));
  if (ssm) {
    const unsigned tid = tid2, lane = tid & 63, r16 = lane & 15, g = lane >> 4;
    unsigned t_re, t_im, t_dt;
    { const unsigned o = (80*j + (tid < 79 ? tid : 79))*2;
      asm volatile("global_load_ushort %0, %1, %2" : "=a"(t_re) : "v"(o), "s"(lre) : "memory");
      asm volatile("global_load_ushort %0, %1, %2" : "=a"(t_im) : "v"(o), "s"(lim) : "memory");
      asm volatile("global_load_ushort %0, %1, %2" : "=a"(t_dt) : "v"(o), "s"(ldt) : "memory");
    }
    const unsigned xa0 = lds0 + 12288 + (64*wm + r16)*128 + (((4*wm + g) ^ (r16 >> 1)) << 4), xa1 = xa0 ^ 64;
    const unsigned rho = 8*(r16 & 1) + (r16 >> 1);
    const unsigned wa0 = lds0 + 61440 + (80*wn + rho)*128 + (((4*wm + g) ^ ((rho & 6) | (rho >> 3))) << 4), wa1 = wa0 ^ 64;
    u4 ones = {0x3f803f80u, 0x3f803f80u, 0x3f803f80u, 0x3f803f80u};
    u4 ones3 = ones*(xc & 1);
    asm volatile("" : "+v"(ones), "+v"(ones3));
    const unsigned vl16 = lane*16, vl16h = vl16 + 4096;
    const unsigned kso = 5*j, ksp = 5*(j ^ 1), ksa = 40 + 12*kq;
    unsigned sw, sh;
    const unsigned lw2 = lds0 + wn*5120 - wm*10240, swb = 262144 + cq*10240 + wn*5120;
    u4 X0[4], X1[4], W0[5], W1[5]; f4 acc[4][5], cs[5];
    unsigned fl; u2 st; f4 c4[2];
    asm volatile("s_waitcnt vmcnt(15) lgkmcnt(0)\n\ts_barrier" ::: "memory");
    asm volatile("ds_read_b128 %0, %1 offset:0" : "=v"(X0[0]) : "v"(xa0) : "memory");
    asm volatile("ds_read_b128 %0, %1 offset:2048" : "=v"(X0[1]) : "v"(xa0) : "memory");
    asm volatile("ds_read_b128 %0, %1 offset:4096" : "=v"(X0[2]) : "v"(xa0) : "memory");
    asm volatile("ds_read_b128 %0, %1 offset:6144" : "=v"(X0[3]) : "v"(xa0) : "memory");
    asm volatile("ds_read_b128 %0, %1 offset:0" : "=v"(W0[0]) : "v"(wa0) : "memory");
    asm volatile("ds_read_b128 %0, %1 offset:2048" : "=v"(W0[1]) : "v"(wa0) : "memory");
    asm volatile("ds_read_b128 %0, %1 offset:4096" : "=v"(W0[2]) : "v"(wa0) : "memory");
    asm volatile("ds_read_b128 %0, %1 offset:6144" : "=v"(W0[3]) : "v"(wa0) : "memory");
    asm volatile("ds_read_b128 %0, %1 offset:8192" : "=v"(W0[4]) : "v"(wa0) : "memory");
    asm volatile("s_waitcnt lgkmcnt(0)" ::: "memory");
    asm volatile("v_mfma_f32_16x16x32_bf16 %0, %1, %2, 0" : "=&v"(acc[0][0]) : "v"(X0[0]), "v"(W0[0]));
    asm volatile("ds_read_b128 %0, %1 offset:0" : "=v"(X1[0]) : "v"(xa1) : "memory");
    asm volatile("s_add_u32 m0, %0, 45056" :: "s"(lw) : "scc", "memory");
    asm volatile("v_mfma_f32_16x16x32_bf16 %0, %1, %2, 0" : "=&v"(acc[1][0]) : "v"(X0[1]), "v"(W0[0]));
    asm volatile("ds_read_b128 %0, %1 offset:2048" : "=v"(X1[1]) : "v"(xa1) : "memory");
    asm volatile("buffer_load_dwordx4 %0, %1, %2 offen lds" :: "v"(xv[0]), "s"(rX), "s"(kx) : "memory");
    asm volatile("s_addk_i32 m0, 0x1000" ::: "scc", "memory");
    asm volatile("v_mfma_f32_16x16x32_bf16 %0, %1, %2, 0" : "=&v"(acc[2][0]) : "v"(X0[2]), "v"(W0[0]));
    asm volatile("ds_read_b128 %0, %1 offset:4096" : "=v"(X1[2]) : "v"(xa1) : "memory");
    asm volatile("buffer_load_dwordx4 %0, %1, %2 offen lds" :: "v"(xv[1]), "s"(rX), "s"(kx) : "memory");
    asm volatile("v_mfma_f32_16x16x32_bf16 %0, %1, %2, 0" : "=&v"(acc[3][0]) : "v"(X0[3]), "v"(W0[0]));
    asm volatile("ds_read_b128 %0, %1 offset:6144" : "=v"(X1[3]) : "v"(xa1) : "memory");
    asm volatile("s_addk_i32 m0, 0x1000" ::: "scc", "memory");
    asm volatile("v_mfma_f32_16x16x32_bf16 %0, %1, %2, 0" : "=&v"(cs[0]) : "v"(ones), "v"(W0[0]));
    asm volatile("ds_read_b128 %0, %1 offset:0" : "=v"(W1[0]) : "v"(wa1) : "memory");
    asm volatile("buffer_load_dwordx4 %0, %1, %2 offen lds" :: "v"(xv[2]), "s"(rX), "s"(kx) : "memory");
    asm volatile("v_mfma_f32_16x16x32_bf16 %0, %1, %2, 0" : "=&v"(acc[0][1]) : "v"(X0[0]), "v"(W0[1]));
    asm volatile("ds_read_b128 %0, %1 offset:2048" : "=v"(W1[1]) : "v"(wa1) : "memory");
    asm volatile("s_addk_i32 m0, 0x1000" ::: "scc", "memory");
    asm volatile("v_mfma_f32_16x16x32_bf16 %0, %1, %2, 0" : "=&v"(acc[1][1]) : "v"(X0[1]), "v"(W0[1]));
    asm volatile("ds_read_b128 %0, %1 offset:4096" : "=v"(W1[2]) : "v"(wa1) : "memory");
    asm volatile("buffer_load_dwordx4 %0, %1, %2 offen lds" :: "v"(xv[3]), "s"(rX), "s"(kx) : "memory");
    asm volatile("s_add_u32 %0, %0, 128\n\ts_cmp_eq_u32 %0, 1280\n\ts_cselect_b32 %0, 0, %0" : "+s"(kx) :: "scc");
    asm volatile("s_add_u32 m0, %0, 102400" :: "s"(lw) : "scc", "memory");
    asm volatile("v_mfma_f32_16x16x32_bf16 %0, %1, %2, 0" : "=&v"(acc[2][1]) : "v"(X0[2]), "v"(W0[1]));
    asm volatile("ds_read_b128 %0, %1 offset:6144" : "=v"(W1[3]) : "v"(wa1) : "memory");
    asm volatile("buffer_load_dwordx4 %0, %1, %2 offen lds" :: "v"(wv[0]), "s"(rW), "s"(kw) : "memory");
    asm volatile("v_mfma_f32_16x16x32_bf16 %0, %1, %2, 0" : "=&v"(acc[3][1]) : "v"(X0[3]), "v"(W0[1]));
    asm volatile("ds_read_b128 %0, %1 offset:8192" : "=v"(W1[4]) : "v"(wa1) : "memory");
    asm volatile("s_addk_i32 m0, 0x1000" ::: "scc", "memory");
    asm volatile("v_mfma_f32_16x16x32_bf16 %0, %1, %2, 0" : "=&v"(cs[1]) : "v"(ones), "v"(W0[1]));
    asm volatile("buffer_load_dwordx4 %0, %1, %2 offen lds" :: "v"(wv[1]), "s"(rW), "s"(kw) : "memory");
    asm volatile("v_mfma_f32_16x16x32_bf16 %0, %1, %2, 0" : "=&v"(acc[0][2]) : "v"(X0[0]), "v"(W0[2]));
    asm volatile("s_addk_i32 m0, 0x1000" ::: "scc", "memory");
    asm volatile("v_mfma_f32_16x16x32_bf16 %0, %1, %2, 0" : "=&v"(acc[1][2]) : "v"(X0[1]), "v"(W0[2]));
    asm volatile("buffer_load_dwordx4 %0, %1, %2 offen lds" :: "v"(wv[2]), "s"(rW), "s"(kw) : "memory");
    asm volatile("s_addk_i32 m0, 0x1000" ::: "scc", "memory");
    asm volatile("v_mfma_f32_16x16x32_bf16 %0, %1, %2, 0" : "=&v"(acc[2][2]) : "v"(X0[2]), "v"(W0[2]));
    asm volatile("buffer_load_dwordx4 %0, %1, %2 offen lds" :: "v"(wv[3]), "s"(rW), "s"(kw) : "memory");
    asm volatile("v_mfma_f32_16x16x32_bf16 %0, %1, %2, 0" : "=&v"(acc[3][2]) : "v"(X0[3]), "v"(W0[2]));
    asm volatile("s_addk_i32 m0, 0x1000" ::: "scc", "memory");
    asm volatile("v_mfma_f32_16x16x32_bf16 %0, %1, %2, 0" : "=&v"(cs[2]) : "v"(ones), "v"(W0[2]));
    asm volatile("buffer_load_dwordx4 %0, %1, %2 offen lds" :: "v"(wv[4]), "s"(rW), "s"(kw) : "memory");
    asm volatile("s_add_u32 %0, %0, 128\n\ts_cmp_eq_u32 %0, 1280\n\ts_cselect_b32 %0, 0, %0" : "+s"(kw) :: "scc");
    asm volatile("v_mfma_f32_16x16x32_bf16 %0, %1, %2, 0" : "=&v"(acc[0][3]) : "v"(X0[0]), "v"(W0[3]));
    asm volatile("s_add_u32 m0, %0, 28672" :: "s"(lw) : "scc", "memory");
    asm volatile("v_mfma_f32_16x16x32_bf16 %0, %1, %2, 0" : "=&v"(acc[1][3]) : "v"(X0[1]), "v"(W0[3]));
    asm volatile("buffer_load_dwordx4 %0, %1, %2 offen lds" :: "v"(xv[0]), "s"(rX), "s"(kx) : "memory");
    asm volatile("s_addk_i32 m0, 0x1000" ::: "scc", "memory");
    asm volatile("v_mfma_f32_16x16x32_bf16 %0, %1, %2, 0" : "=&v"(acc[2][3]) : "v"(X0[2]), "v"(W0[3]));
    asm volatile("buffer_load_dwordx4 %0, %1, %2 offen lds" :: "v"(xv[1]), "s"(rX), "s"(kx) : "memory");
    asm volatile("v_mfma_f32_16x16x32_bf16 %0, %1, %2, 0" : "=&v"(acc[3][3]) : "v"(X0[3]), "v"(W0[3]));
    asm volatile("s_addk_i32 m0, 0x1000" ::: "scc", "memory");
    asm volatile("v_mfma_f32_16x16x32_bf16 %0, %1, %2, 0" : "=&v"(cs[3]) : "v"(ones), "v"(W0[3]));
    asm volatile("buffer_load_dwordx4 %0, %1, %2 offen lds" :: "v"(xv[2]), "s"(rX), "s"(kx) : "memory");
    asm volatile("v_mfma_f32_16x16x32_bf16 %0, %1, %2, 0" : "=&v"(acc[0][4]) : "v"(X0[0]), "v"(W0[4]));
    asm volatile("s_addk_i32 m0, 0x1000" ::: "scc", "memory");
    asm volatile("v_mfma_f32_16x16x32_bf16 %0, %1, %2, 0" : "=&v"(acc[1][4]) : "v"(X0[1]), "v"(W0[4]));
    asm volatile("buffer_load_dwordx4 %0, %1, %2 offen lds" :: "v"(xv[3]), "s"(rX), "s"(kx) : "memory");
    asm volatile("s_add_u32 %0, %0, 128\n\ts_cmp_eq_u32 %0, 1280\n\ts_cselect_b32 %0, 0, %0" : "+s"(kx) :: "scc");
    asm volatile("v_mfma_f32_16x16x32_bf16 %0, %1, %2, 0" : "=&v"(acc[2][4]) : "v"(X0[2]), "v"(W0[4]));
    asm volatile("v_mfma_f32_16x16x32_bf16 %0, %1, %2, 0" : "=&v"(acc[3][4]) : "v"(X0[3]), "v"(W0[4]));
    asm volatile("v_mfma_f32_16x16x32_bf16 %0, %1, %2, 0" : "=&v"(cs[4]) : "v"(ones), "v"(W0[4]));
    __builtin_amdgcn_sched_barrier(0);
    asm volatile("s_waitcnt vmcnt(16)\n\ts_barrier" ::: "memory");
    asm volatile("" : "+v"(pfd));
    asm volatile("" :: "a"(wtd));
    { u2 cre[2], cim[2];
      const unsigned oc = lds0 + 143360 + (r16 >> 3)*2048 + (r16 & 7)*128 + 32*w + 8*g;
      asm volatile("ds_read_b64 %0, %1 offset:0" : "=v"(cre[0]) : "v"(oc) : "memory");
      asm volatile("ds_read_b64 %0, %1 offset:1024" : "=v"(cim[0]) : "v"(oc) : "memory");
      asm volatile("ds_read_b64 %0, %1 offset:4096" : "=v"(cre[1]) : "v"(oc) : "memory");
      asm volatile("ds_read_b64 %0, %1 offset:5120" : "=v"(cim[1]) : "v"(oc) : "memory");
      float a, bb, lr, li; lam_bar(ard(q_re), ard(q_im), ard(q_dt), a, bb, lr, li);
      const float nr = lr - 1.f, dn = 2.f*__builtin_amdgcn_rcpf(a*a + bb*bb), cr = (nr*a + li*bb)*dn, ci = (li*a - nr*bb)*dn;
      const float cr4[4] = {qb<0>(cr), qb<1>(cr), qb<2>(cr), qb<3>(cr)}, ci4[4] = {qb<0>(ci), qb<1>(ci), qb<2>(ci), qb<3>(ci)};
      asm volatile("s_waitcnt lgkmcnt(0)" : "+v"(cre[0]), "+v"(cre[1]), "+v"(cim[0]), "+v"(cim[1]) :: "memory");
      { const f2 r0 = unpk(cre[0].x), r1 = unpk(cre[0].y), i0 = unpk(cim[0].x), i1 = unpk(cim[0].y);
        LDS(u4, 151552 + w*2560 + vl16) = u4{cvtpk(cr4[0]*r0.x - ci4[0]*i0.x, -(cr4[0]*i0.x + ci4[0]*r0.x)), cvtpk(cr4[1]*r0.y - ci4[1]*i0.y, -(cr4[1]*i0.y + ci4[1]*r0.y)),
                   cvtpk(cr4[2]*r1.x - ci4[2]*i1.x, -(cr4[2]*i1.x + ci4[2]*r1.x)), cvtpk(cr4[3]*r1.y - ci4[3]*i1.y, -(cr4[3]*i1.y + ci4[3]*r1.y))}; }
      { const f2 r0 = unpk(cre[1].x), r1 = unpk(cre[1].y), i0 = unpk(cim[1].x), i1 = unpk(cim[1].y);
        LDS(u4, 152576 + w*2560 + vl16) = u4{cvtpk(cr4[0]*r0.x - ci4[0]*i0.x, -(cr4[0]*i0.x + ci4[0]*r0.x)), cvtpk(cr4[1]*r0.y - ci4[1]*i0.y, -(cr4[1]*i0.y + ci4[1]*r0.y)),
                   cvtpk(cr4[2]*r1.x - ci4[2]*i1.x, -(cr4[2]*i1.x + ci4[2]*r1.x)), cvtpk(cr4[3]*r1.y - ci4[3]*i1.y, -(cr4[3]*i1.y + ci4[3]*r1.y))}; }
    }
    { f2 v[12]; f2 s2 = {0.f, 0.f};
      _Pragma("unroll") for (int i = 0; i < 12; ++i) { v[i] = unpk(ard(sx[i >> 2][i & 3])); s2 += v[i]; }
      const float mean = red32(s2.x + s2.y)*(1.f/640.f), m3 = (lane & 16) ? 0.f : mean; f2 q2 = {0.f, 0.f};
      _Pragma("unroll") for (int i = 0; i < 12; ++i) { const f2 d = v[i] - (i < 8 ? mean : m3); q2 += d*d; }
      const float var = red32(q2.x + q2.y)*(1.f/640.f);
      LDS(f2, 153600 + w*2560 + lane*8) = f2{mean, var}; }
    __builtin_amdgcn_sched_barrier(0);
    asm volatile("s_waitcnt vmcnt(4) lgkmcnt(0)\n\ts_barrier" ::: "memory");
    asm volatile("v_mfma_f32_16x16x32_bf16 %0, %1, %2, %0" : "+v"(acc[0][0]) : "v"(X1[0]), "v"(W1[0]));
    asm volatile("ds_read_b128 %0, %1 offset:32768" : "=v"(X0[0]) : "v"(xa0) : "memory");
    asm volatile("s_add_u32 m0, %0, 81920" :: "s"(lw) : "scc", "memory");
    asm volatile("v_mfma_f32_16x16x32_bf16 %0, %1, %2, %0" : "+v"(acc[1][0]) : "v"(X1[1]), "v"(W1[0]));
    asm volatile("ds_read_b128 %0, %1 offset:34816" : "=v"(X0[1]) : "v"(xa0) : "memory");
    asm volatile("buffer_load_dwordx4 %0, %1, %2 offen lds" :: "v"(wv[0]), "s"(rW), "s"(kw) : "memory");
    asm volatile("v_mfma_f32_16x16x32_bf16 %0, %1, %2, %0" : "+v"(acc[2][0]) : "v"(X1[2]), "v"(W1[0]));
    asm volatile("ds_read_b128 %0, %1 offset:36864" : "=v"(X0[2]) : "v"(xa0) : "memory");
    asm volatile("v_mfma_f32_16x16x32_bf16 %0, %1, %2, %0" : "+v"(acc[3][0]) : "v"(X1[3]), "v"(W1[0]));
    asm volatile("ds_read_b128 %0, %1 offset:38912" : "=v"(X0[3]) : "v"(xa0) : "memory");
    asm volatile("s_addk_i32 m0, 0x1000" ::: "scc", "memory");
    asm volatile("v_mfma_f32_16x16x32_bf16 %0, %1, %2, %0" : "+v"(acc[0][1]) : "v"(X1[0]), "v"(W1[1]));
    asm volatile("ds_read_b128 %0, %1 offset:40960" : "=v"(W0[0]) : "v"(wa0) : "memory");
    asm volatile("buffer_load_dwordx4 %0, %1, %2 offen lds" :: "v"(wv[1]), "s"(rW), "s"(kw) : "memory");
    asm volatile("v_mfma_f32_16x16x32_bf16 %0, %1, %2, %0" : "+v"(acc[1][1]) : "v"(X1[1]), "v"(W1[1]));
    asm volatile("ds_read_b128 %0, %1 offset:43008" : "=v"(W0[1]) : "v"(wa0) : "memory");
    asm volatile("v_mfma_f32_16x16x32_bf16 %0, %1, %2, %0" : "+v"(acc[2][1]) : "v"(X1[2]), "v"(W1[1]));
    asm volatile("ds_read_b128 %0, %1 offset:45056" : "=v"(W0[2]) : "v"(wa0) : "memory");
    asm volatile("s_addk_i32 m0, 0x1000" ::: "scc", "memory");
    asm volatile("v_mfma_f32_16x16x32_bf16 %0, %1, %2, %0" : "+v"(acc[3][1]) : "v"(X1[3]), "v"(W1[1]));
    asm volatile("ds_read_b128 %0, %1 offset:47104" : "=v"(W0[3]) : "v"(wa0) : "memory");
    asm volatile("buffer_load_dwordx4 %0, %1, %2 offen lds" :: "v"(wv[2]), "s"(rW), "s"(kw) : "memory");
    asm volatile("v_mfma_f32_16x16x32_bf16 %0, %1, %2, %0" : "+v"(acc[0][2]) : "v"(X1[0]), "v"(W1[2]));
    asm volatile("ds_read_b128 %0, %1 offset:49152" : "=v"(W0[4]) : "v"(wa0) : "memory");
    asm volatile("v_mfma_f32_16x16x32_bf16 %0, %1, %2, %0" : "+v"(acc[1][2]) : "v"(X1[1]), "v"(W1[2]));
    asm volatile("s_addk_i32 m0, 0x1000" ::: "scc", "memory");
    asm volatile("v_mfma_f32_16x16x32_bf16 %0, %1, %2, %0" : "+v"(acc[2][2]) : "v"(X1[2]), "v"(W1[2]));
    asm volatile("buffer_load_dwordx4 %0, %1, %2 offen lds" :: "v"(wv[3]), "s"(rW), "s"(kw) : "memory");
    asm volatile("v_mfma_f32_16x16x32_bf16 %0, %1, %2, %0" : "+v"(acc[3][2]) : "v"(X1[3]), "v"(W1[2]));
    asm volatile("v_mfma_f32_16x16x32_bf16 %0, %1, %2, %0" : "+v"(acc[0][3]) : "v"(X1[0]), "v"(W1[3]));
    asm volatile("s_addk_i32 m0, 0x1000" ::: "scc", "memory");
    asm volatile("v_mfma_f32_16x16x32_bf16 %0, %1, %2, %0" : "+v"(acc[1][3]) : "v"(X1[1]), "v"(W1[3]));
    asm volatile("buffer_load_dwordx4 %0, %1, %2 offen lds" :: "v"(wv[4]), "s"(rW), "s"(kw) : "memory");
    asm volatile("s_add_u32 %0, %0, 128\n\ts_cmp_eq_u32 %0, 1280\n\ts_cselect_b32 %0, 0, %0" : "+s"(kw) :: "scc");
    asm volatile("v_mfma_f32_16x16x32_bf16 %0, %1, %2, %0" : "+v"(acc[2][3]) : "v"(X1[2]), "v"(W1[3]));
    asm volatile("v_mfma_f32_16x16x32_bf16 %0, %1, %2, %0" : "+v"(acc[3][3]) : "v"(X1[3]), "v"(W1[3]));
    asm volatile("s_add_u32 m0, %0, 12288" :: "s"(lw) : "scc", "memory");
    asm volatile("v_mfma_f32_16x16x32_bf16 %0, %1, %2, %0" : "+v"(acc[0][4]) : "v"(X1[0]), "v"(W1[4]));
    asm volatile("buffer_load_dwordx4 %0, %1, %2 offen lds" :: "v"(xv[0]), "s"(rX), "s"(kx) : "memory");
    asm volatile("v_mfma_f32_16x16x32_bf16 %0, %1, %2, %0" : "+v"(acc[1][4]) : "v"(X1[1]), "v"(W1[4]));
    asm volatile("v_mfma_f32_16x16x32_bf16 %0, %1, %2, %0" : "+v"(acc[2][4]) : "v"(X1[2]), "v"(W1[4]));
    asm volatile("s_addk_i32 m0, 0x1000" ::: "scc", "memory");
    asm volatile("v_mfma_f32_16x16x32_bf16 %0, %1, %2, %0" : "+v"(acc[3][4]) : "v"(X1[3]), "v"(W1[4]));
    asm volatile("buffer_load_dwordx4 %0, %1, %2 offen lds" :: "v"(xv[1]), "s"(rX), "s"(kx) : "memory");
    asm volatile("s_waitcnt lgkmcnt(0)" ::: "memory");
    asm volatile("v_mfma_f32_16x16x32_bf16 %0, %1, %2, %0" : "+v"(acc[0][0]) : "v"(X0[0]), "v"(W0[0]));
    asm volatile("ds_read_b128 %0, %1 offset:32768" : "=v"(X1[0]) : "v"(xa1) : "memory");
    asm volatile("v_mfma_f32_16x16x32_bf16 %0, %1, %2, %0" : "+v"(acc[1][0]) : "v"(X0[1]), "v"(W0[0]));
    asm volatile("ds_read_b128 %0, %1 offset:34816" : "=v"(X1[1]) : "v"(xa1) : "memory");
    asm volatile("s_addk_i32 m0, 0x1000" ::: "scc", "memory");
    asm volatile("v_mfma_f32_16x16x32_bf16 %0, %1, %2, %0" : "+v"(acc[2][0]) : "v"(X0[2]), "v"(W0[0]));
    asm volatile("ds_read_b128 %0, %1 offset:36864" : "=v"(X1[2]) : "v"(xa1) : "memory");
    asm volatile("buffer_load_dwordx4 %0, %1, %2 offen lds" :: "v"(xv[2]), "s"(rX), "s"(kx) : "memory");
    asm volatile("v_mfma_f32_16x16x32_bf16 %0, %1, %2, %0" : "+v"(acc[3][0]) : "v"(X0[3]), "v"(W0[0]));
    asm volatile("ds_read_b128 %0, %1 offset:38912" : "=v"(X1[3]) : "v"(xa1) : "memory");
    asm volatile("v_mfma_f32_16x16x32_bf16 %0, %1, %2, %0" : "+v"(cs[0]) : "v"(ones), "v"(W0[0]));
    asm volatile("ds_read_b128 %0, %1 offset:40960" : "=v"(W1[0]) : "v"(wa1) : "memory");
    asm volatile("s_addk_i32 m0, 0x1000" ::: "scc", "memory");
    asm volatile("v_mfma_f32_16x16x32_bf16 %0, %1, %2, %0" : "+v"(acc[0][1]) : "v"(X0[0]), "v"(W0[1]));
    asm volatile("ds_read_b128 %0, %1 offset:43008" : "=v"(W1[1]) : "v"(wa1) : "memory");
    asm volatile("buffer_load_dwordx4 %0, %1, %2 offen lds" :: "v"(xv[3]), "s"(rX), "s"(kx) : "memory");
    asm volatile("s_add_u32 %0, %0, 128\n\ts_cmp_eq_u32 %0, 1280\n\ts_cselect_b32 %0, 0, %0" : "+s"(kx) :: "scc");
    asm volatile("v_mfma_f32_16x16x32_bf16 %0, %1, %2, %0" : "+v"(acc[1][1]) : "v"(X0[1]), "v"(W0[1]));
    asm volatile("ds_read_b128 %0, %1 offset:45056" : "=v"(W1[2]) : "v"(wa1) : "memory");
    asm volatile("v_mfma_f32_16x16x32_bf16 %0, %1, %2, %0" : "+v"(acc[2][1]) : "v"(X0[2]), "v"(W0[1]));
    asm volatile("ds_read_b128 %0, %1 offset:47104" : "=v"(W1[3]) : "v"(wa1) : "memory");
    asm volatile("s_add_u32 m0, %0, 61440" :: "s"(lw) : "scc", "memory");
    asm volatile("v_mfma_f32_16x16x32_bf16 %0, %1, %2, %0" : "+v"(acc[3][1]) : "v"(X0[3]), "v"(W0[1]));
    asm volatile("ds_read_b128 %0, %1 offset:49152" : "=v"(W1[4]) : "v"(wa1) : "memory");
    asm volatile("buffer_load_dwordx4 %0, %1, %2 offen lds" :: "v"(wv[0]), "s"(rW), "s"(kw) : "memory");
    asm volatile("v_mfma_f32_16x16x32_bf16 %0, %1, %2, %0" : "+v"(cs[1]) : "v"(ones), "v"(W0[1]));
    asm volatile("v_mfma_f32_16x16x32_bf16 %0, %1, %2, %0" : "+v"(acc[0][2]) : "v"(X0[0]), "v"(W0[2]));
    asm volatile("s_addk_i32 m0, 0x1000" ::: "scc", "memory");
    asm volatile("v_mfma_f32_16x16x32_bf16 %0, %1, %2, %0" : "+v"(acc[1][2]) : "v"(X0[1]), "v"(W0[2]));
    asm volatile("buffer_load_dwordx4 %0, %1, %2 offen lds" :: "v"(wv[1]), "s"(rW), "s"(kw) : "memory");
    asm volatile("v_mfma_f32_16x16x32_bf16 %0, %1, %2, %0" : "+v"(acc[2][2]) : "v"(X0[2]), "v"(W0[2]));
    asm volatile("v_mfma_f32_16x16x32_bf16 %0, %1, %2, %0" : "+v"(acc[3][2]) : "v"(X0[3]), "v"(W0[2]));
    asm volatile("s_addk_i32 m0, 0x1000" ::: "scc", "memory");
    asm volatile("v_mfma_f32_16x16x32_bf16 %0, %1, %2, %0" : "+v"(cs[2]) : "v"(ones), "v"(W0[2]));
    asm volatile("buffer_load_dwordx4 %0, %1, %2 offen lds" :: "v"(wv[2]), "s"(rW), "s"(kw) : "memory");
    asm volatile("v_mfma_f32_16x16x32_bf16 %0, %1, %2, %0" : "+v"(acc[0][3]) : "v"(X0[0]), "v"(W0[3]));
    asm volatile("v_mfma_f32_16x16x32_bf16 %0, %1, %2, %0" : "+v"(acc[1][3]) : "v"(X0[1]), "v"(W0[3]));
    asm volatile("s_addk_i32 m0, 0x1000" ::: "scc", "memory");
    asm volatile("v_mfma_f32_16x16x32_bf16 %0, %1, %2, %0" : "+v"(acc[2][3]) : "v"(X0[2]), "v"(W0[3]));
    asm volatile("buffer_load_dwordx4 %0, %1, %2 offen lds" :: "v"(wv[3]), "s"(rW), "s"(kw) : "memory");
    asm volatile("v_mfma_f32_16x16x32_bf16 %0, %1, %2, %0" : "+v"(acc[3][3]) : "v"(X0[3]), "v"(W0[3]));
    asm volatile("v_mfma_f32_16x16x32_bf16 %0, %1, %2, %0" : "+v"(cs[3]) : "v"(ones), "v"(W0[3]));
    asm volatile("s_addk_i32 m0, 0x1000" ::: "scc", "memory");
    asm volatile("v_mfma_f32_16x16x32_bf16 %0, %1, %2, %0" : "+v"(acc[0][4]) : "v"(X0[0]), "v"(W0[4]));
    asm volatile("buffer_load_dwordx4 %0, %1, %2 offen lds" :: "v"(wv[4]), "s"(rW), "s"(kw) : "memory");
    asm volatile("s_add_u32 %0, %0, 128\n\ts_cmp_eq_u32 %0, 1280\n\ts_cselect_b32 %0, 0, %0" : "+s"(kw) :: "scc");
    asm volatile("v_mfma_f32_16x16x32_bf16 %0, %1, %2, %0" : "+v"(acc[1][4]) : "v"(X0[1]), "v"(W0[4]));
    asm volatile("v_mfma_f32_16x16x32_bf16 %0, %1, %2, %0" : "+v"(acc[2][4]) : "v"(X0[2]), "v"(W0[4]));
    asm volatile("v_mfma_f32_16x16x32_bf16 %0, %1, %2, %0" : "+v"(acc[3][4]) : "v"(X0[3]), "v"(W0[4]));
    asm volatile("v_mfma_f32_16x16x32_bf16 %0, %1, %2, %0" : "+v"(cs[4]) : "v"(ones), "v"(W0[4]));
    asm volatile("s_waitcnt vmcnt(9) lgkmcnt(0)\n\ts_barrier" ::: "memory");
    asm volatile("v_mfma_f32_16x16x32_bf16 %0, %1, %2, %0" : "+v"(acc[0][0]) : "v"(X1[0]), "v"(W1[0]));
    asm volatile("ds_read_b128 %0, %1 offset:16384" : "=v"(X0[0]) : "v"(xa0) : "memory");
    asm volatile("s_add_u32 m0, %0, 45056" :: "s"(lw) : "scc", "memory");
    asm volatile("v_mfma_f32_16x16x32_bf16 %0, %1, %2, %0" : "+v"(acc[1][0]) : "v"(X1[1]), "v"(W1[0]));
    asm volatile("ds_read_b128 %0, %1 offset:18432" : "=v"(X0[1]) : "v"(xa0) : "memory");
    asm volatile("buffer_load_dwordx4 %0, %1, %2 offen lds" :: "v"(xv[0]), "s"(rX), "s"(kx) : "memory");
    asm volatile("v_mfma_f32_16x16x32_bf16 %0, %1, %2, %0" : "+v"(acc[2][0]) : "v"(X1[2]), "v"(W1[0]));
    asm volatile("ds_read_b128 %0, %1 offset:20480" : "=v"(X0[2]) : "v"(xa0) : "memory");
    asm volatile("v_mfma_f32_16x16x32_bf16 %0, %1, %2, %0" : "+v"(acc[3][0]) : "v"(X1[3]), "v"(W1[0]));
    asm volatile("ds_read_b128 %0, %1 offset:22528" : "=v"(X0[3]) : "v"(xa0) : "memory");
    asm volatile("v_mfma_f32_16x16x32_bf16 %0, %1, %2, %0" : "+v"(acc[0][1]) : "v"(X1[0]), "v"(W1[1]));
    asm volatile("ds_read_b128 %0, %1 offset:20480" : "=v"(W0[0]) : "v"(wa0) : "memory");
    asm volatile("s_addk_i32 m0, 0x1000" ::: "scc", "memory");
    asm volatile("v_mfma_f32_16x16x32_bf16 %0, %1, %2, %0" : "+v"(acc[1][1]) : "v"(X1[1]), "v"(W1[1]));
    asm volatile("ds_read_b128 %0, %1 offset:22528" : "=v"(W0[1]) : "v"(wa0) : "memory");
    asm volatile("buffer_load_dwordx4 %0, %1, %2 offen lds" :: "v"(xv[1]), "s"(rX), "s"(kx) : "memory");
    asm volatile("v_mfma_f32_16x16x32_bf16 %0, %1, %2, %0" : "+v"(acc[2][1]) : "v"(X1[2]), "v"(W1[1]));
    asm volatile("ds_read_b128 %0, %1 offset:24576" : "=v"(W0[2]) : "v"(wa0) : "memory");
    asm volatile("v_mfma_f32_16x16x32_bf16 %0, %1, %2, %0" : "+v"(acc[3][1]) : "v"(X1[3]), "v"(W1[1]));
    asm volatile("ds_read_b128 %0, %1 offset:26624" : "=v"(W0[3]) : "v"(wa0) : "memory");
    asm volatile("v_mfma_f32_16x16x32_bf16 %0, %1, %2, %0" : "+v"(acc[0][2]) : "v"(X1[0]), "v"(W1[2]));
    asm volatile("ds_read_b128 %0, %1 offset:28672" : "=v"(W0[4]) : "v"(wa0) : "memory");
    asm volatile("v_mfma_f32_16x16x32_bf16 %0, %1, %2, %0" : "+v"(acc[1][2]) : "v"(X1[1]), "v"(W1[2]));
    asm volatile("s_addk_i32 m0, 0x1000" ::: "scc", "memory");
    asm volatile("v_mfma_f32_16x16x32_bf16 %0, %1, %2, %0" : "+v"(acc[2][2]) : "v"(X1[2]), "v"(W1[2]));
    asm volatile("buffer_load_dwordx4 %0, %1, %2 offen lds" :: "v"(xv[2]), "s"(rX), "s"(kx) : "memory");
    asm volatile("v_mfma_f32_16x16x32_bf16 %0, %1, %2, %0" : "+v"(acc[3][2]) : "v"(X1[3]), "v"(W1[2]));
    asm volatile("v_mfma_f32_16x16x32_bf16 %0, %1, %2, %0" : "+v"(acc[0][3]) : "v"(X1[0]), "v"(W1[3]));
    asm volatile("v_mfma_f32_16x16x32_bf16 %0, %1, %2, %0" : "+v"(acc[1][3]) : "v"(X1[1]), "v"(W1[3]));
    asm volatile("v_mfma_f32_16x16x32_bf16 %0, %1, %2, %0" : "+v"(acc[2][3]) : "v"(X1[2]), "v"(W1[3]));
    asm volatile("s_addk_i32 m0, 0x1000" ::: "scc", "memory");
    asm volatile("v_mfma_f32_16x16x32_bf16 %0, %1, %2, %0" : "+v"(acc[3][3]) : "v"(X1[3]), "v"(W1[3]));
    asm volatile("buffer_load_dwordx4 %0, %1, %2 offen lds" :: "v"(xv[3]), "s"(rX), "s"(kx) : "memory");
    asm volatile("s_add_u32 %0, %0, 128\n\ts_cmp_eq_u32 %0, 1280\n\ts_cselect_b32 %0, 0, %0" : "+s"(kx) :: "scc");
    asm volatile("v_mfma_f32_16x16x32_bf16 %0, %1, %2, %0" : "+v"(acc[0][4]) : "v"(X1[0]), "v"(W1[4]));
    asm volatile("v_mfma_f32_16x16x32_bf16 %0, %1, %2, %0" : "+v"(acc[1][4]) : "v"(X1[1]), "v"(W1[4]));
    asm volatile("v_mfma_f32_16x16x32_bf16 %0, %1, %2, %0" : "+v"(acc[2][4]) : "v"(X1[2]), "v"(W1[4]));
    asm volatile("s_add_u32 m0, %0, 102400" :: "s"(lw) : "scc", "memory");
    asm volatile("v_mfma_f32_16x16x32_bf16 %0, %1, %2, %0" : "+v"(acc[3][4]) : "v"(X1[3]), "v"(W1[4]));
    asm volatile("buffer_load_dwordx4 %0, %1, %2 offen lds" :: "v"(wv[0]), "s"(rW), "s"(kw) : "memory");
    asm volatile("s_waitcnt lgkmcnt(0)" ::: "memory");
    asm volatile("v_mfma_f32_16x16x32_bf16 %0, %1, %2, %0" : "+v"(acc[0][0]) : "v"(X0[0]), "v"(W0[0]));
    asm volatile("ds_read_b128 %0, %1 offset:16384" : "=v"(X1[0]) : "v"(xa1) : "memory");
    asm volatile("v_mfma_f32_16x16x32_bf16 %0, %1, %2, %0" : "+v"(acc[1][0]) : "v"(X0[1]), "v"(W0[0]));
    asm volatile("ds_read_b128 %0, %1 offset:18432" : "=v"(X1[1]) : "v"(xa1) : "memory");
    asm volatile("v_mfma_f32_16x16x32_bf16 %0, %1, %2, %0" : "+v"(acc[2][0]) : "v"(X0[2]), "v"(W0[0]));
    asm volatile("ds_read_b128 %0, %1 offset:20480" : "=v"(X1[2]) : "v"(xa1) : "memory");
    asm volatile("v_mfma_f32_16x16x32_bf16 %0, %1, %2, %0" : "+v"(acc[3][0]) : "v"(X0[3]), "v"(W0[0]));
    asm volatile("ds_read_b128 %0, %1 offset:22528" : "=v"(X1[3]) : "v"(xa1) : "memory");
    asm volatile("s_addk_i32 m0, 0x1000" ::: "scc", "memory");
    asm volatile("v_mfma_f32_16x16x32_bf16 %0, %1, %2, %0" : "+v"(cs[0]) : "v"(ones3), "v"(W0[0]));
    asm volatile("ds_read_b128 %0, %1 offset:20480" : "=v"(W1[0]) : "v"(wa1) : "memory");
    asm volatile("buffer_load_dwordx4 %0, %1, %2 offen lds" :: "v"(wv[1]), "s"(rW), "s"(kw) : "memory");
    asm volatile("v_mfma_f32_16x16x32_bf16 %0, %1, %2, %0" : "+v"(acc[0][1]) : "v"(X0[0]), "v"(W0[1]));
    asm volatile("ds_read_b128 %0, %1 offset:22528" : "=v"(W1[1]) : "v"(wa1) : "memory");
    asm volatile("v_mfma_f32_16x16x32_bf16 %0, %1, %2, %0" : "+v"(acc[1][1]) : "v"(X0[1]), "v"(W0[1]));
    asm volatile("ds_read_b128 %0, %1 offset:24576" : "=v"(W1[2]) : "v"(wa1) : "memory");
    asm volatile("v_mfma_f32_16x16x32_bf16 %0, %1, %2, %0" : "+v"(acc[2][1]) : "v"(X0[2]), "v"(W0[1]));
    asm volatile("ds_read_b128 %0, %1 offset:26624" : "=v"(W1[3]) : "v"(wa1) : "memory");
    asm volatile("v_mfma_f32_16x16x32_bf16 %0, %1, %2, %0" : "+v"(acc[3][1]) : "v"(X0[3]), "v"(W0[1]));
    asm volatile("ds_read_b128 %0, %1 offset:28672" : "=v"(W1[4]) : "v"(wa1) : "memory");
    asm volatile("s_addk_i32 m0, 0x1000" ::: "scc", "memory");
    asm volatile("v_mfma_f32_16x16x32_bf16 %0, %1, %2, %0" : "+v"(cs[1]) : "v"(ones3), "v"(W0[1]));
    asm volatile("buffer_load_dwordx4 %0, %1, %2 offen lds" :: "v"(wv[2]), "s"(rW), "s"(kw) : "memory");
    asm volatile("v_mfma_f32_16x16x32_bf16 %0, %1, %2, %0" : "+v"(acc[0][2]) : "v"(X0[0]), "v"(W0[2]));
    asm volatile("v_mfma_f32_16x16x32_bf16 %0, %1, %2, %0" : "+v"(acc[1][2]) : "v"(X0[1]), "v"(W0[2]));
    asm volatile("v_mfma_f32_16x16x32_bf16 %0, %1, %2, %0" : "+v"(acc[2][2]) : "v"(X0[2]), "v"(W0[2]));
    asm volatile("s_addk_i32 m0, 0x1000" ::: "scc", "memory");
    asm volatile("v_mfma_f32_16x16x32_bf16 %0, %1, %2, %0" : "+v"(acc[3][2]) : "v"(X0[3]), "v"(W0[2]));
    asm volatile("buffer_load_dwordx4 %0, %1, %2 offen lds" :: "v"(wv[3]), "s"(rW), "s"(kw) : "memory");
    asm volatile("v_mfma_f32_16x16x32_bf16 %0, %1, %2, %0" : "+v"(cs[2]) : "v"(ones3), "v"(W0[2]));
    asm volatile("v_mfma_f32_16x16x32_bf16 %0, %1, %2, %0" : "+v"(acc[0][3]) : "v"(X0[0]), "v"(W0[3]));
    asm volatile("v_mfma_f32_16x16x32_bf16 %0, %1, %2, %0" : "+v"(acc[1][3]) : "v"(X0[1]), "v"(W0[3]));
    asm volatile("v_mfma_f32_16x16x32_bf16 %0, %1, %2, %0" : "+v"(acc[2][3]) : "v"(X0[2]), "v"(W0[3]));
    asm volatile("s_addk_i32 m0, 0x1000" ::: "scc", "memory");
    asm volatile("v_mfma_f32_16x16x32_bf16 %0, %1, %2, %0" : "+v"(acc[3][3]) : "v"(X0[3]), "v"(W0[3]));
    asm volatile("buffer_load_dwordx4 %0, %1, %2 offen lds" :: "v"(wv[4]), "s"(rW), "s"(kw) : "memory");
    asm volatile("s_add_u32 %0, %0, 128\n\ts_cmp_eq_u32 %0, 1280\n\ts_cselect_b32 %0, 0, %0" : "+s"(kw) :: "scc");
    asm volatile("v_mfma_f32_16x16x32_bf16 %0, %1, %2, %0" : "+v"(cs[3]) : "v"(ones3), "v"(W0[3]));
    asm volatile("v_mfma_f32_16x16x32_bf16 %0, %1, %2, %0" : "+v"(acc[0][4]) : "v"(X0[0]), "v"(W0[4]));
    asm volatile("v_mfma_f32_16x16x32_bf16 %0, %1, %2, %0" : "+v"(acc[1][4]) : "v"(X0[1]), "v"(W0[4]));
    asm volatile("v_mfma_f32_16x16x32_bf16 %0, %1, %2, %0" : "+v"(acc[2][4]) : "v"(X0[2]), "v"(W0[4]));
    asm volatile("v_mfma_f32_16x16x32_bf16 %0, %1, %2, %0" : "+v"(acc[3][4]) : "v"(X0[3]), "v"(W0[4]));
    asm volatile("v_mfma_f32_16x16x32_bf16 %0, %1, %2, %0" : "+v"(cs[4]) : "v"(ones3), "v"(W0[4]));
    asm volatile("s_waitcnt vmcnt(9) lgkmcnt(0)\n\ts_barrier" ::: "memory");
    asm volatile("v_mfma_f32_16x16x32_bf16 %0, %1, %2, %0" : "+v"(acc[0][0]) : "v"(X1[0]), "v"(W1[0]));
    asm volatile("ds_read_b128 %0, %1 offset:0" : "=v"(X0[0]) : "v"(xa0) : "memory");
    asm volatile("s_add_u32 m0, %0, 28672" :: "s"(lw) : "scc", "memory");
    asm volatile("v_mfma_f32_16x16x32_bf16 %0, %1, %2, %0" : "+v"(acc[1][0]) : "v"(X1[1]), "v"(W1[0]));
    asm volatile("ds_read_b128 %0, %1 offset:2048" : "=v"(X0[1]) : "v"(xa0) : "memory");
    asm volatile("buffer_load_dwordx4 %0, %1, %2 offen lds" :: "v"(xv[0]), "s"(rX), "s"(kx) : "memory");
    asm volatile("v_mfma_f32_16x16x32_bf16 %0, %1, %2, %0" : "+v"(acc[2][0]) : "v"(X1[2]), "v"(W1[0]));
    asm volatile("ds_read_b128 %0, %1 offset:4096" : "=v"(X0[2]) : "v"(xa0) : "memory");
    asm volatile("v_mfma_f32_16x16x32_bf16 %0, %1, %2, %0" : "+v"(acc[3][0]) : "v"(X1[3]), "v"(W1[0]));
    asm volatile("ds_read_b128 %0, %1 offset:6144" : "=v"(X0[3]) : "v"(xa0) : "memory");
    asm volatile("v_mfma_f32_16x16x32_bf16 %0, %1, %2, %0" : "+v"(acc[0][1]) : "v"(X1[0]), "v"(W1[1]));
    asm volatile("ds_read_b128 %0, %1 offset:0" : "=v"(W0[0]) : "v"(wa0) : "memory");
    asm volatile("s_addk_i32 m0, 0x1000" ::: "scc", "memory");
    asm volatile("" : "+v"(cs[0]), "+v"(cs[1]), "+v"(cs[2]), "+v"(cs[3]), "+v"(cs[4]));
    const unsigned cso = (idx & 6) == 0 ? 4096000 + (0 + j)*6144 + (80*wn + lane)*32 + (4*((idx ^ (idx >> 3)) & 1) + 2*(xc & 1) + wm)*4 : OOB;
    asm volatile("buffer_store_dword %0, %1, %2, 0 offen offset:0 sc1" :: "v"(g == 0 ? cs[0].x : g == 1 ? cs[1].x : g == 2 ? cs[2].x : cs[3].x), "v"(cso), "s"(rS) : "memory");
    asm volatile("v_mfma_f32_16x16x32_bf16 %0, %1, %2, %0" : "+v"(acc[1][1]) : "v"(X1[1]), "v"(W1[1]));
    asm volatile("ds_read_b128 %0, %1 offset:2048" : "=v"(W0[1]) : "v"(wa0) : "memory");
    asm volatile("buffer_load_dwordx4 %0, %1, %2 offen lds" :: "v"(xv[1]), "s"(rX), "s"(kx) : "memory");
    asm volatile("v_mfma_f32_16x16x32_bf16 %0, %1, %2, %0" : "+v"(acc[2][1]) : "v"(X1[2]), "v"(W1[1]));
    asm volatile("ds_read_b128 %0, %1 offset:4096" : "=v"(W0[2]) : "v"(wa0) : "memory");
    asm volatile("v_mfma_f32_16x16x32_bf16 %0, %1, %2, %0" : "+v"(acc[3][1]) : "v"(X1[3]), "v"(W1[1]));
    asm volatile("ds_read_b128 %0, %1 offset:6144" : "=v"(W0[3]) : "v"(wa0) : "memory");
    asm volatile("v_mfma_f32_16x16x32_bf16 %0, %1, %2, %0" : "+v"(acc[0][2]) : "v"(X1[0]), "v"(W1[2]));
    asm volatile("ds_read_b128 %0, %1 offset:8192" : "=v"(W0[4]) : "v"(wa0) : "memory");
    asm volatile("s_addk_i32 m0, 0x1000" ::: "scc", "memory");
    asm volatile("buffer_store_dword %0, %1, %2, 0 offen offset:2048 sc1" :: "v"(cs[4].x), "v"(g < 1 ? cso : OOB), "s"(rS) : "memory");
    asm volatile("v_mfma_f32_16x16x32_bf16 %0, %1, %2, %0" : "+v"(acc[1][2]) : "v"(X1[1]), "v"(W1[2]));
    asm volatile("buffer_load_dwordx4 %0, %1, %2 offen lds" :: "v"(xv[2]), "s"(rX), "s"(kx) : "memory");
    asm volatile("v_mfma_f32_16x16x32_bf16 %0, %1, %2, %0" : "+v"(acc[2][2]) : "v"(X1[2]), "v"(W1[2]));
    asm volatile("v_mfma_f32_16x16x32_bf16 %0, %1, %2, %0" : "+v"(acc[3][2]) : "v"(X1[3]), "v"(W1[2]));
    u2 pa0, pa1, pb0, pb1, sst; u4 so2, so3; const unsigned po = lds0 + 135168 + (4*g + (r16 >> 2))*128 + 32*w + 8*(r16 & 3), pk = lds0 + 151552 + w*2560 + vl16;
    asm volatile("v_mfma_f32_16x16x32_bf16 %0, %1, %2, %0" : "+v"(acc[0][3]) : "v"(X1[0]), "v"(W1[3]));
    asm volatile("s_addk_i32 m0, 0x1000" ::: "scc", "memory");
    asm volatile("ds_read_b64_tr_b16 %0, %1 offset:0" : "=v"(pa0) : "v"(po) : "memory");
    asm volatile("v_mfma_f32_16x16x32_bf16 %0, %1, %2, %0" : "+v"(acc[1][3]) : "v"(X1[1]), "v"(W1[3]));
    asm volatile("buffer_load_dwordx4 %0, %1, %2 offen lds" :: "v"(xv[3]), "s"(rX), "s"(kx) : "memory");
    asm volatile("s_add_u32 %0, %0, 128\n\ts_cmp_eq_u32 %0, 1280\n\ts_cselect_b32 %0, 0, %0" : "+s"(kx) :: "scc");
    asm volatile("ds_read_b64_tr_b16 %0, %1 offset:2048" : "=v"(pa1) : "v"(po) : "memory");
    asm volatile("v_mfma_f32_16x16x32_bf16 %0, %1, %2, %0" : "+v"(acc[2][3]) : "v"(X1[2]), "v"(W1[3]));
    asm volatile("ds_read_b64_tr_b16 %0, %1 offset:4096" : "=v"(pb0) : "v"(po) : "memory");
    asm volatile("v_mfma_f32_16x16x32_bf16 %0, %1, %2, %0" : "+v"(acc[3][3]) : "v"(X1[3]), "v"(W1[3]));
    asm volatile("ds_read_b64_tr_b16 %0, %1 offset:6144" : "=v"(pb1) : "v"(po) : "memory");
    asm volatile("v_mfma_f32_16x16x32_bf16 %0, %1, %2, %0" : "+v"(acc[0][4]) : "v"(X1[0]), "v"(W1[4]));
    asm volatile("s_add_u32 m0, %0, 81920" :: "s"(lw) : "scc", "memory");
    asm volatile("ds_read_b128 %0, %1" : "=a"(so2) : "v"(pk) : "memory");
    asm volatile("v_mfma_f32_16x16x32_bf16 %0, %1, %2, %0" : "+v"(acc[1][4]) : "v"(X1[1]), "v"(W1[4]));
    asm volatile("buffer_load_dwordx4 %0, %1, %2 offen lds" :: "v"(wv[0]), "s"(rW), "s"(kw) : "memory");
    asm volatile("ds_read_b128 %0, %1 offset:1024" : "=a"(so3) : "v"(pk) : "memory");
    asm volatile("v_mfma_f32_16x16x32_bf16 %0, %1, %2, %0" : "+v"(acc[2][4]) : "v"(X1[2]), "v"(W1[4]));
    asm volatile("ds_read_b64 %0, %1 offset:2048" : "=a"(sst) : "v"(pk - lane*8) : "memory");
    asm volatile("v_mfma_f32_16x16x32_bf16 %0, %1, %2, %0" : "+v"(acc[3][4]) : "v"(X1[3]), "v"(W1[4]));
    asm volatile("s_waitcnt lgkmcnt(0)" ::: "memory");
    asm volatile("v_mfma_f32_16x16x32_bf16 %0, %1, %2, %0" : "+v"(acc[0][0]) : "v"(X0[0]), "v"(W0[0]));
    asm volatile("ds_read_b128 %0, %1 offset:0" : "=v"(X1[0]) : "v"(xa1) : "memory");
    asm volatile("s_addk_i32 m0, 0x1000" ::: "scc", "memory");
    asm volatile("v_mfma_f32_16x16x32_bf16 %0, %1, %2, %0" : "+v"(acc[1][0]) : "v"(X0[1]), "v"(W0[0]));
    asm volatile("ds_read_b128 %0, %1 offset:2048" : "=v"(X1[1]) : "v"(xa1) : "memory");
    asm volatile("buffer_load_dwordx4 %0, %1, %2 offen lds" :: "v"(wv[1]), "s"(rW), "s"(kw) : "memory");
    asm volatile("v_mfma_f32_16x16x32_bf16 %0, %1, %2, %0" : "+v"(acc[2][0]) : "v"(X0[2]), "v"(W0[0]));
    asm volatile("ds_read_b128 %0, %1 offset:4096" : "=v"(X1[2]) : "v"(xa1) : "memory");
    asm volatile("v_mfma_f32_16x16x32_bf16 %0, %1, %2, %0" : "+v"(acc[3][0]) : "v"(X0[3]), "v"(W0[0]));
    asm volatile("ds_read_b128 %0, %1 offset:6144" : "=v"(X1[3]) : "v"(xa1) : "memory");
    asm volatile("buffer_store_dwordx4 %0, %1, %2, %3 offen sc1\n\ts_nop 1" :: "v"(cat(pa0, pa1)), "v"(vl16), "s"(rS), "s"(262144 + ((40 + k0)*40 + d0 + w)*1024) : "memory");
    asm volatile("v_mfma_f32_16x16x32_bf16 %0, %1, %2, %0" : "+v"(acc[0][1]) : "v"(X0[0]), "v"(W0[1]));
    asm volatile("ds_read_b128 %0, %1 offset:0" : "=v"(W1[0]) : "v"(wa1) : "memory");
    asm volatile("s_addk_i32 m0, 0x1000" ::: "scc", "memory");
    asm volatile("v_mfma_f32_16x16x32_bf16 %0, %1, %2, %0" : "+v"(acc[1][1]) : "v"(X0[1]), "v"(W0[1]));
    asm volatile("ds_read_b128 %0, %1 offset:2048" : "=v"(W1[1]) : "v"(wa1) : "memory");
    asm volatile("buffer_load_dwordx4 %0, %1, %2 offen lds" :: "v"(wv[2]), "s"(rW), "s"(kw) : "memory");
    asm volatile("v_mfma_f32_16x16x32_bf16 %0, %1, %2, %0" : "+v"(acc[2][1]) : "v"(X0[2]), "v"(W0[1]));
    asm volatile("ds_read_b128 %0, %1 offset:4096" : "=v"(W1[2]) : "v"(wa1) : "memory");
    asm volatile("v_mfma_f32_16x16x32_bf16 %0, %1, %2, %0" : "+v"(acc[3][1]) : "v"(X0[3]), "v"(W0[1]));
    asm volatile("ds_read_b128 %0, %1 offset:6144" : "=v"(W1[3]) : "v"(wa1) : "memory");
    asm volatile("buffer_store_dwordx4 %0, %1, %2, %3 offen sc1\n\ts_nop 1" :: "v"(cat(pb0, pb1)), "v"(vl16), "s"(rS), "s"(262144 + ((40 + k1)*40 + d1 + w)*1024) : "memory");
    asm volatile("v_mfma_f32_16x16x32_bf16 %0, %1, %2, %0" : "+v"(acc[0][2]) : "v"(X0[0]), "v"(W0[2]));
    asm volatile("ds_read_b128 %0, %1 offset:8192" : "=v"(W1[4]) : "v"(wa1) : "memory");
    asm volatile("s_addk_i32 m0, 0x1000" ::: "scc", "memory");
    asm volatile("v_mfma_f32_16x16x32_bf16 %0, %1, %2, %0" : "+v"(acc[1][2]) : "v"(X0[1]), "v"(W0[2]));
    asm volatile("buffer_load_dwordx4 %0, %1, %2 offen lds" :: "v"(wv[3]), "s"(rW), "s"(kw) : "memory");
    asm volatile("v_mfma_f32_16x16x32_bf16 %0, %1, %2, %0" : "+v"(acc[2][2]) : "v"(X0[2]), "v"(W0[2]));
    asm volatile("v_mfma_f32_16x16x32_bf16 %0, %1, %2, %0" : "+v"(acc[3][2]) : "v"(X0[3]), "v"(W0[2]));
    asm volatile("buffer_store_dwordx4 %0, %1, %2, %3 offen sc1\n\ts_nop 1" :: "a"(so2), "v"(vl16), "s"(rS), "s"(262144 + ((4*cpb + w)*40 + cdt)*1024) : "memory");
    asm volatile("v_mfma_f32_16x16x32_bf16 %0, %1, %2, %0" : "+v"(acc[0][3]) : "v"(X0[0]), "v"(W0[3]));
    asm volatile("s_addk_i32 m0, 0x1000" ::: "scc", "memory");
    asm volatile("v_mfma_f32_16x16x32_bf16 %0, %1, %2, %0" : "+v"(acc[1][3]) : "v"(X0[1]), "v"(W0[3]));
    asm volatile("buffer_load_dwordx4 %0, %1, %2 offen lds" :: "v"(wv[4]), "s"(rW), "s"(kw) : "memory");
    asm volatile("s_add_u32 %0, %0, 128\n\ts_cmp_eq_u32 %0, 1280\n\ts_cselect_b32 %0, 0, %0" : "+s"(kw) :: "scc");
    asm volatile("v_mfma_f32_16x16x32_bf16 %0, %1, %2, %0" : "+v"(acc[2][3]) : "v"(X0[2]), "v"(W0[3]));
    asm volatile("v_mfma_f32_16x16x32_bf16 %0, %1, %2, %0" : "+v"(acc[3][3]) : "v"(X0[3]), "v"(W0[3]));
    asm volatile("buffer_store_dwordx4 %0, %1, %2, %3 offen sc1\n\ts_nop 1" :: "a"(so3), "v"(vl16), "s"(rS), "s"(262144 + ((4*cpb + w)*40 + cdt + 1)*1024) : "memory");
    asm volatile("v_mfma_f32_16x16x32_bf16 %0, %1, %2, %0" : "+v"(acc[0][4]) : "v"(X0[0]), "v"(W0[4]));
    asm volatile("v_mfma_f32_16x16x32_bf16 %0, %1, %2, %0" : "+v"(acc[1][4]) : "v"(X0[1]), "v"(W0[4]));
    asm volatile("v_mfma_f32_16x16x32_bf16 %0, %1, %2, %0" : "+v"(acc[2][4]) : "v"(X0[2]), "v"(W0[4]));
    asm volatile("v_mfma_f32_16x16x32_bf16 %0, %1, %2, %0" : "+v"(acc[3][4]) : "v"(X0[3]), "v"(W0[4]));
    asm volatile("buffer_store_dwordx2 %0, %1, %2, 0 offen sc1" :: "a"(sst), "v"((lane & 31) == 0 ? 40960 + (srow + (lane >> 5))*8 : OOB), "s"(rS) : "memory");
    asm volatile("s_waitcnt vmcnt(16) lgkmcnt(0)\n\ts_barrier" ::: "memory");
    asm volatile("v_mfma_f32_16x16x32_bf16 %0, %1, %2, %0" : "+v"(acc[0][0]) : "v"(X1[0]), "v"(W1[0]));
    asm volatile("ds_read_b128 %0, %1 offset:32768" : "=v"(X0[0]) : "v"(xa0) : "memory");
    asm volatile("s_add_u32 m0, %0, 12288" :: "s"(lw) : "scc", "memory");
    asm volatile("v_mfma_f32_16x16x32_bf16 %0, %1, %2, %0" : "+v"(acc[1][0]) : "v"(X1[1]), "v"(W1[0]));
    asm volatile("ds_read_b128 %0, %1 offset:34816" : "=v"(X0[1]) : "v"(xa0) : "memory");
    asm volatile("buffer_load_dwordx4 %0, %1, %2 offen lds" :: "v"(xv[0]), "s"(rX), "s"(kx) : "memory");
    asm volatile("v_mfma_f32_16x16x32_bf16 %0, %1, %2, %0" : "+v"(acc[2][0]) : "v"(X1[2]), "v"(W1[0]));
    asm volatile("ds_read_b128 %0, %1 offset:36864" : "=v"(X0[2]) : "v"(xa0) : "memory");
    asm volatile("v_mfma_f32_16x16x32_bf16 %0, %1, %2, %0" : "+v"(acc[3][0]) : "v"(X1[3]), "v"(W1[0]));
    asm volatile("ds_read_b128 %0, %1 offset:38912" : "=v"(X0[3]) : "v"(xa0) : "memory");
    asm volatile("v_mfma_f32_16x16x32_bf16 %0, %1, %2, %0" : "+v"(acc[0][1]) : "v"(X1[0]), "v"(W1[1]));
    asm volatile("ds_read_b128 %0, %1 offset:40960" : "=v"(W0[0]) : "v"(wa0) : "memory");
    asm volatile("s_addk_i32 m0, 0x1000" ::: "scc", "memory");
    asm volatile("v_mfma_f32_16x16x32_bf16 %0, %1, %2, %0" : "+v"(acc[1][1]) : "v"(X1[1]), "v"(W1[1]));
    asm volatile("ds_read_b128 %0, %1 offset:43008" : "=v"(W0[1]) : "v"(wa0) : "memory");
    asm volatile("buffer_load_dwordx4 %0, %1, %2 offen lds" :: "v"(xv[1]), "s"(rX), "s"(kx) : "memory");
    asm volatile("v_mfma_f32_16x16x32_bf16 %0, %1, %2, %0" : "+v"(acc[2][1]) : "v"(X1[2]), "v"(W1[1]));
    asm volatile("ds_read_b128 %0, %1 offset:45056" : "=v"(W0[2]) : "v"(wa0) : "memory");
    asm volatile("v_mfma_f32_16x16x32_bf16 %0, %1, %2, %0" : "+v"(acc[3][1]) : "v"(X1[3]), "v"(W1[1]));
    asm volatile("ds_read_b128 %0, %1 offset:47104" : "=v"(W0[3]) : "v"(wa0) : "memory");
    asm volatile("v_mfma_f32_16x16x32_bf16 %0, %1, %2, %0" : "+v"(acc[0][2]) : "v"(X1[0]), "v"(W1[2]));
    asm volatile("ds_read_b128 %0, %1 offset:49152" : "=v"(W0[4]) : "v"(wa0) : "memory");
    asm volatile("s_addk_i32 m0, 0x1000" ::: "scc", "memory");
    asm volatile("v_mfma_f32_16x16x32_bf16 %0, %1, %2, %0" : "+v"(acc[1][2]) : "v"(X1[1]), "v"(W1[2]));
    asm volatile("buffer_load_dwordx4 %0, %1, %2 offen lds" :: "v"(xv[2]), "s"(rX), "s"(kx) : "memory");
    asm volatile("v_mfma_f32_16x16x32_bf16 %0, %1, %2, %0" : "+v"(acc[2][2]) : "v"(X1[2]), "v"(W1[2]));
    asm volatile("v_mfma_f32_16x16x32_bf16 %0, %1, %2, %0" : "+v"(acc[3][2]) : "v"(X1[3]), "v"(W1[2]));
    asm volatile("v_mfma_f32_16x16x32_bf16 %0, %1, %2, %0" : "+v"(acc[0][3]) : "v"(X1[0]), "v"(W1[3]));
    asm volatile("s_addk_i32 m0, 0x1000" ::: "scc", "memory");
    asm volatile("v_mfma_f32_16x16x32_bf16 %0, %1, %2, %0" : "+v"(acc[1][3]) : "v"(X1[1]), "v"(W1[3]));
    asm volatile("buffer_load_dwordx4 %0, %1, %2 offen lds" :: "v"(xv[3]), "s"(rX), "s"(kx) : "memory");
    asm volatile("s_add_u32 %0, %0, 128\n\ts_cmp_eq_u32 %0, 1280\n\ts_cselect_b32 %0, 0, %0" : "+s"(kx) :: "scc");
    asm volatile("v_mfma_f32_16x16x32_bf16 %0, %1, %2, %0" : "+v"(acc[2][3]) : "v"(X1[2]), "v"(W1[3]));
    asm volatile("v_mfma_f32_16x16x32_bf16 %0, %1, %2, %0" : "+v"(acc[3][3]) : "v"(X1[3]), "v"(W1[3]));
    asm volatile("v_mfma_f32_16x16x32_bf16 %0, %1, %2, %0" : "+v"(acc[0][4]) : "v"(X1[0]), "v"(W1[4]));
    asm volatile("s_add_u32 m0, %0, 61440" :: "s"(lw) : "scc", "memory");
    asm volatile("v_mfma_f32_16x16x32_bf16 %0, %1, %2, %0" : "+v"(acc[1][4]) : "v"(X1[1]), "v"(W1[4]));
    asm volatile("buffer_load_dwordx4 %0, %1, %2 offen lds" :: "v"(wv[0]), "s"(rW), "s"(kw) : "memory");
    asm volatile("v_mfma_f32_16x16x32_bf16 %0, %1, %2, %0" : "+v"(acc[2][4]) : "v"(X1[2]), "v"(W1[4]));
    asm volatile("v_mfma_f32_16x16x32_bf16 %0, %1, %2, %0" : "+v"(acc[3][4]) : "v"(X1[3]), "v"(W1[4]));
    asm volatile("s_waitcnt lgkmcnt(0)" ::: "memory");
    asm volatile("v_mfma_f32_16x16x32_bf16 %0, %1, %2, %0" : "+v"(acc[0][0]) : "v"(X0[0]), "v"(W0[0]));
    asm volatile("ds_read_b128 %0, %1 offset:32768" : "=v"(X1[0]) : "v"(xa1) : "memory");
    asm volatile("s_addk_i32 m0, 0x1000" ::: "scc", "memory");
    asm volatile("v_mfma_f32_16x16x32_bf16 %0, %1, %2, %0" : "+v"(acc[1][0]) : "v"(X0[1]), "v"(W0[0]));
    asm volatile("ds_read_b128 %0, %1 offset:34816" : "=v"(X1[1]) : "v"(xa1) : "memory");
    asm volatile("buffer_load_dwordx4 %0, %1, %2 offen lds" :: "v"(wv[1]), "s"(rW), "s"(kw) : "memory");
    asm volatile("v_mfma_f32_16x16x32_bf16 %0, %1, %2, %0" : "+v"(acc[2][0]) : "v"(X0[2]), "v"(W0[0]));
    asm volatile("ds_read_b128 %0, %1 offset:36864" : "=v"(X1[2]) : "v"(xa1) : "memory");
    asm volatile("v_mfma_f32_16x16x32_bf16 %0, %1, %2, %0" : "+v"(acc[3][0]) : "v"(X0[3]), "v"(W0[0]));
    asm volatile("ds_read_b128 %0, %1 offset:38912" : "=v"(X1[3]) : "v"(xa1) : "memory");
    asm volatile("v_mfma_f32_16x16x32_bf16 %0, %1, %2, %0" : "+v"(acc[0][1]) : "v"(X0[0]), "v"(W0[1]));
    asm volatile("ds_read_b128 %0, %1 offset:40960" : "=v"(W1[0]) : "v"(wa1) : "memory");
    asm volatile("s_addk_i32 m0, 0x1000" ::: "scc", "memory");
    asm volatile("v_mfma_f32_16x16x32_bf16 %0, %1, %2, %0" : "+v"(acc[1][1]) : "v"(X0[1]), "v"(W0[1]));
    asm volatile("ds_read_b128 %0, %1 offset:43008" : "=v"(W1[1]) : "v"(wa1) : "memory");
    asm volatile("buffer_load_dwordx4 %0, %1, %2 offen lds" :: "v"(wv[2]), "s"(rW), "s"(kw) : "memory");
    asm volatile("v_mfma_f32_16x16x32_bf16 %0, %1, %2, %0" : "+v"(acc[2][1]) : "v"(X0[2]), "v"(W0[1]));
    asm volatile("ds_read_b128 %0, %1 offset:45056" : "=v"(W1[2]) : "v"(wa1) : "memory");
    asm volatile("v_mfma_f32_16x16x32_bf16 %0, %1, %2, %0" : "+v"(acc[3][1]) : "v"(X0[3]), "v"(W0[1]));
    asm volatile("ds_read_b128 %0, %1 offset:47104" : "=v"(W1[3]) : "v"(wa1) : "memory");
    asm volatile("v_mfma_f32_16x16x32_bf16 %0, %1, %2, %0" : "+v"(acc[0][2]) : "v"(X0[0]), "v"(W0[2]));
    asm volatile("ds_read_b128 %0, %1 offset:49152" : "=v"(W1[4]) : "v"(wa1) : "memory");
    asm volatile("s_addk_i32 m0, 0x1000" ::: "scc", "memory");
    asm volatile("v_mfma_f32_16x16x32_bf16 %0, %1, %2, %0" : "+v"(acc[1][2]) : "v"(X0[1]), "v"(W0[2]));
    asm volatile("buffer_load_dwordx4 %0, %1, %2 offen lds" :: "v"(wv[3]), "s"(rW), "s"(kw) : "memory");
    asm volatile("v_mfma_f32_16x16x32_bf16 %0, %1, %2, %0" : "+v"(acc[2][2]) : "v"(X0[2]), "v"(W0[2]));
    asm volatile("v_mfma_f32_16x16x32_bf16 %0, %1, %2, %0" : "+v"(acc[3][2]) : "v"(X0[3]), "v"(W0[2]));
    asm volatile("v_mfma_f32_16x16x32_bf16 %0, %1, %2, %0" : "+v"(acc[0][3]) : "v"(X0[0]), "v"(W0[3]));
    asm volatile("s_addk_i32 m0, 0x1000" ::: "scc", "memory");
    asm volatile("v_mfma_f32_16x16x32_bf16 %0, %1, %2, %0" : "+v"(acc[1][3]) : "v"(X0[1]), "v"(W0[3]));
    asm volatile("buffer_load_dwordx4 %0, %1, %2 offen lds" :: "v"(wv[4]), "s"(rW), "s"(kw) : "memory");
    asm volatile("s_add_u32 %0, %0, 128\n\ts_cmp_eq_u32 %0, 1280\n\ts_cselect_b32 %0, 0, %0" : "+s"(kw) :: "scc");
    asm volatile("v_mfma_f32_16x16x32_bf16 %0, %1, %2, %0" : "+v"(acc[2][3]) : "v"(X0[2]), "v"(W0[3]));
    asm volatile("v_mfma_f32_16x16x32_bf16 %0, %1, %2, %0" : "+v"(acc[3][3]) : "v"(X0[3]), "v"(W0[3]));
    asm volatile("v_mfma_f32_16x16x32_bf16 %0, %1, %2, %0" : "+v"(acc[0][4]) : "v"(X0[0]), "v"(W0[4]));
    asm volatile("v_mfma_f32_16x16x32_bf16 %0, %1, %2, %0" : "+v"(acc[1][4]) : "v"(X0[1]), "v"(W0[4]));
    asm volatile("v_mfma_f32_16x16x32_bf16 %0, %1, %2, %0" : "+v"(acc[2][4]) : "v"(X0[2]), "v"(W0[4]));
    asm volatile("v_mfma_f32_16x16x32_bf16 %0, %1, %2, %0" : "+v"(acc[3][4]) : "v"(X0[3]), "v"(W0[4]));
    asm volatile("s_waitcnt vmcnt(9) lgkmcnt(0)\n\ts_barrier" ::: "memory");
    __builtin_amdgcn_raw_buffer_store_b32(tag, rS, tid == 0 ? 3932160 + b*64 : OOB, 0, SC1);
    asm volatile("v_mfma_f32_16x16x32_bf16 %0, %1, %2, %0" : "+v"(acc[0][0]) : "v"(X1[0]), "v"(W1[0]));
    asm volatile("ds_read_b128 %0, %1 offset:16384" : "=v"(X0[0]) : "v"(xa0) : "memory");
    asm volatile("s_add_u32 m0, %0, 45056" :: "s"(lw) : "scc", "memory");
    asm volatile("v_mfma_f32_16x16x32_bf16 %0, %1, %2, %0" : "+v"(acc[1][0]) : "v"(X1[1]), "v"(W1[0]));
    asm volatile("ds_read_b128 %0, %1 offset:18432" : "=v"(X0[1]) : "v"(xa0) : "memory");
    asm volatile("buffer_load_dwordx4 %0, %1, %2 offen lds" :: "v"(xv[0]), "s"(rX), "s"(kx) : "memory");
    asm volatile("v_mfma_f32_16x16x32_bf16 %0, %1, %2, %0" : "+v"(acc[2][0]) : "v"(X1[2]), "v"(W1[0]));
    asm volatile("ds_read_b128 %0, %1 offset:20480" : "=v"(X0[2]) : "v"(xa0) : "memory");
    asm volatile("v_mfma_f32_16x16x32_bf16 %0, %1, %2, %0" : "+v"(acc[3][0]) : "v"(X1[3]), "v"(W1[0]));
    asm volatile("ds_read_b128 %0, %1 offset:22528" : "=v"(X0[3]) : "v"(xa0) : "memory");
    asm volatile("v_mfma_f32_16x16x32_bf16 %0, %1, %2, %0" : "+v"(acc[0][1]) : "v"(X1[0]), "v"(W1[1]));
    asm volatile("ds_read_b128 %0, %1 offset:20480" : "=v"(W0[0]) : "v"(wa0) : "memory");
    asm volatile("s_addk_i32 m0, 0x1000" ::: "scc", "memory");
    asm volatile("v_mfma_f32_16x16x32_bf16 %0, %1, %2, %0" : "+v"(acc[1][1]) : "v"(X1[1]), "v"(W1[1]));
    asm volatile("ds_read_b128 %0, %1 offset:22528" : "=v"(W0[1]) : "v"(wa0) : "memory");
    asm volatile("buffer_load_dwordx4 %0, %1, %2 offen lds" :: "v"(xv[1]), "s"(rX), "s"(kx) : "memory");
    asm volatile("v_mfma_f32_16x16x32_bf16 %0, %1, %2, %0" : "+v"(acc[2][1]) : "v"(X1[2]), "v"(W1[1]));
    asm volatile("ds_read_b128 %0, %1 offset:24576" : "=v"(W0[2]) : "v"(wa0) : "memory");
    asm volatile("v_mfma_f32_16x16x32_bf16 %0, %1, %2, %0" : "+v"(acc[3][1]) : "v"(X1[3]), "v"(W1[1]));
    asm volatile("ds_read_b128 %0, %1 offset:26624" : "=v"(W0[3]) : "v"(wa0) : "memory");
    asm volatile("v_mfma_f32_16x16x32_bf16 %0, %1, %2, %0" : "+v"(acc[0][2]) : "v"(X1[0]), "v"(W1[2]));
    asm volatile("ds_read_b128 %0, %1 offset:28672" : "=v"(W0[4]) : "v"(wa0) : "memory");
    asm volatile("s_addk_i32 m0, 0x1000" ::: "scc", "memory");
    asm volatile("v_mfma_f32_16x16x32_bf16 %0, %1, %2, %0" : "+v"(acc[1][2]) : "v"(X1[1]), "v"(W1[2]));
    asm volatile("buffer_load_dwordx4 %0, %1, %2 offen lds" :: "v"(xv[2]), "s"(rX), "s"(kx) : "memory");
    asm volatile("v_mfma_f32_16x16x32_bf16 %0, %1, %2, %0" : "+v"(acc[2][2]) : "v"(X1[2]), "v"(W1[2]));
    asm volatile("v_mfma_f32_16x16x32_bf16 %0, %1, %2, %0" : "+v"(acc[3][2]) : "v"(X1[3]), "v"(W1[2]));
    asm volatile("v_mfma_f32_16x16x32_bf16 %0, %1, %2, %0" : "+v"(acc[0][3]) : "v"(X1[0]), "v"(W1[3]));
    asm volatile("s_addk_i32 m0, 0x1000" ::: "scc", "memory");
    asm volatile("v_mfma_f32_16x16x32_bf16 %0, %1, %2, %0" : "+v"(acc[1][3]) : "v"(X1[1]), "v"(W1[3]));
    asm volatile("buffer_load_dwordx4 %0, %1, %2 offen lds" :: "v"(xv[3]), "s"(rX), "s"(kx) : "memory");
    asm volatile("s_add_u32 %0, %0, 128\n\ts_cmp_eq_u32 %0, 1280\n\ts_cselect_b32 %0, 0, %0" : "+s"(kx) :: "scc");
    asm volatile("v_mfma_f32_16x16x32_bf16 %0, %1, %2, %0" : "+v"(acc[2][3]) : "v"(X1[2]), "v"(W1[3]));
    asm volatile("v_mfma_f32_16x16x32_bf16 %0, %1, %2, %0" : "+v"(acc[3][3]) : "v"(X1[3]), "v"(W1[3]));
    asm volatile("v_mfma_f32_16x16x32_bf16 %0, %1, %2, %0" : "+v"(acc[0][4]) : "v"(X1[0]), "v"(W1[4]));
    asm volatile("s_add_u32 m0, %0, 102400" :: "s"(lw) : "scc", "memory");
    asm volatile("v_mfma_f32_16x16x32_bf16 %0, %1, %2, %0" : "+v"(acc[1][4]) : "v"(X1[1]), "v"(W1[4]));
    asm volatile("buffer_load_dwordx4 %0, %1, %2 offen lds" :: "v"(wv[0]), "s"(rW), "s"(kw) : "memory");
    asm volatile("v_mfma_f32_16x16x32_bf16 %0, %1, %2, %0" : "+v"(acc[2][4]) : "v"(X1[2]), "v"(W1[4]));
    asm volatile("v_mfma_f32_16x16x32_bf16 %0, %1, %2, %0" : "+v"(acc[3][4]) : "v"(X1[3]), "v"(W1[4]));
    asm volatile("s_waitcnt lgkmcnt(0)" ::: "memory");
    asm volatile("v_mfma_f32_16x16x32_bf16 %0, %1, %2, %0" : "+v"(acc[0][0]) : "v"(X0[0]), "v"(W0[0]));
    asm volatile("ds_read_b128 %0, %1 offset:16384" : "=v"(X1[0]) : "v"(xa1) : "memory");
    asm volatile("s_addk_i32 m0, 0x1000" ::: "scc", "memory");
    asm volatile("v_mfma_f32_16x16x32_bf16 %0, %1, %2, %0" : "+v"(acc[1][0]) : "v"(X0[1]), "v"(W0[0]));
    asm volatile("ds_read_b128 %0, %1 offset:18432" : "=v"(X1[1]) : "v"(xa1) : "memory");
    asm volatile("buffer_load_dwordx4 %0, %1, %2 offen lds" :: "v"(wv[1]), "s"(rW), "s"(kw) : "memory");
    asm volatile("v_mfma_f32_16x16x32_bf16 %0, %1, %2, %0" : "+v"(acc[2][0]) : "v"(X0[2]), "v"(W0[0]));
    asm volatile("ds_read_b128 %0, %1 offset:20480" : "=v"(X1[2]) : "v"(xa1) : "memory");
    asm volatile("v_mfma_f32_16x16x32_bf16 %0, %1, %2, %0" : "+v"(acc[3][0]) : "v"(X0[3]), "v"(W0[0]));
    asm volatile("ds_read_b128 %0, %1 offset:22528" : "=v"(X1[3]) : "v"(xa1) : "memory");
    asm volatile("v_mfma_f32_16x16x32_bf16 %0, %1, %2, %0" : "+v"(acc[0][1]) : "v"(X0[0]), "v"(W0[1]));
    asm volatile("ds_read_b128 %0, %1 offset:20480" : "=v"(W1[0]) : "v"(wa1) : "memory");
    asm volatile("s_addk_i32 m0, 0x1000" ::: "scc", "memory");
    asm volatile("v_mfma_f32_16x16x32_bf16 %0, %1, %2, %0" : "+v"(acc[1][1]) : "v"(X0[1]), "v"(W0[1]));
    asm volatile("ds_read_b128 %0, %1 offset:22528" : "=v"(W1[1]) : "v"(wa1) : "memory");
    asm volatile("buffer_load_dwordx4 %0, %1, %2 offen lds" :: "v"(wv[2]), "s"(rW), "s"(kw) : "memory");
    asm volatile("v_mfma_f32_16x16x32_bf16 %0, %1, %2, %0" : "+v"(acc[2][1]) : "v"(X0[2]), "v"(W0[1]));
    asm volatile("ds_read_b128 %0, %1 offset:24576" : "=v"(W1[2]) : "v"(wa1) : "memory");
    asm volatile("v_mfma_f32_16x16x32_bf16 %0, %1, %2, %0" : "+v"(acc[3][1]) : "v"(X0[3]), "v"(W0[1]));
    asm volatile("ds_read_b128 %0, %1 offset:26624" : "=v"(W1[3]) : "v"(wa1) : "memory");
    asm volatile("v_mfma_f32_16x16x32_bf16 %0, %1, %2, %0" : "+v"(acc[0][2]) : "v"(X0[0]), "v"(W0[2]));
    asm volatile("ds_read_b128 %0, %1 offset:28672" : "=v"(W1[4]) : "v"(wa1) : "memory");
    asm volatile("s_addk_i32 m0, 0x1000" ::: "scc", "memory");
    asm volatile("v_mfma_f32_16x16x32_bf16 %0, %1, %2, %0" : "+v"(acc[1][2]) : "v"(X0[1]), "v"(W0[2]));
    asm volatile("buffer_load_dwordx4 %0, %1, %2 offen lds" :: "v"(wv[3]), "s"(rW), "s"(kw) : "memory");
    asm volatile("v_mfma_f32_16x16x32_bf16 %0, %1, %2, %0" : "+v"(acc[2][2]) : "v"(X0[2]), "v"(W0[2]));
    asm volatile("v_mfma_f32_16x16x32_bf16 %0, %1, %2, %0" : "+v"(acc[3][2]) : "v"(X0[3]), "v"(W0[2]));
    asm volatile("v_mfma_f32_16x16x32_bf16 %0, %1, %2, %0" : "+v"(acc[0][3]) : "v"(X0[0]), "v"(W0[3]));
    asm volatile("s_addk_i32 m0, 0x1000" ::: "scc", "memory");
    asm volatile("v_mfma_f32_16x16x32_bf16 %0, %1, %2, %0" : "+v"(acc[1][3]) : "v"(X0[1]), "v"(W0[3]));
    asm volatile("buffer_load_dwordx4 %0, %1, %2 offen lds" :: "v"(wv[4]), "s"(rW), "s"(kw) : "memory");
    asm volatile("s_add_u32 %0, %0, 128\n\ts_cmp_eq_u32 %0, 1280\n\ts_cselect_b32 %0, 0, %0" : "+s"(kw) :: "scc");
    asm volatile("v_mfma_f32_16x16x32_bf16 %0, %1, %2, %0" : "+v"(acc[2][3]) : "v"(X0[2]), "v"(W0[3]));
    asm volatile("v_mfma_f32_16x16x32_bf16 %0, %1, %2, %0" : "+v"(acc[3][3]) : "v"(X0[3]), "v"(W0[3]));
    asm volatile("v_mfma_f32_16x16x32_bf16 %0, %1, %2, %0" : "+v"(acc[0][4]) : "v"(X0[0]), "v"(W0[4]));
    asm volatile("v_mfma_f32_16x16x32_bf16 %0, %1, %2, %0" : "+v"(acc[1][4]) : "v"(X0[1]), "v"(W0[4]));
    asm volatile("v_mfma_f32_16x16x32_bf16 %0, %1, %2, %0" : "+v"(acc[2][4]) : "v"(X0[2]), "v"(W0[4]));
    asm volatile("v_mfma_f32_16x16x32_bf16 %0, %1, %2, %0" : "+v"(acc[3][4]) : "v"(X0[3]), "v"(W0[4]));
    asm volatile("s_waitcnt vmcnt(10) lgkmcnt(0)\n\ts_barrier" ::: "memory");
    asm volatile("v_mfma_f32_16x16x32_bf16 %0, %1, %2, %0" : "+v"(acc[0][0]) : "v"(X1[0]), "v"(W1[0]));
    asm volatile("ds_read_b128 %0, %1 offset:0" : "=v"(X0[0]) : "v"(xa0) : "memory");
    asm volatile("s_add_u32 m0, %0, 28672" :: "s"(lw) : "scc", "memory");
    asm volatile("v_mfma_f32_16x16x32_bf16 %0, %1, %2, %0" : "+v"(acc[1][0]) : "v"(X1[1]), "v"(W1[0]));
    asm volatile("ds_read_b128 %0, %1 offset:2048" : "=v"(X0[1]) : "v"(xa0) : "memory");
    asm volatile("buffer_load_dwordx4 %0, %1, %2 offen lds" :: "v"(xv[0]), "s"(rX), "s"(kx) : "memory");
    asm volatile("v_mfma_f32_16x16x32_bf16 %0, %1, %2, %0" : "+v"(acc[2][0]) : "v"(X1[2]), "v"(W1[0]));
    asm volatile("ds_read_b128 %0, %1 offset:4096" : "=v"(X0[2]) : "v"(xa0) : "memory");
    asm volatile("v_mfma_f32_16x16x32_bf16 %0, %1, %2, %0" : "+v"(acc[3][0]) : "v"(X1[3]), "v"(W1[0]));
    asm volatile("ds_read_b128 %0, %1 offset:6144" : "=v"(X0[3]) : "v"(xa0) : "memory");
    asm volatile("v_mfma_f32_16x16x32_bf16 %0, %1, %2, %0" : "+v"(acc[0][1]) : "v"(X1[0]), "v"(W1[1]));
    asm volatile("ds_read_b128 %0, %1 offset:0" : "=v"(W0[0]) : "v"(wa0) : "memory");
    asm volatile("s_addk_i32 m0, 0x1000" ::: "scc", "memory");
    asm volatile("v_mfma_f32_16x16x32_bf16 %0, %1, %2, %0" : "+v"(acc[1][1]) : "v"(X1[1]), "v"(W1[1]));
    asm volatile("ds_read_b128 %0, %1 offset:2048" : "=v"(W0[1]) : "v"(wa0) : "memory");
    asm volatile("buffer_load_dwordx4 %0, %1, %2 offen lds" :: "v"(xv[1]), "s"(rX), "s"(kx) : "memory");
    asm volatile("v_mfma_f32_16x16x32_bf16 %0, %1, %2, %0" : "+v"(acc[2][1]) : "v"(X1[2]), "v"(W1[1]));
    asm volatile("ds_read_b128 %0, %1 offset:4096" : "=v"(W0[2]) : "v"(wa0) : "memory");
    asm volatile("v_mfma_f32_16x16x32_bf16 %0, %1, %2, %0" : "+v"(acc[3][1]) : "v"(X1[3]), "v"(W1[1]));
    asm volatile("ds_read_b128 %0, %1 offset:6144" : "=v"(W0[3]) : "v"(wa0) : "memory");
    asm volatile("v_mfma_f32_16x16x32_bf16 %0, %1, %2, %0" : "+v"(acc[0][2]) : "v"(X1[0]), "v"(W1[2]));
    asm volatile("ds_read_b128 %0, %1 offset:8192" : "=v"(W0[4]) : "v"(wa0) : "memory");
    asm volatile("s_addk_i32 m0, 0x1000" ::: "scc", "memory");
    asm volatile("v_mfma_f32_16x16x32_bf16 %0, %1, %2, %0" : "+v"(acc[1][2]) : "v"(X1[1]), "v"(W1[2]));
    asm volatile("buffer_load_dwordx4 %0, %1, %2 offen lds" :: "v"(xv[2]), "s"(rX), "s"(kx) : "memory");
    asm volatile("v_mfma_f32_16x16x32_bf16 %0, %1, %2, %0" : "+v"(acc[2][2]) : "v"(X1[2]), "v"(W1[2]));
    asm volatile("v_mfma_f32_16x16x32_bf16 %0, %1, %2, %0" : "+v"(acc[3][2]) : "v"(X1[3]), "v"(W1[2]));
    asm volatile("v_mfma_f32_16x16x32_bf16 %0, %1, %2, %0" : "+v"(acc[0][3]) : "v"(X1[0]), "v"(W1[3]));
    asm volatile("s_addk_i32 m0, 0x1000" ::: "scc", "memory");
    asm volatile("v_mfma_f32_16x16x32_bf16 %0, %1, %2, %0" : "+v"(acc[1][3]) : "v"(X1[1]), "v"(W1[3]));
    asm volatile("buffer_load_dwordx4 %0, %1, %2 offen lds" :: "v"(xv[3]), "s"(rX), "s"(kx) : "memory");
    asm volatile("s_add_u32 %0, %0, 128\n\ts_cmp_eq_u32 %0, 1280\n\ts_cselect_b32 %0, 0, %0" : "+s"(kx) :: "scc");
    asm volatile("v_mfma_f32_16x16x32_bf16 %0, %1, %2, %0" : "+v"(acc[2][3]) : "v"(X1[2]), "v"(W1[3]));
    asm volatile("v_mfma_f32_16x16x32_bf16 %0, %1, %2, %0" : "+v"(acc[3][3]) : "v"(X1[3]), "v"(W1[3]));
    asm volatile("v_mfma_f32_16x16x32_bf16 %0, %1, %2, %0" : "+v"(acc[0][4]) : "v"(X1[0]), "v"(W1[4]));
    asm volatile("s_add_u32 m0, %0, 81920" :: "s"(lw) : "scc", "memory");
    asm volatile("v_mfma_f32_16x16x32_bf16 %0, %1, %2, %0" : "+v"(acc[1][4]) : "v"(X1[1]), "v"(W1[4]));
    asm volatile("buffer_load_dwordx4 %0, %1, %2 offen lds" :: "v"(wv[0]), "s"(rW), "s"(kw) : "memory");
    asm volatile("v_mfma_f32_16x16x32_bf16 %0, %1, %2, %0" : "+v"(acc[2][4]) : "v"(X1[2]), "v"(W1[4]));
    asm volatile("v_mfma_f32_16x16x32_bf16 %0, %1, %2, %0" : "+v"(acc[3][4]) : "v"(X1[3]), "v"(W1[4]));
    asm volatile("s_waitcnt lgkmcnt(0)" ::: "memory");
    asm volatile("v_mfma_f32_16x16x32_bf16 %0, %1, %2, %0" : "+v"(acc[0][0]) : "v"(X0[0]), "v"(W0[0]));
    asm volatile("ds_read_b128 %0, %1 offset:0" : "=v"(X1[0]) : "v"(xa1) : "memory");
    asm volatile("s_addk_i32 m0, 0x1000" ::: "scc", "memory");
    asm volatile("v_mfma_f32_16x16x32_bf16 %0, %1, %2, %0" : "+v"(acc[1][0]) : "v"(X0[1]), "v"(W0[0]));
    asm volatile("ds_read_b128 %0, %1 offset:2048" : "=v"(X1[1]) : "v"(xa1) : "memory");
    asm volatile("buffer_load_dwordx4 %0, %1, %2 offen lds" :: "v"(wv[1]), "s"(rW), "s"(kw) : "memory");
    asm volatile("v_mfma_f32_16x16x32_bf16 %0, %1, %2, %0" : "+v"(acc[2][0]) : "v"(X0[2]), "v"(W0[0]));
    asm volatile("ds_read_b128 %0, %1 offset:4096" : "=v"(X1[2]) : "v"(xa1) : "memory");
    asm volatile("v_mfma_f32_16x16x32_bf16 %0, %1, %2, %0" : "+v"(acc[3][0]) : "v"(X0[3]), "v"(W0[0]));
    asm volatile("ds_read_b128 %0, %1 offset:6144" : "=v"(X1[3]) : "v"(xa1) : "memory");
    asm volatile("v_mfma_f32_16x16x32_bf16 %0, %1, %2, %0" : "+v"(acc[0][1]) : "v"(X0[0]), "v"(W0[1]));
    asm volatile("ds_read_b128 %0, %1 offset:0" : "=v"(W1[0]) : "v"(wa1) : "memory");
    asm volatile("s_addk_i32 m0, 0x1000" ::: "scc", "memory");
    asm volatile("v_mfma_f32_16x16x32_bf16 %0, %1, %2, %0" : "+v"(acc[1][1]) : "v"(X0[1]), "v"(W0[1]));
    asm volatile("ds_read_b128 %0, %1 offset:2048" : "=v"(W1[1]) : "v"(wa1) : "memory");
    asm volatile("buffer_load_dwordx4 %0, %1, %2 offen lds" :: "v"(wv[2]), "s"(rW), "s"(kw) : "memory");
    asm volatile("v_mfma_f32_16x16x32_bf16 %0, %1, %2, %0" : "+v"(acc[2][1]) : "v"(X0[2]), "v"(W0[1]));
    asm volatile("ds_read_b128 %0, %1 offset:4096" : "=v"(W1[2]) : "v"(wa1) : "memory");
    asm volatile("v_mfma_f32_16x16x32_bf16 %0, %1, %2, %0" : "+v"(acc[3][1]) : "v"(X0[3]), "v"(W0[1]));
    asm volatile("ds_read_b128 %0, %1 offset:6144" : "=v"(W1[3]) : "v"(wa1) : "memory");
    asm volatile("v_mfma_f32_16x16x32_bf16 %0, %1, %2, %0" : "+v"(acc[0][2]) : "v"(X0[0]), "v"(W0[2]));
    asm volatile("ds_read_b128 %0, %1 offset:8192" : "=v"(W1[4]) : "v"(wa1) : "memory");
    asm volatile("s_addk_i32 m0, 0x1000" ::: "scc", "memory");
    asm volatile("v_mfma_f32_16x16x32_bf16 %0, %1, %2, %0" : "+v"(acc[1][2]) : "v"(X0[1]), "v"(W0[2]));
    asm volatile("buffer_load_dwordx4 %0, %1, %2 offen lds" :: "v"(wv[3]), "s"(rW), "s"(kw) : "memory");
    asm volatile("v_mfma_f32_16x16x32_bf16 %0, %1, %2, %0" : "+v"(acc[2][2]) : "v"(X0[2]), "v"(W0[2]));
    asm volatile("v_mfma_f32_16x16x32_bf16 %0, %1, %2, %0" : "+v"(acc[3][2]) : "v"(X0[3]), "v"(W0[2]));
    asm volatile("v_mfma_f32_16x16x32_bf16 %0, %1, %2, %0" : "+v"(acc[0][3]) : "v"(X0[0]), "v"(W0[3]));
    asm volatile("s_addk_i32 m0, 0x1000" ::: "scc", "memory");
    asm volatile("v_mfma_f32_16x16x32_bf16 %0, %1, %2, %0" : "+v"(acc[1][3]) : "v"(X0[1]), "v"(W0[3]));
    asm volatile("buffer_load_dwordx4 %0, %1, %2 offen lds" :: "v"(wv[4]), "s"(rW), "s"(kw) : "memory");
    asm volatile("s_add_u32 %0, %0, 128\n\ts_cmp_eq_u32 %0, 1280\n\ts_cselect_b32 %0, 0, %0" : "+s"(kw) :: "scc");
    asm volatile("v_mfma_f32_16x16x32_bf16 %0, %1, %2, %0" : "+v"(acc[2][3]) : "v"(X0[2]), "v"(W0[3]));
    asm volatile("v_mfma_f32_16x16x32_bf16 %0, %1, %2, %0" : "+v"(acc[3][3]) : "v"(X0[3]), "v"(W0[3]));
    asm volatile("v_mfma_f32_16x16x32_bf16 %0, %1, %2, %0" : "+v"(acc[0][4]) : "v"(X0[0]), "v"(W0[4]));
    asm volatile("v_mfma_f32_16x16x32_bf16 %0, %1, %2, %0" : "+v"(acc[1][4]) : "v"(X0[1]), "v"(W0[4]));
    asm volatile("v_mfma_f32_16x16x32_bf16 %0, %1, %2, %0" : "+v"(acc[2][4]) : "v"(X0[2]), "v"(W0[4]));
    asm volatile("v_mfma_f32_16x16x32_bf16 %0, %1, %2, %0" : "+v"(acc[3][4]) : "v"(X0[3]), "v"(W0[4]));
    asm volatile("s_waitcnt vmcnt(9) lgkmcnt(0)\n\ts_barrier" ::: "memory");
    asm volatile("v_mfma_f32_16x16x32_bf16 %0, %1, %2, %0" : "+v"(acc[0][0]) : "v"(X1[0]), "v"(W1[0]));
    asm volatile("ds_read_b128 %0, %1 offset:32768" : "=v"(X0[0]) : "v"(xa0) : "memory");
    asm volatile("s_add_u32 m0, %0, 12288" :: "s"(lw) : "scc", "memory");
    asm volatile("v_mfma_f32_16x16x32_bf16 %0, %1, %2, %0" : "+v"(acc[1][0]) : "v"(X1[1]), "v"(W1[0]));
    asm volatile("ds_read_b128 %0, %1 offset:34816" : "=v"(X0[1]) : "v"(xa0) : "memory");
    asm volatile("buffer_load_dwordx4 %0, %1, %2 offen lds" :: "v"(xv[0]), "s"(rX), "s"(kx) : "memory");
    asm volatile("v_mfma_f32_16x16x32_bf16 %0, %1, %2, %0" : "+v"(acc[2][0]) : "v"(X1[2]), "v"(W1[0]));
    asm volatile("ds_read_b128 %0, %1 offset:36864" : "=v"(X0[2]) : "v"(xa0) : "memory");
    asm volatile("v_mfma_f32_16x16x32_bf16 %0, %1, %2, %0" : "+v"(acc[3][0]) : "v"(X1[3]), "v"(W1[0]));
    asm volatile("ds_read_b128 %0, %1 offset:38912" : "=v"(X0[3]) : "v"(xa0) : "memory");
    asm volatile("v_mfma_f32_16x16x32_bf16 %0, %1, %2, %0" : "+v"(acc[0][1]) : "v"(X1[0]), "v"(W1[1]));
    asm volatile("ds_read_b128 %0, %1 offset:40960" : "=v"(W0[0]) : "v"(wa0) : "memory");
    asm volatile("s_addk_i32 m0, 0x1000" ::: "scc", "memory");
    asm volatile("v_mfma_f32_16x16x32_bf16 %0, %1, %2, %0" : "+v"(acc[1][1]) : "v"(X1[1]), "v"(W1[1]));
    asm volatile("ds_read_b128 %0, %1 offset:43008" : "=v"(W0[1]) : "v"(wa0) : "memory");
    asm volatile("buffer_load_dwordx4 %0, %1, %2 offen lds" :: "v"(xv[1]), "s"(rX), "s"(kx) : "memory");
    asm volatile("v_mfma_f32_16x16x32_bf16 %0, %1, %2, %0" : "+v"(acc[2][1]) : "v"(X1[2]), "v"(W1[1]));
    asm volatile("ds_read_b128 %0, %1 offset:45056" : "=v"(W0[2]) : "v"(wa0) : "memory");
    asm volatile("v_mfma_f32_16x16x32_bf16 %0, %1, %2, %0" : "+v"(acc[3][1]) : "v"(X1[3]), "v"(W1[1]));
    asm volatile("ds_read_b128 %0, %1 offset:47104" : "=v"(W0[3]) : "v"(wa0) : "memory");
    asm volatile("v_mfma_f32_16x16x32_bf16 %0, %1, %2, %0" : "+v"(acc[0][2]) : "v"(X1[0]), "v"(W1[2]));
    asm volatile("ds_read_b128 %0, %1 offset:49152" : "=v"(W0[4]) : "v"(wa0) : "memory");
    asm volatile("s_addk_i32 m0, 0x1000" ::: "scc", "memory");
    asm volatile("v_mfma_f32_16x16x32_bf16 %0, %1, %2, %0" : "+v"(acc[1][2]) : "v"(X1[1]), "v"(W1[2]));
    asm volatile("buffer_load_dwordx4 %0, %1, %2 offen lds" :: "v"(xv[2]), "s"(rX), "s"(kx) : "memory");
    asm volatile("v_mfma_f32_16x16x32_bf16 %0, %1, %2, %0" : "+v"(acc[2][2]) : "v"(X1[2]), "v"(W1[2]));
    asm volatile("v_mfma_f32_16x16x32_bf16 %0, %1, %2, %0" : "+v"(acc[3][2]) : "v"(X1[3]), "v"(W1[2]));
    asm volatile("v_mfma_f32_16x16x32_bf16 %0, %1, %2, %0" : "+v"(acc[0][3]) : "v"(X1[0]), "v"(W1[3]));
    asm volatile("s_addk_i32 m0, 0x1000" ::: "scc", "memory");
    asm volatile("v_mfma_f32_16x16x32_bf16 %0, %1, %2, %0" : "+v"(acc[1][3]) : "v"(X1[1]), "v"(W1[3]));
    asm volatile("buffer_load_dwordx4 %0, %1, %2 offen lds" :: "v"(xv[3]), "s"(rX), "s"(kx) : "memory");
    asm volatile("s_add_u32 %0, %0, 128\n\ts_cmp_eq_u32 %0, 1280\n\ts_cselect_b32 %0, 0, %0" : "+s"(kx) :: "scc");
    asm volatile("v_mfma_f32_16x16x32_bf16 %0, %1, %2, %0" : "+v"(acc[2][3]) : "v"(X1[2]), "v"(W1[3]));
    asm volatile("v_mfma_f32_16x16x32_bf16 %0, %1, %2, %0" : "+v"(acc[3][3]) : "v"(X1[3]), "v"(W1[3]));
    asm volatile("v_mfma_f32_16x16x32_bf16 %0, %1, %2, %0" : "+v"(acc[0][4]) : "v"(X1[0]), "v"(W1[4]));
    asm volatile("s_add_u32 m0, %0, 61440" :: "s"(lw) : "scc", "memory");
    asm volatile("v_mfma_f32_16x16x32_bf16 %0, %1, %2, %0" : "+v"(acc[1][4]) : "v"(X1[1]), "v"(W1[4]));
    asm volatile("buffer_load_dwordx4 %0, %1, %2 offen lds" :: "v"(wv[0]), "s"(rW), "s"(kw) : "memory");
    asm volatile("v_mfma_f32_16x16x32_bf16 %0, %1, %2, %0" : "+v"(acc[2][4]) : "v"(X1[2]), "v"(W1[4]));
    asm volatile("v_mfma_f32_16x16x32_bf16 %0, %1, %2, %0" : "+v"(acc[3][4]) : "v"(X1[3]), "v"(W1[4]));
    asm volatile("s_waitcnt lgkmcnt(0)" ::: "memory");
    asm volatile("v_mfma_f32_16x16x32_bf16 %0, %1, %2, %0" : "+v"(acc[0][0]) : "v"(X0[0]), "v"(W0[0]));
    asm volatile("ds_read_b128 %0, %1 offset:32768" : "=v"(X1[0]) : "v"(xa1) : "memory");
    asm volatile("s_addk_i32 m0, 0x1000" ::: "scc", "memory");
    asm volatile("v_mfma_f32_16x16x32_bf16 %0, %1, %2, %0" : "+v"(acc[1][0]) : "v"(X0[1]), "v"(W0[0]));
    asm volatile("ds_read_b128 %0, %1 offset:34816" : "=v"(X1[1]) : "v"(xa1) : "memory");
    asm volatile("buffer_load_dwordx4 %0, %1, %2 offen lds" :: "v"(wv[1]), "s"(rW), "s"(kw) : "memory");
    asm volatile("v_mfma_f32_16x16x32_bf16 %0, %1, %2, %0" : "+v"(acc[2][0]) : "v"(X0[2]), "v"(W0[0]));
    asm volatile("ds_read_b128 %0, %1 offset:36864" : "=v"(X1[2]) : "v"(xa1) : "memory");
    asm volatile("v_mfma_f32_16x16x32_bf16 %0, %1, %2, %0" : "+v"(acc[3][0]) : "v"(X0[3]), "v"(W0[0]));
    asm volatile("ds_read_b128 %0, %1 offset:38912" : "=v"(X1[3]) : "v"(xa1) : "memory");
    asm volatile("v_mfma_f32_16x16x32_bf16 %0, %1, %2, %0" : "+v"(acc[0][1]) : "v"(X0[0]), "v"(W0[1]));
    asm volatile("ds_read_b128 %0, %1 offset:40960" : "=v"(W1[0]) : "v"(wa1) : "memory");
    asm volatile("s_addk_i32 m0, 0x1000" ::: "scc", "memory");
    asm volatile("v_mfma_f32_16x16x32_bf16 %0, %1, %2, %0" : "+v"(acc[1][1]) : "v"(X0[1]), "v"(W0[1]));
    asm volatile("ds_read_b128 %0, %1 offset:43008" : "=v"(W1[1]) : "v"(wa1) : "memory");
    asm volatile("buffer_load_dwordx4 %0, %1, %2 offen lds" :: "v"(wv[2]), "s"(rW), "s"(kw) : "memory");
    asm volatile("v_mfma_f32_16x16x32_bf16 %0, %1, %2, %0" : "+v"(acc[2][1]) : "v"(X0[2]), "v"(W0[1]));
    asm volatile("ds_read_b128 %0, %1 offset:45056" : "=v"(W1[2]) : "v"(wa1) : "memory");
    asm volatile("v_mfma_f32_16x16x32_bf16 %0, %1, %2, %0" : "+v"(acc[3][1]) : "v"(X0[3]), "v"(W0[1]));
    asm volatile("ds_read_b128 %0, %1 offset:47104" : "=v"(W1[3]) : "v"(wa1) : "memory");
    asm volatile("v_mfma_f32_16x16x32_bf16 %0, %1, %2, %0" : "+v"(acc[0][2]) : "v"(X0[0]), "v"(W0[2]));
    asm volatile("ds_read_b128 %0, %1 offset:49152" : "=v"(W1[4]) : "v"(wa1) : "memory");
    asm volatile("s_addk_i32 m0, 0x1000" ::: "scc", "memory");
    asm volatile("v_mfma_f32_16x16x32_bf16 %0, %1, %2, %0" : "+v"(acc[1][2]) : "v"(X0[1]), "v"(W0[2]));
    asm volatile("buffer_load_dwordx4 %0, %1, %2 offen lds" :: "v"(wv[3]), "s"(rW), "s"(kw) : "memory");
    asm volatile("v_mfma_f32_16x16x32_bf16 %0, %1, %2, %0" : "+v"(acc[2][2]) : "v"(X0[2]), "v"(W0[2]));
    asm volatile("v_mfma_f32_16x16x32_bf16 %0, %1, %2, %0" : "+v"(acc[3][2]) : "v"(X0[3]), "v"(W0[2]));
    asm volatile("v_mfma_f32_16x16x32_bf16 %0, %1, %2, %0" : "+v"(acc[0][3]) : "v"(X0[0]), "v"(W0[3]));
    asm volatile("s_addk_i32 m0, 0x1000" ::: "scc", "memory");
    asm volatile("v_mfma_f32_16x16x32_bf16 %0, %1, %2, %0" : "+v"(acc[1][3]) : "v"(X0[1]), "v"(W0[3]));
    asm volatile("buffer_load_dwordx4 %0, %1, %2 offen lds" :: "v"(wv[4]), "s"(rW), "s"(kw) : "memory");
    asm volatile("s_add_u32 %0, %0, 128\n\ts_cmp_eq_u32 %0, 1280\n\ts_cselect_b32 %0, 0, %0" : "+s"(kw) :: "scc");
    asm volatile("v_mfma_f32_16x16x32_bf16 %0, %1, %2, %0" : "+v"(acc[2][3]) : "v"(X0[2]), "v"(W0[3]));
    asm volatile("v_mfma_f32_16x16x32_bf16 %0, %1, %2, %0" : "+v"(acc[3][3]) : "v"(X0[3]), "v"(W0[3]));
    asm volatile("v_mfma_f32_16x16x32_bf16 %0, %1, %2, %0" : "+v"(acc[0][4]) : "v"(X0[0]), "v"(W0[4]));
    asm volatile("v_mfma_f32_16x16x32_bf16 %0, %1, %2, %0" : "+v"(acc[1][4]) : "v"(X0[1]), "v"(W0[4]));
    asm volatile("v_mfma_f32_16x16x32_bf16 %0, %1, %2, %0" : "+v"(acc[2][4]) : "v"(X0[2]), "v"(W0[4]));
    asm volatile("v_mfma_f32_16x16x32_bf16 %0, %1, %2, %0" : "+v"(acc[3][4]) : "v"(X0[3]), "v"(W0[4]));
    asm volatile("s_waitcnt vmcnt(9) lgkmcnt(0)\n\ts_barrier" ::: "memory");
    asm volatile("v_mfma_f32_16x16x32_bf16 %0, %1, %2, %0" : "+v"(acc[0][0]) : "v"(X1[0]), "v"(W1[0]));
    asm volatile("ds_read_b128 %0, %1 offset:16384" : "=v"(X0[0]) : "v"(xa0) : "memory");
    asm volatile("v_mfma_f32_16x16x32_bf16 %0, %1, %2, %0" : "+v"(acc[1][0]) : "v"(X1[1]), "v"(W1[0]));
    asm volatile("ds_read_b128 %0, %1 offset:18432" : "=v"(X0[1]) : "v"(xa0) : "memory");
    asm volatile("v_mfma_f32_16x16x32_bf16 %0, %1, %2, %0" : "+v"(acc[2][0]) : "v"(X1[2]), "v"(W1[0]));
    asm volatile("ds_read_b128 %0, %1 offset:20480" : "=v"(X0[2]) : "v"(xa0) : "memory");
    asm volatile("v_mfma_f32_16x16x32_bf16 %0, %1, %2, %0" : "+v"(acc[3][0]) : "v"(X1[3]), "v"(W1[0]));
    asm volatile("ds_read_b128 %0, %1 offset:22528" : "=v"(X0[3]) : "v"(xa0) : "memory");
    asm volatile("v_mfma_f32_16x16x32_bf16 %0, %1, %2, %0" : "+v"(acc[0][1]) : "v"(X1[0]), "v"(W1[1]));
    asm volatile("ds_read_b128 %0, %1 offset:20480" : "=v"(W0[0]) : "v"(wa0) : "memory");
    asm volatile("v_mfma_f32_16x16x32_bf16 %0, %1, %2, %0" : "+v"(acc[1][1]) : "v"(X1[1]), "v"(W1[1]));
    asm volatile("ds_read_b128 %0, %1 offset:22528" : "=v"(W0[1]) : "v"(wa0) : "memory");
    asm volatile("v_mfma_f32_16x16x32_bf16 %0, %1, %2, %0" : "+v"(acc[2][1]) : "v"(X1[2]), "v"(W1[1]));
    asm volatile("ds_read_b128 %0, %1 offset:24576" : "=v"(W0[2]) : "v"(wa0) : "memory");
    asm volatile("v_mfma_f32_16x16x32_bf16 %0, %1, %2, %0" : "+v"(acc[3][1]) : "v"(X1[3]), "v"(W1[1]));
    asm volatile("ds_read_b128 %0, %1 offset:26624" : "=v"(W0[3]) : "v"(wa0) : "memory");
    asm volatile("v_mfma_f32_16x16x32_bf16 %0, %1, %2, %0" : "+v"(acc[0][2]) : "v"(X1[0]), "v"(W1[2]));
    asm volatile("ds_read_b128 %0, %1 offset:28672" : "=v"(W0[4]) : "v"(wa0) : "memory");
    asm volatile("v_mfma_f32_16x16x32_bf16 %0, %1, %2, %0" : "+v"(acc[1][2]) : "v"(X1[1]), "v"(W1[2]));
    float ta, tb, tc, td, tx, ty; const unsigned tl_ = lds0 + (tid < 79 ? tid : 79)*8; const float k_l2e = 1.4426950408889634f, k_i2p = 0.15915494309189535f;
    asm volatile("v_accvgpr_read_b32 %0, %1" : "=v"(tc) : "a"(t_dt));
    asm volatile("v_mfma_f32_16x16x32_bf16 %0, %1, %2, %0" : "+v"(acc[2][2]) : "v"(X1[2]), "v"(W1[2]));
    asm volatile("v_accvgpr_read_b32 %0, %1" : "=v"(ta) : "a"(t_re));
    asm volatile("v_accvgpr_read_b32 %0, %1" : "=v"(tb) : "a"(t_im));
    asm volatile("buffer_load_dword %0, %1, %2, 0 offen sc1" : "=v"(fl) : "v"(3932160 + tid*64), "s"(rS) : "memory");
    asm volatile("v_mfma_f32_16x16x32_bf16 %0, %1, %2, %0" : "+v"(acc[3][2]) : "v"(X1[3]), "v"(W1[2]));
    asm volatile("v_lshlrev_b32 %0, 16, %0" : "+v"(tc) : );
    asm volatile("v_lshlrev_b32 %0, 16, %0" : "+v"(ta) : );
    asm volatile("v_mfma_f32_16x16x32_bf16 %0, %1, %2, %0" : "+v"(acc[0][3]) : "v"(X1[0]), "v"(W1[3]));
    asm volatile("v_lshlrev_b32 %0, 16, %0" : "+v"(tb) : );
    asm volatile("v_mul_f32 %0, %1, %0" : "+v"(tc) : "s"(k_l2e));
    asm volatile("v_mfma_f32_16x16x32_bf16 %0, %1, %2, %0" : "+v"(acc[1][3]) : "v"(X1[1]), "v"(W1[3]));
    asm volatile("v_mul_f32 %0, %1, %0" : "+v"(ta) : "s"(k_l2e));
    asm volatile("v_exp_f32 %0, %0" : "+v"(tc) : );
    asm volatile("v_mfma_f32_16x16x32_bf16 %0, %1, %2, %0" : "+v"(acc[2][3]) : "v"(X1[2]), "v"(W1[3]));
    asm volatile("v_mul_f32 %0, %1, %0" : "+v"(tb) : "s"(k_i2p));
    asm volatile("v_cvt_pk_bf16_f32 %0, %0, %0" : "+v"(tc) : );
    asm volatile("v_mfma_f32_16x16x32_bf16 %0, %1, %2, %0" : "+v"(acc[3][3]) : "v"(X1[3]), "v"(W1[3]));
    asm volatile("v_lshlrev_b32 %0, 16, %0" : "+v"(tc) : );
    asm volatile("v_mul_f32 %0, %1, %0" : "+v"(ta) : "v"(tc));
    asm volatile("v_mfma_f32_16x16x32_bf16 %0, %1, %2, %0" : "+v"(acc[0][4]) : "v"(X1[0]), "v"(W1[4]));
    asm volatile("v_mul_f32 %0, %1, %0" : "+v"(tb) : "v"(tc));
    asm volatile("v_exp_f32 %0, %0" : "+v"(ta) : );
    asm volatile("v_mfma_f32_16x16x32_bf16 %0, %1, %2, %0" : "+v"(acc[1][4]) : "v"(X1[1]), "v"(W1[4]));
    asm volatile("v_rndne_f32 %0, %1" : "=v"(td) : "v"(tb));
    asm volatile("v_sub_f32 %0, %0, %1" : "+v"(tb) : "v"(td));
    asm volatile("v_mfma_f32_16x16x32_bf16 %0, %1, %2, %0" : "+v"(acc[2][4]) : "v"(X1[2]), "v"(W1[4]));
    asm volatile("v_cos_f32 %0, %1" : "=v"(tx) : "v"(tb));
    asm volatile("v_sin_f32 %0, %1" : "=v"(ty) : "v"(tb));
    asm volatile("v_mfma_f32_16x16x32_bf16 %0, %1, %2, %0" : "+v"(acc[3][4]) : "v"(X1[3]), "v"(W1[4]));
    asm volatile("v_mul_f32 %0, %1, %0" : "+v"(tx) : "v"(ta));
    asm volatile("v_mul_f32 %0, %1, %0" : "+v"(ty) : "v"(ta));
    asm volatile("s_waitcnt lgkmcnt(0)" ::: "memory");
    asm volatile("v_mfma_f32_16x16x32_bf16 %0, %1, %2, %0" : "+v"(acc[0][0]) : "v"(X0[0]), "v"(W0[0]));
    asm volatile("ds_read_b128 %0, %1 offset:16384" : "=v"(X1[0]) : "v"(xa1) : "memory");
    asm volatile("v_mfma_f32_16x16x32_bf16 %0, %1, %2, %0" : "+v"(acc[1][0]) : "v"(X0[1]), "v"(W0[0]));
    asm volatile("ds_read_b128 %0, %1 offset:18432" : "=v"(X1[1]) : "v"(xa1) : "memory");
    asm volatile("v_mfma_f32_16x16x32_bf16 %0, %1, %2, %0" : "+v"(acc[2][0]) : "v"(X0[2]), "v"(W0[0]));
    asm volatile("ds_read_b128 %0, %1 offset:20480" : "=v"(X1[2]) : "v"(xa1) : "memory");
    asm volatile("v_mfma_f32_16x16x32_bf16 %0, %1, %2, %0" : "+v"(acc[3][0]) : "v"(X0[3]), "v"(W0[0]));
    asm volatile("ds_read_b128 %0, %1 offset:22528" : "=v"(X1[3]) : "v"(xa1) : "memory");
    asm volatile("v_mfma_f32_16x16x32_bf16 %0, %1, %2, %0" : "+v"(acc[0][1]) : "v"(X0[0]), "v"(W0[1]));
    asm volatile("ds_read_b128 %0, %1 offset:20480" : "=v"(W1[0]) : "v"(wa1) : "memory");
    asm volatile("v_mfma_f32_16x16x32_bf16 %0, %1, %2, %0" : "+v"(acc[1][1]) : "v"(X0[1]), "v"(W0[1]));
    asm volatile("ds_read_b128 %0, %1 offset:22528" : "=v"(W1[1]) : "v"(wa1) : "memory");
    asm volatile("v_mfma_f32_16x16x32_bf16 %0, %1, %2, %0" : "+v"(acc[2][1]) : "v"(X0[2]), "v"(W0[1]));
    asm volatile("ds_read_b128 %0, %1 offset:24576" : "=v"(W1[2]) : "v"(wa1) : "memory");
    asm volatile("v_mfma_f32_16x16x32_bf16 %0, %1, %2, %0" : "+v"(acc[3][1]) : "v"(X0[3]), "v"(W0[1]));
    asm volatile("ds_read_b128 %0, %1 offset:26624" : "=v"(W1[3]) : "v"(wa1) : "memory");
    asm volatile("v_mfma_f32_16x16x32_bf16 %0, %1, %2, %0" : "+v"(acc[0][2]) : "v"(X0[0]), "v"(W0[2]));
    asm volatile("ds_read_b128 %0, %1 offset:28672" : "=v"(W1[4]) : "v"(wa1) : "memory");
    asm volatile("v_mfma_f32_16x16x32_bf16 %0, %1, %2, %0" : "+v"(acc[1][2]) : "v"(X0[1]), "v"(W0[2]));
    asm volatile("ds_write2_b32 %0, %1, %2 offset0:0 offset1:1" :: "v"(tl_ + 2560), "v"(tx), "v"(ty) : "memory");
    asm volatile("v_mul_f32 %0, %1, %1" : "=v"(tc) : "v"(ty));
    asm volatile("v_mfma_f32_16x16x32_bf16 %0, %1, %2, %0" : "+v"(acc[2][2]) : "v"(X0[2]), "v"(W0[2]));
    asm volatile("v_add_f32 %0, %1, %1" : "=v"(td) : "v"(tx));
    asm volatile("v_fma_f32 %0, %0, %0, -%1" : "+v"(tx) : "v"(tc));
    asm volatile("v_mfma_f32_16x16x32_bf16 %0, %1, %2, %0" : "+v"(acc[3][2]) : "v"(X0[3]), "v"(W0[2]));
    asm volatile("v_mul_f32 %0, %1, %0" : "+v"(ty) : "v"(td));
    asm volatile("v_mul_f32 %0, %1, %1" : "=v"(tc) : "v"(ty));
    asm volatile("v_mfma_f32_16x16x32_bf16 %0, %1, %2, %0" : "+v"(acc[0][3]) : "v"(X0[0]), "v"(W0[3]));
    asm volatile("v_add_f32 %0, %1, %1" : "=v"(td) : "v"(tx));
    asm volatile("v_fma_f32 %0, %0, %0, -%1" : "+v"(tx) : "v"(tc));
    asm volatile("v_mfma_f32_16x16x32_bf16 %0, %1, %2, %0" : "+v"(acc[1][3]) : "v"(X0[1]), "v"(W0[3]));
    asm volatile("v_mul_f32 %0, %1, %0" : "+v"(ty) : "v"(td));
    asm volatile("v_mul_f32 %0, %1, %1" : "=v"(tc) : "v"(ty));
    asm volatile("v_mfma_f32_16x16x32_bf16 %0, %1, %2, %0" : "+v"(acc[2][3]) : "v"(X0[2]), "v"(W0[3]));
    asm volatile("v_add_f32 %0, %1, %1" : "=v"(td) : "v"(tx));
    asm volatile("v_fma_f32 %0, %0, %0, -%1" : "+v"(tx) : "v"(tc));
    asm volatile("v_mfma_f32_16x16x32_bf16 %0, %1, %2, %0" : "+v"(acc[3][3]) : "v"(X0[3]), "v"(W0[3]));
    asm volatile("v_mul_f32 %0, %1, %0" : "+v"(ty) : "v"(td));
    asm volatile("v_mul_f32 %0, %1, %1" : "=v"(tc) : "v"(ty));
    asm volatile("v_mfma_f32_16x16x32_bf16 %0, %1, %2, %0" : "+v"(acc[0][4]) : "v"(X0[0]), "v"(W0[4]));
    asm volatile("v_add_f32 %0, %1, %1" : "=v"(td) : "v"(tx));
    asm volatile("v_fma_f32 %0, %0, %0, -%1" : "+v"(tx) : "v"(tc));
    asm volatile("v_mfma_f32_16x16x32_bf16 %0, %1, %2, %0" : "+v"(acc[1][4]) : "v"(X0[1]), "v"(W0[4]));
    asm volatile("v_mul_f32 %0, %1, %0" : "+v"(ty) : "v"(td));
    asm volatile("ds_write2_b32 %0, %1, %2 offset0:0 offset1:1" :: "v"(tl_ + 3200), "v"(tx), "v"(ty) : "memory");
    asm volatile("v_mfma_f32_16x16x32_bf16 %0, %1, %2, %0" : "+v"(acc[2][4]) : "v"(X0[2]), "v"(W0[4]));
    asm volatile("v_mul_f32 %0, %1, %1" : "=v"(tc) : "v"(ty));
    asm volatile("v_add_f32 %0, %1, %1" : "=v"(td) : "v"(tx));
    asm volatile("v_mfma_f32_16x16x32_bf16 %0, %1, %2, %0" : "+v"(acc[3][4]) : "v"(X0[3]), "v"(W0[4]));
    asm volatile("v_fma_f32 %0, %0, %0, -%1" : "+v"(tx) : "v"(tc));
    asm volatile("v_mul_f32 %0, %1, %0" : "+v"(ty) : "v"(td));
    asm volatile("v_mul_f32 %0, %1, %1" : "=v"(tc) : "v"(ty));
    asm volatile("v_add_f32 %0, %1, %1" : "=v"(td) : "v"(tx));
    asm volatile("v_fma_f32 %0, %0, %0, -%1" : "+v"(tx) : "v"(tc));
    asm volatile("v_mul_f32 %0, %1, %0" : "+v"(ty) : "v"(td));
    asm volatile("v_mul_f32 %0, %1, %1" : "=v"(tc) : "v"(ty));
    asm volatile("v_add_f32 %0, %1, %1" : "=v"(td) : "v"(tx));
    asm volatile("v_fma_f32 %0, %0, %0, -%1" : "+v"(tx) : "v"(tc));
    asm volatile("v_mul_f32 %0, %1, %0" : "+v"(ty) : "v"(td));
    asm volatile("ds_write2_b32 %0, %1, %2 offset0:0 offset1:1" :: "v"(tl_ + 3840), "v"(tx), "v"(ty) : "memory");
    asm volatile("s_waitcnt vmcnt(0)" ::: "memory");
    asm volatile("" : "+v"(fl));
    if (!__all(fl == tag)) spin1(rS, 3932160 + tid*64, false, tag);
    asm volatile("s_waitcnt lgkmcnt(0)\n\ts_barrier" ::: "memory");
    if (b == 0 && w == 0) __builtin_amdgcn_raw_buffer_store_b32(tag, rS, lane == 0 ? 0 : OOB, 0, SC1);
    asm volatile("buffer_load_dwordx2 %0, %1, %2, 0 offen sc1" : "=v"(st) : "v"(40960 + (128*c + (tid & 127))*8), "s"(rS) : "memory");
    asm volatile("buffer_load_dwordx4 %0, %1, %2, 0 offen offset:0 sc1" : "=v"(c4[0]) : "v"(tid < 160 ? 4096000 + (0 + j)*6144 + tid*32 : OOB), "s"(rS) : "memory");
    asm volatile("buffer_load_dwordx4 %0, %1, %2, 0 offen offset:16 sc1" : "=v"(c4[1]) : "v"(tid < 160 ? 4096000 + (0 + j)*6144 + tid*32 : OOB), "s"(rS) : "memory");
    __builtin_amdgcn_sched_barrier(0);
    asm volatile("v_mfma_f32_16x16x32_bf16 %0, %1, %2, %0" : "+v"(acc[0][0]) : "v"(X1[0]), "v"(W1[0]));
    asm volatile("ds_read_b128 %0, %1 offset:0" : "=v"(X0[0]) : "v"(xa0) : "memory");
    sw = swb + (kso + 0 + wm*(1u))*40960;
    asm volatile("" : "+s"(sw));
    asm volatile("s_add_u32 m0, %0, 153600" :: "s"(lw2) : "scc", "memory");
    asm volatile("v_mfma_f32_16x16x32_bf16 %0, %1, %2, %0" : "+v"(acc[1][0]) : "v"(X1[1]), "v"(W1[0]));
    asm volatile("ds_read_b128 %0, %1 offset:2048" : "=v"(X0[1]) : "v"(xa0) : "memory");
    asm volatile("buffer_load_dwordx4 %0, %1, %2 offen offset:0 sc1 lds" :: "v"(vl16), "s"(rS), "s"(sw) : "memory");
    asm volatile("v_mfma_f32_16x16x32_bf16 %0, %1, %2, %0" : "+v"(acc[2][0]) : "v"(X1[2]), "v"(W1[0]));
    asm volatile("ds_read_b128 %0, %1 offset:4096" : "=v"(X0[2]) : "v"(xa0) : "memory");
    asm volatile("v_mfma_f32_16x16x32_bf16 %0, %1, %2, %0" : "+v"(acc[3][0]) : "v"(X1[3]), "v"(W1[0]));
    asm volatile("ds_read_b128 %0, %1 offset:6144" : "=v"(X0[3]) : "v"(xa0) : "memory");
    asm volatile("v_mfma_f32_16x16x32_bf16 %0, %1, %2, %0" : "+v"(acc[0][1]) : "v"(X1[0]), "v"(W1[1]));
    asm volatile("ds_read_b128 %0, %1 offset:0" : "=v"(W0[0]) : "v"(wa0) : "memory");
    asm volatile("v_mfma_f32_16x16x32_bf16 %0, %1, %2, %0" : "+v"(acc[1][1]) : "v"(X1[1]), "v"(W1[1]));
    asm volatile("ds_read_b128 %0, %1 offset:2048" : "=v"(W0[1]) : "v"(wa0) : "memory");
    asm volatile("buffer_load_dwordx4 %0, %1, %2 offen offset:1024 sc1 lds" :: "v"(vl16), "s"(rS), "s"(sw) : "memory");
    asm volatile("v_mfma_f32_16x16x32_bf16 %0, %1, %2, %0" : "+v"(acc[2][1]) : "v"(X1[2]), "v"(W1[1]));
    asm volatile("ds_read_b128 %0, %1 offset:4096" : "=v"(W0[2]) : "v"(wa0) : "memory");
    asm volatile("v_mfma_f32_16x16x32_bf16 %0, %1, %2, %0" : "+v"(acc[3][1]) : "v"(X1[3]), "v"(W1[1]));
    asm volatile("ds_read_b128 %0, %1 offset:6144" : "=v"(W0[3]) : "v"(wa0) : "memory");
    asm volatile("v_mfma_f32_16x16x32_bf16 %0, %1, %2, %0" : "+v"(acc[0][2]) : "v"(X1[0]), "v"(W1[2]));
    asm volatile("ds_read_b128 %0, %1 offset:8192" : "=v"(W0[4]) : "v"(wa0) : "memory");
    asm volatile("v_mfma_f32_16x16x32_bf16 %0, %1, %2, %0" : "+v"(acc[1][2]) : "v"(X1[1]), "v"(W1[2]));
    asm volatile("v_mfma_f32_16x16x32_bf16 %0, %1, %2, %0" : "+v"(acc[2][2]) : "v"(X1[2]), "v"(W1[2]));
    asm volatile("buffer_load_dwordx4 %0, %1, %2 offen offset:2048 sc1 lds" :: "v"(vl16), "s"(rS), "s"(sw) : "memory");
    asm volatile("v_mfma_f32_16x16x32_bf16 %0, %1, %2, %0" : "+v"(acc[3][2]) : "v"(X1[3]), "v"(W1[2]));
    asm volatile("v_mfma_f32_16x16x32_bf16 %0, %1, %2, %0" : "+v"(acc[0][3]) : "v"(X1[0]), "v"(W1[3]));
    asm volatile("v_mfma_f32_16x16x32_bf16 %0, %1, %2, %0" : "+v"(acc[1][3]) : "v"(X1[1]), "v"(W1[3]));
    asm volatile("v_mfma_f32_16x16x32_bf16 %0, %1, %2, %0" : "+v"(acc[2][3]) : "v"(X1[2]), "v"(W1[3]));
    asm volatile("buffer_load_dwordx4 %0, %1, %2 offen offset:3072 sc1 lds" :: "v"(vl16), "s"(rS), "s"(sw) : "memory");
    asm volatile("v_mfma_f32_16x16x32_bf16 %0, %1, %2, %0" : "+v"(acc[3][3]) : "v"(X1[3]), "v"(W1[3]));
    asm volatile("v_mfma_f32_16x16x32_bf16 %0, %1, %2, %0" : "+v"(acc[0][4]) : "v"(X1[0]), "v"(W1[4]));
    asm volatile("v_mfma_f32_16x16x32_bf16 %0, %1, %2, %0" : "+v"(acc[1][4]) : "v"(X1[1]), "v"(W1[4]));
    asm volatile("v_mfma_f32_16x16x32_bf16 %0, %1, %2, %0" : "+v"(acc[2][4]) : "v"(X1[2]), "v"(W1[4]));
    asm volatile("s_addk_i32 m0, 0x1000" ::: "scc", "memory");
    asm volatile("v_mfma_f32_16x16x32_bf16 %0, %1, %2, %0" : "+v"(acc[3][4]) : "v"(X1[3]), "v"(W1[4]));
    asm volatile("buffer_load_dwordx4 %0, %1, %2 offen offset:0 sc1 lds" :: "v"(vl16h), "s"(rS), "s"(sw) : "memory");
    asm volatile("s_waitcnt lgkmcnt(0)" ::: "memory");
    asm volatile("v_mfma_f32_16x16x32_bf16 %0, %1, %2, %0" : "+v"(acc[0][0]) : "v"(X0[0]), "v"(W0[0]));
    asm volatile("ds_read_b128 %0, %1 offset:0" : "=v"(X1[0]) : "v"(xa1) : "memory");
    asm volatile("v_mfma_f32_16x16x32_bf16 %0, %1, %2, %0" : "+v"(acc[1][0]) : "v"(X0[1]), "v"(W0[0]));
    asm volatile("ds_read_b128 %0, %1 offset:2048" : "=v"(X1[1]) : "v"(xa1) : "memory");
    asm volatile("v_mfma_f32_16x16x32_bf16 %0, %1, %2, %0" : "+v"(acc[2][0]) : "v"(X0[2]), "v"(W0[0]));
    asm volatile("ds_read_b128 %0, %1 offset:4096" : "=v"(X1[2]) : "v"(xa1) : "memory");
    asm volatile("v_mfma_f32_16x16x32_bf16 %0, %1, %2, %0" : "+v"(acc[3][0]) : "v"(X0[3]), "v"(W0[0]));
    asm volatile("ds_read_b128 %0, %1 offset:6144" : "=v"(X1[3]) : "v"(xa1) : "memory");
    sw = swb + (kso + 2 + wm*(1u))*40960;
    asm volatile("" : "+s"(sw));
    asm volatile("s_add_u32 m0, %0, 133120" :: "s"(lw2) : "scc", "memory");
    asm volatile("v_mfma_f32_16x16x32_bf16 %0, %1, %2, %0" : "+v"(acc[0][1]) : "v"(X0[0]), "v"(W0[1]));
    asm volatile("ds_read_b128 %0, %1 offset:0" : "=v"(W1[0]) : "v"(wa1) : "memory");
    asm volatile("buffer_load_dwordx4 %0, %1, %2 offen offset:0 sc1 lds" :: "v"(vl16), "s"(rS), "s"(sw) : "memory");
    asm volatile("v_mfma_f32_16x16x32_bf16 %0, %1, %2, %0" : "+v"(acc[1][1]) : "v"(X0[1]), "v"(W0[1]));
    asm volatile("ds_read_b128 %0, %1 offset:2048" : "=v"(W1[1]) : "v"(wa1) : "memory");
    asm volatile("v_mfma_f32_16x16x32_bf16 %0, %1, %2, %0" : "+v"(acc[2][1]) : "v"(X0[2]), "v"(W0[1]));
    asm volatile("ds_read_b128 %0, %1 offset:4096" : "=v"(W1[2]) : "v"(wa1) : "memory");
    asm volatile("v_mfma_f32_16x16x32_bf16 %0, %1, %2, %0" : "+v"(acc[3][1]) : "v"(X0[3]), "v"(W0[1]));
    asm volatile("ds_read_b128 %0, %1 offset:6144" : "=v"(W1[3]) : "v"(wa1) : "memory");
    asm volatile("v_mfma_f32_16x16x32_bf16 %0, %1, %2, %0" : "+v"(acc[0][2]) : "v"(X0[0]), "v"(W0[2]));
    asm volatile("ds_read_b128 %0, %1 offset:8192" : "=v"(W1[4]) : "v"(wa1) : "memory");
    asm volatile("buffer_load_dwordx4 %0, %1, %2 offen offset:1024 sc1 lds" :: "v"(vl16), "s"(rS), "s"(sw) : "memory");
    asm volatile("v_mfma_f32_16x16x32_bf16 %0, %1, %2, %0" : "+v"(acc[1][2]) : "v"(X0[1]), "v"(W0[2]));
    asm volatile("v_mfma_f32_16x16x32_bf16 %0, %1, %2, %0" : "+v"(acc[2][2]) : "v"(X0[2]), "v"(W0[2]));
    asm volatile("v_mfma_f32_16x16x32_bf16 %0, %1, %2, %0" : "+v"(acc[3][2]) : "v"(X0[3]), "v"(W0[2]));
    asm volatile("v_mfma_f32_16x16x32_bf16 %0, %1, %2, %0" : "+v"(acc[0][3]) : "v"(X0[0]), "v"(W0[3]));
    asm volatile("v_mfma_f32_16x16x32_bf16 %0, %1, %2, %0" : "+v"(acc[1][3]) : "v"(X0[1]), "v"(W0[3]));
    asm volatile("buffer_load_dwordx4 %0, %1, %2 offen offset:2048 sc1 lds" :: "v"(vl16), "s"(rS), "s"(sw) : "memory");
    asm volatile("v_mfma_f32_16x16x32_bf16 %0, %1, %2, %0" : "+v"(acc[2][3]) : "v"(X0[2]), "v"(W0[3]));
    asm volatile("s_waitcnt vmcnt(8)" ::: "memory");
    float la_, lb_, lc, ld, le, lm; const float k_eps = 1e-5f; const unsigned lra = lds0 + (tid & 127)*4;
    asm volatile("v_cvt_pk_bf16_f32 %0, %1, %1" : "=v"(la_) : "v"(st.y));
    asm volatile("v_mfma_f32_16x16x32_bf16 %0, %1, %2, %0" : "+v"(acc[3][3]) : "v"(X0[3]), "v"(W0[3]));
    asm volatile("v_lshlrev_b32 %0, 16, %0" : "+v"(la_) : );
    asm volatile("v_add_f32 %0, %1, %0" : "+v"(la_) : "s"(k_eps));
    asm volatile("v_mfma_f32_16x16x32_bf16 %0, %1, %2, %0" : "+v"(acc[0][4]) : "v"(X0[0]), "v"(W0[4]));
    asm volatile("v_cvt_pk_bf16_f32 %0, %1, %1" : "=v"(la_) : "v"(la_));
    asm volatile("v_lshlrev_b32 %0, 16, %0" : "+v"(la_) : );
    asm volatile("v_mfma_f32_16x16x32_bf16 %0, %1, %2, %0" : "+v"(acc[1][4]) : "v"(X0[1]), "v"(W0[4]));
    asm volatile("v_sqrt_f32 %0, %0" : "+v"(la_) : );
    asm volatile("v_add_f32 %0, %1, %2" : "=v"(lc) : "v"(c4[0].x), "v"(c4[0].y));
    asm volatile("v_mfma_f32_16x16x32_bf16 %0, %1, %2, %0" : "+v"(acc[2][4]) : "v"(X0[2]), "v"(W0[4]));
    asm volatile("v_cvt_pk_bf16_f32 %0, %1, %1" : "=v"(la_) : "v"(la_));
    asm volatile("v_lshlrev_b32 %0, 16, %0" : "+v"(la_) : );
    asm volatile("v_mfma_f32_16x16x32_bf16 %0, %1, %2, %0" : "+v"(acc[3][4]) : "v"(X0[3]), "v"(W0[4]));
    asm volatile("s_waitcnt lgkmcnt(0)" ::: "memory");
    asm volatile("v_mfma_f32_16x16x32_bf16 %0, %1, %2, %0" : "+v"(acc[0][0]) : "v"(X1[0]), "v"(W1[0]));
    asm volatile("v_rcp_f32 %0, %0" : "+v"(la_) : );
    asm volatile("v_add_f32 %0, %1, %2" : "=v"(ld) : "v"(c4[0].z), "v"(c4[0].w));
    asm volatile("v_mfma_f32_16x16x32_bf16 %0, %1, %2, %0" : "+v"(acc[1][0]) : "v"(X1[1]), "v"(W1[0]));
    asm volatile("v_cvt_pk_bf16_f32 %0, %1, %1" : "=v"(lm) : "v"(st.x));
    asm volatile("v_lshlrev_b32 %0, 16, %0" : "+v"(lm) : );
    asm volatile("v_mfma_f32_16x16x32_bf16 %0, %1, %2, %0" : "+v"(acc[2][0]) : "v"(X1[2]), "v"(W1[0]));
    asm volatile("v_mul_f32 %0, %0, %1" : "+v"(lm) : "v"(la_));
    asm volatile("v_add_f32 %0, %1, %2" : "=v"(lc) : "v"(lc), "v"(ld));
    asm volatile("v_mfma_f32_16x16x32_bf16 %0, %1, %2, %0" : "+v"(acc[3][0]) : "v"(X1[3]), "v"(W1[0]));
    asm volatile("v_add_f32 %0, %1, %2" : "=v"(ld) : "v"(c4[1].x), "v"(c4[1].y));
    asm volatile("v_add_f32 %0, %1, %2" : "=v"(le) : "v"(c4[1].z), "v"(c4[1].w));
    asm volatile("v_mfma_f32_16x16x32_bf16 %0, %1, %2, %0" : "+v"(acc[0][1]) : "v"(X1[0]), "v"(W1[1]));
    asm volatile("v_add_f32 %0, %1, %2" : "=v"(ld) : "v"(ld), "v"(le));
    asm volatile("v_add_f32 %0, %1, %2" : "=v"(lc) : "v"(lc), "v"(ld));
    asm volatile("v_mfma_f32_16x16x32_bf16 %0, %1, %2, %0" : "+v"(acc[1][1]) : "v"(X1[1]), "v"(W1[1]));
    asm volatile("ds_write2st64_b32 %0, %1, %2 offset0:0 offset1:2" :: "v"(lra), "v"(la_), "v"(lm) : "memory");
    asm volatile("ds_write_b32 %0, %1 offset:1024" :: "v"(lds0 + tid*4), "v"(lc) : "memory");
    asm volatile("v_mfma_f32_16x16x32_bf16 %0, %1, %2, %0" : "+v"(acc[2][1]) : "v"(X1[2]), "v"(W1[1]));
    asm volatile("v_mfma_f32_16x16x32_bf16 %0, %1, %2, %0" : "+v"(acc[3][1]) : "v"(X1[3]), "v"(W1[1]));
    asm volatile("v_mfma_f32_16x16x32_bf16 %0, %1, %2, %0" : "+v"(acc[0][2]) : "v"(X1[0]), "v"(W1[2]));
    asm volatile("s_waitcnt lgkmcnt(0)\n\ts_barrier" ::: "memory");
    f4 rs[4], mr[4]; float sp_[5]; f2 l1[5];
    const unsigned ea = lds0 + (64*wm + 16*g)*4, eb = lds0 + 1024 + (80*wn + r16)*4, ec = lds0 + 2560 + (40*wn + (r16 >> 1))*8;
    asm volatile("v_mfma_f32_16x16x32_bf16 %0, %1, %2, %0" : "+v"(acc[1][2]) : "v"(X1[1]), "v"(W1[2]));
    asm volatile("ds_read_b32 %0, %1 offset:0" : "=v"(sp_[0]) : "v"(eb) : "memory");
    asm volatile("ds_read_b32 %0, %1 offset:64" : "=v"(sp_[1]) : "v"(eb) : "memory");
    asm volatile("ds_read_b32 %0, %1 offset:128" : "=v"(sp_[2]) : "v"(eb) : "memory");
    asm volatile("v_mfma_f32_16x16x32_bf16 %0, %1, %2, %0" : "+v"(acc[2][2]) : "v"(X1[2]), "v"(W1[2]));
    asm volatile("ds_read_b32 %0, %1 offset:192" : "=v"(sp_[3]) : "v"(eb) : "memory");
    asm volatile("ds_read_b32 %0, %1 offset:256" : "=v"(sp_[4]) : "v"(eb) : "memory");
    asm volatile("buffer_load_dwordx4 %0, %1, %2 offen offset:3072 sc1 lds" :: "v"(vl16), "s"(rS), "s"(sw) : "memory");
    asm volatile("v_mfma_f32_16x16x32_bf16 %0, %1, %2, %0" : "+v"(acc[3][2]) : "v"(X1[3]), "v"(W1[2]));
    asm volatile("ds_read_b128 %0, %1 offset:0" : "=v"(rs[0]) : "v"(ea) : "memory");
    asm volatile("ds_read_b128 %0, %1 offset:512" : "=v"(mr[0]) : "v"(ea) : "memory");
    asm volatile("s_addk_i32 m0, 0x1000" ::: "scc", "memory");
    asm volatile("v_mfma_f32_16x16x32_bf16 %0, %1, %2, %0" : "+v"(acc[0][3]) : "v"(X1[0]), "v"(W1[3]));
    asm volatile("ds_read_b128 %0, %1 offset:16" : "=v"(rs[1]) : "v"(ea) : "memory");
    asm volatile("ds_read_b128 %0, %1 offset:528" : "=v"(mr[1]) : "v"(ea) : "memory");
    asm volatile("buffer_load_dwordx4 %0, %1, %2 offen offset:0 sc1 lds" :: "v"(vl16h), "s"(rS), "s"(sw) : "memory");
    asm volatile("v_mfma_f32_16x16x32_bf16 %0, %1, %2, %0" : "+v"(acc[1][3]) : "v"(X1[1]), "v"(W1[3]));
    asm volatile("ds_read_b128 %0, %1 offset:32" : "=v"(rs[2]) : "v"(ea) : "memory");
    asm volatile("ds_read_b128 %0, %1 offset:544" : "=v"(mr[2]) : "v"(ea) : "memory");
    asm volatile("ds_read_b128 %0, %1 offset:48" : "=v"(rs[3]) : "v"(ea) : "memory");
    asm volatile("v_mfma_f32_16x16x32_bf16 %0, %1, %2, %0" : "+v"(acc[2][3]) : "v"(X1[2]), "v"(W1[3]));
    asm volatile("ds_read_b128 %0, %1 offset:560" : "=v"(mr[3]) : "v"(ea) : "memory");
    asm volatile("ds_read_b64 %0, %1 offset:0" : "=v"(l1[0]) : "v"(ec) : "memory");
    asm volatile("v_mfma_f32_16x16x32_bf16 %0, %1, %2, %0" : "+v"(acc[3][3]) : "v"(X1[3]), "v"(W1[3]));
    asm volatile("ds_read_b64 %0, %1 offset:64" : "=v"(l1[1]) : "v"(ec) : "memory");
    asm volatile("ds_read_b64 %0, %1 offset:128" : "=v"(l1[2]) : "v"(ec) : "memory");
    asm volatile("v_mfma_f32_16x16x32_bf16 %0, %1, %2, %0" : "+v"(acc[0][4]) : "v"(X1[0]), "v"(W1[4]));
    asm volatile("ds_read_b64 %0, %1 offset:192" : "=v"(l1[3]) : "v"(ec) : "memory");
    asm volatile("ds_read_b64 %0, %1 offset:256" : "=v"(l1[4]) : "v"(ec) : "memory");
    asm volatile("v_mfma_f32_16x16x32_bf16 %0, %1, %2, %0" : "+v"(acc[1][4]) : "v"(X1[1]), "v"(W1[4]));
    asm volatile("v_mfma_f32_16x16x32_bf16 %0, %1, %2, %0" : "+v"(acc[2][4]) : "v"(X1[2]), "v"(W1[4]));
    asm volatile("v_mfma_f32_16x16x32_bf16 %0, %1, %2, %0" : "+v"(acc[3][4]) : "v"(X1[3]), "v"(W1[4]));
    asm volatile("s_waitcnt lgkmcnt(0)\n\ts_nop 6" ::: "memory");
    asm volatile("" : "+v"(sp_[0]), "+v"(sp_[1]), "+v"(sp_[2]), "+v"(sp_[3]), "+v"(sp_[4]), "+v"(l1[0]), "+v"(l1[1]), "+v"(l1[2]), "+v"(l1[3]), "+v"(l1[4]), "+v"(rs[0]), "+v"(mr[0]));
    asm volatile("" : "+v"(rs[1]), "+v"(mr[1]), "+v"(rs[2]), "+v"(mr[2]), "+v"(rs[3]), "+v"(mr[3]));
    asm volatile("" : "+v"(acc[0][0]), "+v"(acc[0][1]), "+v"(acc[0][2]), "+v"(acc[0][3]), "+v"(acc[0][4]), "+v"(acc[1][0]), "+v"(acc[1][1]), "+v"(acc[1][2]), "+v"(acc[1][3]), "+v"(acc[1][4]), "+v"(acc[2][0]), "+v"(acc[2][1]));
    asm volatile("" : "+v"(acc[2][2]), "+v"(acc[2][3]), "+v"(acc[2][4]), "+v"(acc[3][0]), "+v"(acc[3][1]), "+v"(acc[3][2]), "+v"(acc[3][3]), "+v"(acc[3][4]));
    const unsigned flev = (lane >> 1) & 3, fo = 4096 + (c*16 + (flev < 3 ? 8 : 0) + 2*kq + (lane & 1))*128 + (flev < 3 ? 4*flev : 0);
    const bool fown = (lane & 1) == (j & 1) && flev == 3;
    unsigned f2v;
    const unsigned hv0 = vl16 + (4*wm + ((2*wn + kq) & 3))*1024, hv1 = vl16 + (4*wm + ((2*wn + 1 + kq) & 3))*1024, lh = lds0 + w*2048;
    float la[5], lb[5], en[5];
    _Pragma("unroll") for (int n = 0; n < 5; ++n) { const float S = sp_[n]; la[n] = l1[n].x; lb[n] = (r16 & 1) ? l1[n].y : -l1[n].y;
      _Pragma("unroll") for (int m = 0; m < 4; ++m) acc[m][n] = acc[m][n]*rs[m] - mr[m]*S; }
    float tq[5];
    asm volatile("" : "+v"(la[0]), "+v"(la[1]), "+v"(la[2]), "+v"(la[3]), "+v"(la[4]), "+v"(lb[0]), "+v"(lb[1]), "+v"(lb[2]), "+v"(lb[3]), "+v"(lb[4]));
    asm volatile("" : "+v"(acc[0][0]), "+v"(acc[0][1]), "+v"(acc[0][2]), "+v"(acc[0][3]), "+v"(acc[0][4]), "+v"(acc[1][0]), "+v"(acc[1][1]), "+v"(acc[1][2]), "+v"(acc[1][3]), "+v"(acc[1][4]), "+v"(acc[2][0]), "+v"(acc[2][1]));
    asm volatile("" : "+v"(acc[2][2]), "+v"(acc[2][3]), "+v"(acc[2][4]), "+v"(acc[3][0]), "+v"(acc[3][1]), "+v"(acc[3][2]), "+v"(acc[3][3]), "+v"(acc[3][4]));
    asm volatile("s_nop 1");
    asm volatile("v_fma_f32 %0, %5, %15, %20\n\tv_fma_f32 %1, %6, %16, %21\n\tv_fma_f32 %2, %7, %17, %22\n\tv_fma_f32 %3, %8, %18, %23\n\tv_fma_f32 %4, %9, %19, %24\n\tv_fmac_f32_dpp %0, %15, %10 quad_perm:[1,0,3,2] row_mask:0xf bank_mask:0xf\n\tv_fmac_f32_dpp %1, %16, %11 quad_perm:[1,0,3,2] row_mask:0xf bank_mask:0xf\n\tv_fmac_f32_dpp %2, %17, %12 quad_perm:[1,0,3,2] row_mask:0xf bank_mask:0xf\n\tv_fmac_f32_dpp %3, %18, %13 quad_perm:[1,0,3,2] row_mask:0xf bank_mask:0xf\n\tv_fmac_f32_dpp %4, %19, %14 quad_perm:[1,0,3,2] row_mask:0xf bank_mask:0xf" : "=&v"(tq[0]), "=&v"(tq[1]), "=&v"(tq[2]), "=&v"(tq[3]), "=&v"(tq[4]) : "v"(la[0]), "v"(la[1]), "v"(la[2]), "v"(la[3]), "v"(la[4]), "v"(lb[0]), "v"(lb[1]), "v"(lb[2]), "v"(lb[3]), "v"(lb[4]), "v"(acc[0][0].x), "v"(acc[0][1].x), "v"(acc[0][2].x), "v"(acc[0][3].x), "v"(acc[0][4].x), "v"(acc[0][0].y), "v"(acc[0][1].y), "v"(acc[0][2].y), "v"(acc[0][3].y), "v"(acc[0][4].y));
    sw = swb + (kso + 4 + wm*(ksa + 0 - kso - 4))*40960;
    asm volatile("" : "+s"(sw));
    asm volatile("s_add_u32 m0, %0, 112640" :: "s"(lw2) : "scc", "memory");
    asm volatile("s_nop 0");
    asm volatile("buffer_load_dwordx4 %0, %1, %2 offen offset:0 sc1 lds" :: "v"(vl16), "s"(rS), "s"(sw) : "memory");
    asm volatile("v_fma_f32 %0, %5, %15, %20\n\tv_fma_f32 %1, %6, %16, %21\n\tv_fma_f32 %2, %7, %17, %22\n\tv_fma_f32 %3, %8, %18, %23\n\tv_fma_f32 %4, %9, %19, %24\n\tv_fmac_f32_dpp %0, %15, %10 quad_perm:[1,0,3,2] row_mask:0xf bank_mask:0xf\n\tv_fmac_f32_dpp %1, %16, %11 quad_perm:[1,0,3,2] row_mask:0xf bank_mask:0xf\n\tv_fmac_f32_dpp %2, %17, %12 quad_perm:[1,0,3,2] row_mask:0xf bank_mask:0xf\n\tv_fmac_f32_dpp %3, %18, %13 quad_perm:[1,0,3,2] row_mask:0xf bank_mask:0xf\n\tv_fmac_f32_dpp %4, %19, %14 quad_perm:[1,0,3,2] row_mask:0xf bank_mask:0xf" : "=&v"(en[0]), "=&v"(en[1]), "=&v"(en[2]), "=&v"(en[3]), "=&v"(en[4]) : "v"(la[0]), "v"(la[1]), "v"(la[2]), "v"(la[3]), "v"(la[4]), "v"(lb[0]), "v"(lb[1]), "v"(lb[2]), "v"(lb[3]), "v"(lb[4]), "v"(tq[0]), "v"(tq[1]), "v"(tq[2]), "v"(tq[3]), "v"(tq[4]), "v"(acc[0][0].z), "v"(acc[0][1].z), "v"(acc[0][2].z), "v"(acc[0][3].z), "v"(acc[0][4].z));
    asm volatile("v_fma_f32 %0, %5, %15, %20\n\tv_fma_f32 %1, %6, %16, %21\n\tv_fma_f32 %2, %7, %17, %22\n\tv_fma_f32 %3, %8, %18, %23\n\tv_fma_f32 %4, %9, %19, %24\n\tv_fmac_f32_dpp %0, %15, %10 quad_perm:[1,0,3,2] row_mask:0xf bank_mask:0xf\n\tv_fmac_f32_dpp %1, %16, %11 quad_perm:[1,0,3,2] row_mask:0xf bank_mask:0xf\n\tv_fmac_f32_dpp %2, %17, %12 quad_perm:[1,0,3,2] row_mask:0xf bank_mask:0xf\n\tv_fmac_f32_dpp %3, %18, %13 quad_perm:[1,0,3,2] row_mask:0xf bank_mask:0xf\n\tv_fmac_f32_dpp %4, %19, %14 quad_perm:[1,0,3,2] row_mask:0xf bank_mask:0xf" : "=&v"(tq[0]), "=&v"(tq[1]), "=&v"(tq[2]), "=&v"(tq[3]), "=&v"(tq[4]) : "v"(la[0]), "v"(la[1]), "v"(la[2]), "v"(la[3]), "v"(la[4]), "v"(lb[0]), "v"(lb[1]), "v"(lb[2]), "v"(lb[3]), "v"(lb[4]), "v"(en[0]), "v"(en[1]), "v"(en[2]), "v"(en[3]), "v"(en[4]), "v"(acc[0][0].w), "v"(acc[0][1].w), "v"(acc[0][2].w), "v"(acc[0][3].w), "v"(acc[0][4].w));
    asm volatile("buffer_load_dwordx4 %0, %1, %2 offen offset:1024 sc1 lds" :: "v"(vl16), "s"(rS), "s"(sw) : "memory");
    asm volatile("v_fma_f32 %0, %5, %15, %20\n\tv_fma_f32 %1, %6, %16, %21\n\tv_fma_f32 %2, %7, %17, %22\n\tv_fma_f32 %3, %8, %18, %23\n\tv_fma_f32 %4, %9, %19, %24\n\tv_fmac_f32_dpp %0, %15, %10 quad_perm:[1,0,3,2] row_mask:0xf bank_mask:0xf\n\tv_fmac_f32_dpp %1, %16, %11 quad_perm:[1,0,3,2] row_mask:0xf bank_mask:0xf\n\tv_fmac_f32_dpp %2, %17, %12 quad_perm:[1,0,3,2] row_mask:0xf bank_mask:0xf\n\tv_fmac_f32_dpp %3, %18, %13 quad_perm:[1,0,3,2] row_mask:0xf bank_mask:0xf\n\tv_fmac_f32_dpp %4, %19, %14 quad_perm:[1,0,3,2] row_mask:0xf bank_mask:0xf" : "=&v"(en[0]), "=&v"(en[1]), "=&v"(en[2]), "=&v"(en[3]), "=&v"(en[4]) : "v"(la[0]), "v"(la[1]), "v"(la[2]), "v"(la[3]), "v"(la[4]), "v"(lb[0]), "v"(lb[1]), "v"(lb[2]), "v"(lb[3]), "v"(lb[4]), "v"(tq[0]), "v"(tq[1]), "v"(tq[2]), "v"(tq[3]), "v"(tq[4]), "v"(acc[1][0].x), "v"(acc[1][1].x), "v"(acc[1][2].x), "v"(acc[1][3].x), "v"(acc[1][4].x));
    asm volatile("v_fma_f32 %0, %5, %15, %20\n\tv_fma_f32 %1, %6, %16, %21\n\tv_fma_f32 %2, %7, %17, %22\n\tv_fma_f32 %3, %8, %18, %23\n\tv_fma_f32 %4, %9, %19, %24\n\tv_fmac_f32_dpp %0, %15, %10 quad_perm:[1,0,3,2] row_mask:0xf bank_mask:0xf\n\tv_fmac_f32_dpp %1, %16, %11 quad_perm:[1,0,3,2] row_mask:0xf bank_mask:0xf\n\tv_fmac_f32_dpp %2, %17, %12 quad_perm:[1,0,3,2] row_mask:0xf bank_mask:0xf\n\tv_fmac_f32_dpp %3, %18, %13 quad_perm:[1,0,3,2] row_mask:0xf bank_mask:0xf\n\tv_fmac_f32_dpp %4, %19, %14 quad_perm:[1,0,3,2] row_mask:0xf bank_mask:0xf" : "=&v"(tq[0]), "=&v"(tq[1]), "=&v"(tq[2]), "=&v"(tq[3]), "=&v"(tq[4]) : "v"(la[0]), "v"(la[1]), "v"(la[2]), "v"(la[3]), "v"(la[4]), "v"(lb[0]), "v"(lb[1]), "v"(lb[2]), "v"(lb[3]), "v"(lb[4]), "v"(en[0]), "v"(en[1]), "v"(en[2]), "v"(en[3]), "v"(en[4]), "v"(acc[1][0].y), "v"(acc[1][1].y), "v"(acc[1][2].y), "v"(acc[1][3].y), "v"(acc[1][4].y));
    asm volatile("buffer_load_dwordx4 %0, %1, %2 offen offset:2048 sc1 lds" :: "v"(vl16), "s"(rS), "s"(sw) : "memory");
    asm volatile("v_fma_f32 %0, %5, %15, %20\n\tv_fma_f32 %1, %6, %16, %21\n\tv_fma_f32 %2, %7, %17, %22\n\tv_fma_f32 %3, %8, %18, %23\n\tv_fma_f32 %4, %9, %19, %24\n\tv_fmac_f32_dpp %0, %15, %10 quad_perm:[1,0,3,2] row_mask:0xf bank_mask:0xf\n\tv_fmac_f32_dpp %1, %16, %11 quad_perm:[1,0,3,2] row_mask:0xf bank_mask:0xf\n\tv_fmac_f32_dpp %2, %17, %12 quad_perm:[1,0,3,2] row_mask:0xf bank_mask:0xf\n\tv_fmac_f32_dpp %3, %18, %13 quad_perm:[1,0,3,2] row_mask:0xf bank_mask:0xf\n\tv_fmac_f32_dpp %4, %19, %14 quad_perm:[1,0,3,2] row_mask:0xf bank_mask:0xf" : "=&v"(en[0]), "=&v"(en[1]), "=&v"(en[2]), "=&v"(en[3]), "=&v"(en[4]) : "v"(la[0]), "v"(la[1]), "v"(la[2]), "v"(la[3]), "v"(la[4]), "v"(lb[0]), "v"(lb[1]), "v"(lb[2]), "v"(lb[3]), "v"(lb[4]), "v"(tq[0]), "v"(tq[1]), "v"(tq[2]), "v"(tq[3]), "v"(tq[4]), "v"(acc[1][0].z), "v"(acc[1][1].z), "v"(acc[1][2].z), "v"(acc[1][3].z), "v"(acc[1][4].z));
    asm volatile("v_fma_f32 %0, %5, %15, %20\n\tv_fma_f32 %1, %6, %16, %21\n\tv_fma_f32 %2, %7, %17, %22\n\tv_fma_f32 %3, %8, %18, %23\n\tv_fma_f32 %4, %9, %19, %24\n\tv_fmac_f32_dpp %0, %15, %10 quad_perm:[1,0,3,2] row_mask:0xf bank_mask:0xf\n\tv_fmac_f32_dpp %1, %16, %11 quad_perm:[1,0,3,2] row_mask:0xf bank_mask:0xf\n\tv_fmac_f32_dpp %2, %17, %12 quad_perm:[1,0,3,2] row_mask:0xf bank_mask:0xf\n\tv_fmac_f32_dpp %3, %18, %13 quad_perm:[1,0,3,2] row_mask:0xf bank_mask:0xf\n\tv_fmac_f32_dpp %4, %19, %14 quad_perm:[1,0,3,2] row_mask:0xf bank_mask:0xf" : "=&v"(tq[0]), "=&v"(tq[1]), "=&v"(tq[2]), "=&v"(tq[3]), "=&v"(tq[4]) : "v"(la[0]), "v"(la[1]), "v"(la[2]), "v"(la[3]), "v"(la[4]), "v"(lb[0]), "v"(lb[1]), "v"(lb[2]), "v"(lb[3]), "v"(lb[4]), "v"(en[0]), "v"(en[1]), "v"(en[2]), "v"(en[3]), "v"(en[4]), "v"(acc[1][0].w), "v"(acc[1][1].w), "v"(acc[1][2].w), "v"(acc[1][3].w), "v"(acc[1][4].w));
    asm volatile("buffer_load_dwordx4 %0, %1, %2 offen offset:3072 sc1 lds" :: "v"(vl16), "s"(rS), "s"(sw) : "memory");
    asm volatile("v_fma_f32 %0, %5, %15, %20\n\tv_fma_f32 %1, %6, %16, %21\n\tv_fma_f32 %2, %7, %17, %22\n\tv_fma_f32 %3, %8, %18, %23\n\tv_fma_f32 %4, %9, %19, %24\n\tv_fmac_f32_dpp %0, %15, %10 quad_perm:[1,0,3,2] row_mask:0xf bank_mask:0xf\n\tv_fmac_f32_dpp %1, %16, %11 quad_perm:[1,0,3,2] row_mask:0xf bank_mask:0xf\n\tv_fmac_f32_dpp %2, %17, %12 quad_perm:[1,0,3,2] row_mask:0xf bank_mask:0xf\n\tv_fmac_f32_dpp %3, %18, %13 quad_perm:[1,0,3,2] row_mask:0xf bank_mask:0xf\n\tv_fmac_f32_dpp %4, %19, %14 quad_perm:[1,0,3,2] row_mask:0xf bank_mask:0xf" : "=&v"(en[0]), "=&v"(en[1]), "=&v"(en[2]), "=&v"(en[3]), "=&v"(en[4]) : "v"(la[0]), "v"(la[1]), "v"(la[2]), "v"(la[3]), "v"(la[4]), "v"(lb[0]), "v"(lb[1]), "v"(lb[2]), "v"(lb[3]), "v"(lb[4]), "v"(tq[0]), "v"(tq[1]), "v"(tq[2]), "v"(tq[3]), "v"(tq[4]), "v"(acc[2][0].x), "v"(acc[2][1].x), "v"(acc[2][2].x), "v"(acc[2][3].x), "v"(acc[2][4].x));
    asm volatile("v_fma_f32 %0, %5, %15, %20\n\tv_fma_f32 %1, %6, %16, %21\n\tv_fma_f32 %2, %7, %17, %22\n\tv_fma_f32 %3, %8, %18, %23\n\tv_fma_f32 %4, %9, %19, %24\n\tv_fmac_f32_dpp %0, %15, %10 quad_perm:[1,0,3,2] row_mask:0xf bank_mask:0xf\n\tv_fmac_f32_dpp %1, %16, %11 quad_perm:[1,0,3,2] row_mask:0xf bank_mask:0xf\n\tv_fmac_f32_dpp %2, %17, %12 quad_perm:[1,0,3,2] row_mask:0xf bank_mask:0xf\n\tv_fmac_f32_dpp %3, %18, %13 quad_perm:[1,0,3,2] row_mask:0xf bank_mask:0xf\n\tv_fmac_f32_dpp %4, %19, %14 quad_perm:[1,0,3,2] row_mask:0xf bank_mask:0xf" : "=&v"(tq[0]), "=&v"(tq[1]), "=&v"(tq[2]), "=&v"(tq[3]), "=&v"(tq[4]) : "v"(la[0]), "v"(la[1]), "v"(la[2]), "v"(la[3]), "v"(la[4]), "v"(lb[0]), "v"(lb[1]), "v"(lb[2]), "v"(lb[3]), "v"(lb[4]), "v"(en[0]), "v"(en[1]), "v"(en[2]), "v"(en[3]), "v"(en[4]), "v"(acc[2][0].y), "v"(acc[2][1].y), "v"(acc[2][2].y), "v"(acc[2][3].y), "v"(acc[2][4].y));
    asm volatile("s_addk_i32 m0, 0x1000" ::: "scc", "memory");
    asm volatile("s_nop 0");
    asm volatile("buffer_load_dwordx4 %0, %1, %2 offen offset:0 sc1 lds" :: "v"(vl16h), "s"(rS), "s"(sw) : "memory");
    asm volatile("v_fma_f32 %0, %5, %15, %20\n\tv_fma_f32 %1, %6, %16, %21\n\tv_fma_f32 %2, %7, %17, %22\n\tv_fma_f32 %3, %8, %18, %23\n\tv_fma_f32 %4, %9, %19, %24\n\tv_fmac_f32_dpp %0, %15, %10 quad_perm:[1,0,3,2] row_mask:0xf bank_mask:0xf\n\tv_fmac_f32_dpp %1, %16, %11 quad_perm:[1,0,3,2] row_mask:0xf bank_mask:0xf\n\tv_fmac_f32_dpp %2, %17, %12 quad_perm:[1,0,3,2] row_mask:0xf bank_mask:0xf\n\tv_fmac_f32_dpp %3, %18, %13 quad_perm:[1,0,3,2] row_mask:0xf bank_mask:0xf\n\tv_fmac_f32_dpp %4, %19, %14 quad_perm:[1,0,3,2] row_mask:0xf bank_mask:0xf" : "=&v"(en[0]), "=&v"(en[1]), "=&v"(en[2]), "=&v"(en[3]), "=&v"(en[4]) : "v"(la[0]), "v"(la[1]), "v"(la[2]), "v"(la[3]), "v"(la[4]), "v"(lb[0]), "v"(lb[1]), "v"(lb[2]), "v"(lb[3]), "v"(lb[4]), "v"(tq[0]), "v"(tq[1]), "v"(tq[2]), "v"(tq[3]), "v"(tq[4]), "v"(acc[2][0].z), "v"(acc[2][1].z), "v"(acc[2][2].z), "v"(acc[2][3].z), "v"(acc[2][4].z));
    asm volatile("v_fma_f32 %0, %5, %15, %20\n\tv_fma_f32 %1, %6, %16, %21\n\tv_fma_f32 %2, %7, %17, %22\n\tv_fma_f32 %3, %8, %18, %23\n\tv_fma_f32 %4, %9, %19, %24\n\tv_fmac_f32_dpp %0, %15, %10 quad_perm:[1,0,3,2] row_mask:0xf bank_mask:0xf\n\tv_fmac_f32_dpp %1, %16, %11 quad_perm:[1,0,3,2] row_mask:0xf bank_mask:0xf\n\tv_fmac_f32_dpp %2, %17, %12 quad_perm:[1,0,3,2] row_mask:0xf bank_mask:0xf\n\tv_fmac_f32_dpp %3, %18, %13 quad_perm:[1,0,3,2] row_mask:0xf bank_mask:0xf\n\tv_fmac_f32_dpp %4, %19, %14 quad_perm:[1,0,3,2] row_mask:0xf bank_mask:0xf" : "=&v"(tq[0]), "=&v"(tq[1]), "=&v"(tq[2]), "=&v"(tq[3]), "=&v"(tq[4]) : "v"(la[0]), "v"(la[1]), "v"(la[2]), "v"(la[3]), "v"(la[4]), "v"(lb[0]), "v"(lb[1]), "v"(lb[2]), "v"(lb[3]), "v"(lb[4]), "v"(en[0]), "v"(en[1]), "v"(en[2]), "v"(en[3]), "v"(en[4]), "v"(acc[2][0].w), "v"(acc[2][1].w), "v"(acc[2][2].w), "v"(acc[2][3].w), "v"(acc[2][4].w));
    asm volatile("v_fma_f32 %0, %5, %15, %20\n\tv_fma_f32 %1, %6, %16, %21\n\tv_fma_f32 %2, %7, %17, %22\n\tv_fma_f32 %3, %8, %18, %23\n\tv_fma_f32 %4, %9, %19, %24\n\tv_fmac_f32_dpp %0, %15, %10 quad_perm:[1,0,3,2] row_mask:0xf bank_mask:0xf\n\tv_fmac_f32_dpp %1, %16, %11 quad_perm:[1,0,3,2] row_mask:0xf bank_mask:0xf\n\tv_fmac_f32_dpp %2, %17, %12 quad_perm:[1,0,3,2] row_mask:0xf bank_mask:0xf\n\tv_fmac_f32_dpp %3, %18, %13 quad_perm:[1,0,3,2] row_mask:0xf bank_mask:0xf\n\tv_fmac_f32_dpp %4, %19, %14 quad_perm:[1,0,3,2] row_mask:0xf bank_mask:0xf" : "=&v"(en[0]), "=&v"(en[1]), "=&v"(en[2]), "=&v"(en[3]), "=&v"(en[4]) : "v"(la[0]), "v"(la[1]), "v"(la[2]), "v"(la[3]), "v"(la[4]), "v"(lb[0]), "v"(lb[1]), "v"(lb[2]), "v"(lb[3]), "v"(lb[4]), "v"(tq[0]), "v"(tq[1]), "v"(tq[2]), "v"(tq[3]), "v"(tq[4]), "v"(acc[3][0].x), "v"(acc[3][1].x), "v"(acc[3][2].x), "v"(acc[3][3].x), "v"(acc[3][4].x));
    asm volatile("v_fma_f32 %0, %5, %15, %20\n\tv_fma_f32 %1, %6, %16, %21\n\tv_fma_f32 %2, %7, %17, %22\n\tv_fma_f32 %3, %8, %18, %23\n\tv_fma_f32 %4, %9, %19, %24\n\tv_fmac_f32_dpp %0, %15, %10 quad_perm:[1,0,3,2] row_mask:0xf bank_mask:0xf\n\tv_fmac_f32_dpp %1, %16, %11 quad_perm:[1,0,3,2] row_mask:0xf bank_mask:0xf\n\tv_fmac_f32_dpp %2, %17, %12 quad_perm:[1,0,3,2] row_mask:0xf bank_mask:0xf\n\tv_fmac_f32_dpp %3, %18, %13 quad_perm:[1,0,3,2] row_mask:0xf bank_mask:0xf\n\tv_fmac_f32_dpp %4, %19, %14 quad_perm:[1,0,3,2] row_mask:0xf bank_mask:0xf" : "=&v"(tq[0]), "=&v"(tq[1]), "=&v"(tq[2]), "=&v"(tq[3]), "=&v"(tq[4]) : "v"(la[0]), "v"(la[1]), "v"(la[2]), "v"(la[3]), "v"(la[4]), "v"(lb[0]), "v"(lb[1]), "v"(lb[2]), "v"(lb[3]), "v"(lb[4]), "v"(en[0]), "v"(en[1]), "v"(en[2]), "v"(en[3]), "v"(en[4]), "v"(acc[3][0].y), "v"(acc[3][1].y), "v"(acc[3][2].y), "v"(acc[3][3].y), "v"(acc[3][4].y));
    asm volatile("v_fma_f32 %0, %5, %15, %20\n\tv_fma_f32 %1, %6, %16, %21\n\tv_fma_f32 %2, %7, %17, %22\n\tv_fma_f32 %3, %8, %18, %23\n\tv_fma_f32 %4, %9, %19, %24\n\tv_fmac_f32_dpp %0, %15, %10 quad_perm:[1,0,3,2] row_mask:0xf bank_mask:0xf\n\tv_fmac_f32_dpp %1, %16, %11 quad_perm:[1,0,3,2] row_mask:0xf bank_mask:0xf\n\tv_fmac_f32_dpp %2, %17, %12 quad_perm:[1,0,3,2] row_mask:0xf bank_mask:0xf\n\tv_fmac_f32_dpp %3, %18, %13 quad_perm:[1,0,3,2] row_mask:0xf bank_mask:0xf\n\tv_fmac_f32_dpp %4, %19, %14 quad_perm:[1,0,3,2] row_mask:0xf bank_mask:0xf" : "=&v"(en[0]), "=&v"(en[1]), "=&v"(en[2]), "=&v"(en[3]), "=&v"(en[4]) : "v"(la[0]), "v"(la[1]), "v"(la[2]), "v"(la[3]), "v"(la[4]), "v"(lb[0]), "v"(lb[1]), "v"(lb[2]), "v"(lb[3]), "v"(lb[4]), "v"(tq[0]), "v"(tq[1]), "v"(tq[2]), "v"(tq[3]), "v"(tq[4]), "v"(acc[3][0].z), "v"(acc[3][1].z), "v"(acc[3][2].z), "v"(acc[3][3].z), "v"(acc[3][4].z));
    asm volatile("v_fma_f32 %0, %5, %15, %20\n\tv_fma_f32 %1, %6, %16, %21\n\tv_fma_f32 %2, %7, %17, %22\n\tv_fma_f32 %3, %8, %18, %23\n\tv_fma_f32 %4, %9, %19, %24\n\tv_fmac_f32_dpp %0, %15, %10 quad_perm:[1,0,3,2] row_mask:0xf bank_mask:0xf\n\tv_fmac_f32_dpp %1, %16, %11 quad_perm:[1,0,3,2] row_mask:0xf bank_mask:0xf\n\tv_fmac_f32_dpp %2, %17, %12 quad_perm:[1,0,3,2] row_mask:0xf bank_mask:0xf\n\tv_fmac_f32_dpp %3, %18, %13 quad_perm:[1,0,3,2] row_mask:0xf bank_mask:0xf\n\tv_fmac_f32_dpp %4, %19, %14 quad_perm:[1,0,3,2] row_mask:0xf bank_mask:0xf" : "=&v"(tq[0]), "=&v"(tq[1]), "=&v"(tq[2]), "=&v"(tq[3]), "=&v"(tq[4]) : "v"(la[0]), "v"(la[1]), "v"(la[2]), "v"(la[3]), "v"(la[4]), "v"(lb[0]), "v"(lb[1]), "v"(lb[2]), "v"(lb[3]), "v"(lb[4]), "v"(en[0]), "v"(en[1]), "v"(en[2]), "v"(en[3]), "v"(en[4]), "v"(acc[3][0].w), "v"(acc[3][1].w), "v"(acc[3][2].w), "v"(acc[3][3].w), "v"(acc[3][4].w));
    _Pragma("unroll") for (int n = 0; n < 5; ++n) LDS(float, 4608 + (4*wm + g)*640 + (80*wn + 16*n + r16)*4) = tq[n];
    const unsigned kk = lane < 20 ? 0 : lane < 40 ? 1 : 2, pp = lane < 60 ? lane - 20*kk : 19, pl = 20*w + pp;
    const f2 l16 = LDS(f2, 3200 + pl*8), l128 = LDS(f2, 3840 + pl*8);
    asm volatile("s_waitcnt lgkmcnt(0)\n\ts_barrier" ::: "memory");
    f2 sg[8];
    _Pragma("unroll") for (int s = 0; s < 8; ++s) sg[s] = LDS(f2, 4608 + s*640 + pl*8);
    { f2 t = sg[0]; _Pragma("unroll") for (int s = 1; s < 8; ++s) { const f2 u = cfma(l16, t, sg[s]); sg[s] = t; t = u; }
      const u4 v = {__float_as_uint(t.x), tag, __float_as_uint(t.y), tag};
      st128(v, rS, (lane < 24 && c < 15) ? 65536 + (j*4 + w)*6144 + (15 - c)*384 + lane*16 : OOB, 0, SC1); }
    const rsrc_t rK = mk(ws + 65536 + (j*4 + w)*6144 + (16 - c)*384, c*384);
    const unsigned kv0 = kk*384 + pp*16, kv1 = kv0 + 3456;
    const f2 l2 = csq(l128), l3c = cmul(l2, l128), lk = kk == 0 ? f2{1.f, 0.f} : kk == 1 ? l128 : l2;
    const int need = (int)(c + 2 - kk)/3;
    u4 cy[5];
    if (lane < 20) { LDS(f2, 4608 + pl*8) = f2{0.f, 0.f}; _Pragma("unroll") for (int s = 1; s < 8; ++s) LDS(f2, 4608 + s*640 + pl*8) = sg[s]; }
    float pa[5], pb[5];
    _Pragma("unroll") for (int n = 0; n < 5; ++n) { const f2 q1 = LDS(f2, 3200 + (40*wn + 8*n + (r16 >> 1))*8), q2 = csq(q1), q4 = csq(q2);
      f2 t = (g & 1) ? q1 : f2{1.f, 0.f}; t = (g & 2) ? cmul(t, q2) : t; t = wm ? cmul(t, q4) : t; pa[n] = t.x; pb[n] = (r16 & 1) ? t.y : -t.y; }
    asm volatile("" : "+v"(pa[0]), "+v"(pa[1]), "+v"(pa[2]), "+v"(pa[3]), "+v"(pa[4]), "+v"(pb[0]), "+v"(pb[1]), "+v"(pb[2]), "+v"(pb[3]), "+v"(pb[4]));
    _Pragma("nounroll") for (int spin = 0; spin < SPIN_CAP; ++spin) {
      asm volatile("buffer_load_dwordx4 %0, %1, %2, 0 offen offset:0 sc1" : "=v"(cy[0]) : "v"(kv0), "s"(rK) : "memory");
      asm volatile("buffer_load_dwordx4 %0, %1, %2, 0 offen offset:1152 sc1" : "=v"(cy[1]) : "v"(kv0), "s"(rK) : "memory");
      asm volatile("buffer_load_dwordx4 %0, %1, %2, 0 offen offset:2304 sc1" : "=v"(cy[2]) : "v"(kv0), "s"(rK) : "memory");
      asm volatile("buffer_load_dwordx4 %0, %1, %2, 0 offen offset:0 sc1" : "=v"(cy[3]) : "v"(kv1), "s"(rK) : "memory");
      asm volatile("buffer_load_dwordx4 %0, %1, %2, 0 offen offset:1152 sc1" : "=v"(cy[4]) : "v"(kv1), "s"(rK) : "memory");
      asm volatile("s_waitcnt vmcnt(0)" : "+v"(cy[0]), "+v"(cy[1]), "+v"(cy[2]), "+v"(cy[3]), "+v"(cy[4]) :: "memory");
      if (__all((cy[0].y == tag && cy[0].w == tag) + (cy[1].y == tag && cy[1].w == tag) + (cy[2].y == tag && cy[2].w == tag) + (cy[3].y == tag && cy[3].w == tag) + (cy[4].y == tag && cy[4].w == tag) == need)) break;
    }
    sw = swb + (ksa + 3 + wm*(3u))*40960;
    asm volatile("" : "+s"(sw));
    asm volatile("s_add_u32 m0, %0, 92160" :: "s"(lw2) : "scc", "memory");
    asm volatile("s_nop 0");
    asm volatile("buffer_load_dwordx4 %0, %1, %2 offen offset:0 sc1 lds" :: "v"(vl16), "s"(rS), "s"(sw) : "memory");
    asm volatile("buffer_load_dwordx4 %0, %1, %2 offen offset:1024 sc1 lds" :: "v"(vl16), "s"(rS), "s"(sw) : "memory");
    asm volatile("buffer_load_dwordx4 %0, %1, %2 offen offset:2048 sc1 lds" :: "v"(vl16), "s"(rS), "s"(sw) : "memory");
    asm volatile("buffer_load_dwordx4 %0, %1, %2 offen offset:3072 sc1 lds" :: "v"(vl16), "s"(rS), "s"(sw) : "memory");
    asm volatile("s_addk_i32 m0, 0x1000" ::: "scc", "memory");
    asm volatile("s_nop 0");
    asm volatile("buffer_load_dwordx4 %0, %1, %2 offen offset:0 sc1 lds" :: "v"(vl16h), "s"(rS), "s"(sw) : "memory");
    { f2 t = {__uint_as_float(cy[4].x), __uint_as_float(cy[4].z)};
      _Pragma("unroll") for (int k = 3; k >= 0; --k) t = cfma(l3c, t, f2{__uint_as_float(cy[k].x), __uint_as_float(cy[k].z)});
      t = cmul(lk, t);
      const int a1 = 4*(lane + 20), a2 = 4*(lane + 40);
      t.x += shfl(a1, t.x) + shfl(a2, t.x); t.y += shfl(a1, t.y) + shfl(a2, t.y);
      if (lane < 20) LDS(f2, 3840 + pl*8) = t; }
    asm volatile("s_waitcnt lgkmcnt(0)\n\ts_barrier" ::: "memory");
    _Pragma("unroll") for (int n = 0; n < 5; ++n) { const f2 hc = LDS(f2, 3840 + (40*wn + 8*n + (r16 >> 1))*8);
      en[n] = LDS(float, 4608 + (4*wm + g)*640 + (80*wn + 16*n + r16)*4) + pa[n]*((r16 & 1) ? hc.y : hc.x) + pb[n]*((r16 & 1) ? hc.x : hc.y); }
    float hv[5][16];
    _Pragma("unroll") for (int n = 0; n < 5; ++n) _Pragma("unroll") for (int i = 0; i < 16; ++i) hv[n][i] = acc[i >> 2][n][i & 3];
    const unsigned hg0 = (4*wm + g)*1024 + r16*16, hg1 = (4*wm + g)*1024 + 512 + (r16 ^ 4)*16, hso = 4194304 + (c*88 + kso)*8192;
    unsigned hl[2][2];
    { const unsigned a = lds0 + 12288 + (4*wm + ((g - kq) & 3))*1024;
      hl[0][0] = a + r16*16 + wn*16640; hl[0][1] = a + r16*16 + (wn ? 24576 : 256);
      hl[1][0] = a + 512 + (r16 ^ 4)*16 + wn*16640; hl[1][1] = a + 512 + (r16 ^ 4)*16 + (wn ? 24576 : 256); }
    asm volatile("" : "+v"(en[0]), "+v"(en[1]), "+v"(en[2]), "+v"(en[3]), "+v"(en[4]));
    asm volatile("s_nop 1");
    asm volatile("v_fmac_f32 %0, %2, %6\n\tv_fmac_f32 %1, %3, %7\n\tv_fmac_f32_dpp %0, %6, %4 quad_perm:[1,0,3,2] row_mask:0xf bank_mask:0xf\n\tv_fmac_f32_dpp %1, %7, %5 quad_perm:[1,0,3,2] row_mask:0xf bank_mask:0xf" : "+v"(hv[0][0]), "+v"(hv[1][0]) : "v"(la[0]), "v"(la[1]), "v"(lb[0]), "v"(lb[1]), "v"(en[0]), "v"(en[1]));
    sw = swb + (ksa + 9 + wm*(4294967288u))*40960;
    asm volatile("" : "+s"(sw));
    asm volatile("s_add_u32 m0, %0, 71680" :: "s"(lw2) : "scc", "memory");
    asm volatile("s_nop 0");
    asm volatile("buffer_load_dwordx4 %0, %1, %2 offen offset:0 sc1 lds" :: "v"(vl16), "s"(rS), "s"(sw) : "memory");
    asm volatile("v_fmac_f32 %0, %2, %6\n\tv_fmac_f32 %1, %3, %7\n\tv_fmac_f32_dpp %0, %6, %4 quad_perm:[1,0,3,2] row_mask:0xf bank_mask:0xf\n\tv_fmac_f32_dpp %1, %7, %5 quad_perm:[1,0,3,2] row_mask:0xf bank_mask:0xf" : "+v"(hv[0][1]), "+v"(hv[1][1]) : "v"(la[0]), "v"(la[1]), "v"(lb[0]), "v"(lb[1]), "v"(hv[0][0]), "v"(hv[1][0]));
    asm volatile("buffer_load_dwordx4 %0, %1, %2 offen offset:1024 sc1 lds" :: "v"(vl16), "s"(rS), "s"(sw) : "memory");
    asm volatile("v_fmac_f32 %0, %2, %6\n\tv_fmac_f32 %1, %3, %7\n\tv_fmac_f32_dpp %0, %6, %4 quad_perm:[1,0,3,2] row_mask:0xf bank_mask:0xf\n\tv_fmac_f32_dpp %1, %7, %5 quad_perm:[1,0,3,2] row_mask:0xf bank_mask:0xf" : "+v"(hv[0][2]), "+v"(hv[1][2]) : "v"(la[0]), "v"(la[1]), "v"(lb[0]), "v"(lb[1]), "v"(hv[0][1]), "v"(hv[1][1]));
    asm volatile("buffer_load_dword %0, %1, %2, 0 offen sc1" : "=v"(f2v) : "v"(fo), "s"(rS) : "memory");
    asm volatile("v_fmac_f32 %0, %2, %6\n\tv_fmac_f32 %1, %3, %7\n\tv_fmac_f32_dpp %0, %6, %4 quad_perm:[1,0,3,2] row_mask:0xf bank_mask:0xf\n\tv_fmac_f32_dpp %1, %7, %5 quad_perm:[1,0,3,2] row_mask:0xf bank_mask:0xf" : "+v"(hv[0][3]), "+v"(hv[1][3]) : "v"(la[0]), "v"(la[1]), "v"(lb[0]), "v"(lb[1]), "v"(hv[0][2]), "v"(hv[1][2]));
    asm volatile("buffer_load_dwordx4 %0, %1, %2 offen offset:2048 sc1 lds" :: "v"(vl16), "s"(rS), "s"(sw) : "memory");
    asm volatile("v_fmac_f32 %0, %2, %6\n\tv_fmac_f32 %1, %3, %7\n\tv_fmac_f32_dpp %0, %6, %4 quad_perm:[1,0,3,2] row_mask:0xf bank_mask:0xf\n\tv_fmac_f32_dpp %1, %7, %5 quad_perm:[1,0,3,2] row_mask:0xf bank_mask:0xf" : "+v"(hv[0][4]), "+v"(hv[1][4]) : "v"(la[0]), "v"(la[1]), "v"(lb[0]), "v"(lb[1]), "v"(hv[0][3]), "v"(hv[1][3]));
    asm volatile("buffer_load_dwordx4 %0, %1, %2 offen offset:3072 sc1 lds" :: "v"(vl16), "s"(rS), "s"(sw) : "memory");
    asm volatile("v_fmac_f32 %0, %2, %6\n\tv_fmac_f32 %1, %3, %7\n\tv_fmac_f32_dpp %0, %6, %4 quad_perm:[1,0,3,2] row_mask:0xf bank_mask:0xf\n\tv_fmac_f32_dpp %1, %7, %5 quad_perm:[1,0,3,2] row_mask:0xf bank_mask:0xf" : "+v"(hv[0][5]), "+v"(hv[1][5]) : "v"(la[0]), "v"(la[1]), "v"(lb[0]), "v"(lb[1]), "v"(hv[0][4]), "v"(hv[1][4]));
    asm volatile("s_addk_i32 m0, 0x1000" ::: "scc", "memory");
    asm volatile("s_nop 0");
    asm volatile("buffer_load_dwordx4 %0, %1, %2 offen offset:0 sc1 lds" :: "v"(vl16h), "s"(rS), "s"(sw) : "memory");
    asm volatile("v_fmac_f32 %0, %2, %6\n\tv_fmac_f32 %1, %3, %7\n\tv_fmac_f32_dpp %0, %6, %4 quad_perm:[1,0,3,2] row_mask:0xf bank_mask:0xf\n\tv_fmac_f32_dpp %1, %7, %5 quad_perm:[1,0,3,2] row_mask:0xf bank_mask:0xf" : "+v"(hv[0][6]), "+v"(hv[1][6]) : "v"(la[0]), "v"(la[1]), "v"(lb[0]), "v"(lb[1]), "v"(hv[0][5]), "v"(hv[1][5]));
    asm volatile("v_fmac_f32 %0, %2, %6\n\tv_fmac_f32 %1, %3, %7\n\tv_fmac_f32_dpp %0, %6, %4 quad_perm:[1,0,3,2] row_mask:0xf bank_mask:0xf\n\tv_fmac_f32_dpp %1, %7, %5 quad_perm:[1,0,3,2] row_mask:0xf bank_mask:0xf" : "+v"(hv[0][7]), "+v"(hv[1][7]) : "v"(la[0]), "v"(la[1]), "v"(lb[0]), "v"(lb[1]), "v"(hv[0][6]), "v"(hv[1][6]));
    asm volatile("v_fmac_f32 %0, %2, %6\n\tv_fmac_f32 %1, %3, %7\n\tv_fmac_f32_dpp %0, %6, %4 quad_perm:[1,0,3,2] row_mask:0xf bank_mask:0xf\n\tv_fmac_f32_dpp %1, %7, %5 quad_perm:[1,0,3,2] row_mask:0xf bank_mask:0xf" : "+v"(hv[0][8]), "+v"(hv[1][8]) : "v"(la[0]), "v"(la[1]), "v"(lb[0]), "v"(lb[1]), "v"(hv[0][7]), "v"(hv[1][7]));
    { const u4 v = {cvtpk(hv[0][0], hv[0][1]), cvtpk(hv[0][2], hv[0][3]), cvtpk(hv[0][4], hv[0][5]), cvtpk(hv[0][6], hv[0][7])};
      asm volatile("ds_write_b128 %0, %1 offset:0" :: "v"(hl[0][0]), "v"(v) : "memory");
      st128(v, rS, hg0, hso + ((5*wn + 0) >> 1)*8192 + ((wn + 0) & 1)*256, SC1); }
    asm volatile("v_fmac_f32 %0, %2, %6\n\tv_fmac_f32 %1, %3, %7\n\tv_fmac_f32_dpp %0, %6, %4 quad_perm:[1,0,3,2] row_mask:0xf bank_mask:0xf\n\tv_fmac_f32_dpp %1, %7, %5 quad_perm:[1,0,3,2] row_mask:0xf bank_mask:0xf" : "+v"(hv[0][9]), "+v"(hv[1][9]) : "v"(la[0]), "v"(la[1]), "v"(lb[0]), "v"(lb[1]), "v"(hv[0][8]), "v"(hv[1][8]));
    asm volatile("v_fmac_f32 %0, %2, %6\n\tv_fmac_f32 %1, %3, %7\n\tv_fmac_f32_dpp %0, %6, %4 quad_perm:[1,0,3,2] row_mask:0xf bank_mask:0xf\n\tv_fmac_f32_dpp %1, %7, %5 quad_perm:[1,0,3,2] row_mask:0xf bank_mask:0xf" : "+v"(hv[0][10]), "+v"(hv[1][10]) : "v"(la[0]), "v"(la[1]), "v"(lb[0]), "v"(lb[1]), "v"(hv[0][9]), "v"(hv[1][9]));
    { const u4 v = {cvtpk(hv[1][0], hv[1][1]), cvtpk(hv[1][2], hv[1][3]), cvtpk(hv[1][4], hv[1][5]), cvtpk(hv[1][6], hv[1][7])};
      asm volatile("ds_write_b128 %0, %1 offset:0" :: "v"(hl[0][1]), "v"(v) : "memory");
      st128(v, rS, hg0, hso + ((5*wn + 1) >> 1)*8192 + ((wn + 1) & 1)*256, SC1); }
    asm volatile("v_fmac_f32 %0, %2, %6\n\tv_fmac_f32 %1, %3, %7\n\tv_fmac_f32_dpp %0, %6, %4 quad_perm:[1,0,3,2] row_mask:0xf bank_mask:0xf\n\tv_fmac_f32_dpp %1, %7, %5 quad_perm:[1,0,3,2] row_mask:0xf bank_mask:0xf" : "+v"(hv[0][11]), "+v"(hv[1][11]) : "v"(la[0]), "v"(la[1]), "v"(lb[0]), "v"(lb[1]), "v"(hv[0][10]), "v"(hv[1][10]));
    asm volatile("v_fmac_f32 %0, %2, %6\n\tv_fmac_f32 %1, %3, %7\n\tv_fmac_f32_dpp %0, %6, %4 quad_perm:[1,0,3,2] row_mask:0xf bank_mask:0xf\n\tv_fmac_f32_dpp %1, %7, %5 quad_perm:[1,0,3,2] row_mask:0xf bank_mask:0xf" : "+v"(hv[0][12]), "+v"(hv[1][12]) : "v"(la[0]), "v"(la[1]), "v"(lb[0]), "v"(lb[1]), "v"(hv[0][11]), "v"(hv[1][11]));
    asm volatile("v_fmac_f32 %0, %2, %6\n\tv_fmac_f32 %1, %3, %7\n\tv_fmac_f32_dpp %0, %6, %4 quad_perm:[1,0,3,2] row_mask:0xf bank_mask:0xf\n\tv_fmac_f32_dpp %1, %7, %5 quad_perm:[1,0,3,2] row_mask:0xf bank_mask:0xf" : "+v"(hv[0][13]), "+v"(hv[1][13]) : "v"(la[0]), "v"(la[1]), "v"(lb[0]), "v"(lb[1]), "v"(hv[0][12]), "v"(hv[1][12]));
    asm volatile("v_fmac_f32 %0, %2, %6\n\tv_fmac_f32 %1, %3, %7\n\tv_fmac_f32_dpp %0, %6, %4 quad_perm:[1,0,3,2] row_mask:0xf bank_mask:0xf\n\tv_fmac_f32_dpp %1, %7, %5 quad_perm:[1,0,3,2] row_mask:0xf bank_mask:0xf" : "+v"(hv[0][14]), "+v"(hv[1][14]) : "v"(la[0]), "v"(la[1]), "v"(lb[0]), "v"(lb[1]), "v"(hv[0][13]), "v"(hv[1][13]));
    asm volatile("v_fmac_f32 %0, %2, %6\n\tv_fmac_f32 %1, %3, %7\n\tv_fmac_f32_dpp %0, %6, %4 quad_perm:[1,0,3,2] row_mask:0xf bank_mask:0xf\n\tv_fmac_f32_dpp %1, %7, %5 quad_perm:[1,0,3,2] row_mask:0xf bank_mask:0xf" : "+v"(hv[0][15]), "+v"(hv[1][15]) : "v"(la[0]), "v"(la[1]), "v"(lb[0]), "v"(lb[1]), "v"(hv[0][14]), "v"(hv[1][14]));
    asm volatile("v_fmac_f32 %0, %3, %9\n\tv_fmac_f32 %1, %4, %10\n\tv_fmac_f32 %2, %5, %11\n\tv_fmac_f32_dpp %0, %9, %6 quad_perm:[1,0,3,2] row_mask:0xf bank_mask:0xf\n\tv_fmac_f32_dpp %1, %10, %7 quad_perm:[1,0,3,2] row_mask:0xf bank_mask:0xf\n\tv_fmac_f32_dpp %2, %11, %8 quad_perm:[1,0,3,2] row_mask:0xf bank_mask:0xf" : "+v"(hv[2][0]), "+v"(hv[3][0]), "+v"(hv[4][0]) : "v"(la[2]), "v"(la[3]), "v"(la[4]), "v"(lb[2]), "v"(lb[3]), "v"(lb[4]), "v"(en[2]), "v"(en[3]), "v"(en[4]));
    { const u4 v = {cvtpk(hv[0][8], hv[0][9]), cvtpk(hv[0][10], hv[0][11]), cvtpk(hv[0][12], hv[0][13]), cvtpk(hv[0][14], hv[0][15])};
      asm volatile("ds_write_b128 %0, %1 offset:0" :: "v"(hl[1][0]), "v"(v) : "memory");
      st128(v, rS, hg1, hso + ((5*wn + 0) >> 1)*8192 + ((wn + 0) & 1)*256, SC1); }
    asm volatile("v_fmac_f32 %0, %3, %9\n\tv_fmac_f32 %1, %4, %10\n\tv_fmac_f32 %2, %5, %11\n\tv_fmac_f32_dpp %0, %9, %6 quad_perm:[1,0,3,2] row_mask:0xf bank_mask:0xf\n\tv_fmac_f32_dpp %1, %10, %7 quad_perm:[1,0,3,2] row_mask:0xf bank_mask:0xf\n\tv_fmac_f32_dpp %2, %11, %8 quad_perm:[1,0,3,2] row_mask:0xf bank_mask:0xf" : "+v"(hv[2][1]), "+v"(hv[3][1]), "+v"(hv[4][1]) : "v"(la[2]), "v"(la[3]), "v"(la[4]), "v"(lb[2]), "v"(lb[3]), "v"(lb[4]), "v"(hv[2][0]), "v"(hv[3][0]), "v"(hv[4][0]));
    asm volatile("v_fmac_f32 %0, %3, %9\n\tv_fmac_f32 %1, %4, %10\n\tv_fmac_f32 %2, %5, %11\n\tv_fmac_f32_dpp %0, %9, %6 quad_perm:[1,0,3,2] row_mask:0xf bank_mask:0xf\n\tv_fmac_f32_dpp %1, %10, %7 quad_perm:[1,0,3,2] row_mask:0xf bank_mask:0xf\n\tv_fmac_f32_dpp %2, %11, %8 quad_perm:[1,0,3,2] row_mask:0xf bank_mask:0xf" : "+v"(hv[2][2]), "+v"(hv[3][2]), "+v"(hv[4][2]) : "v"(la[2]), "v"(la[3]), "v"(la[4]), "v"(lb[2]), "v"(lb[3]), "v"(lb[4]), "v"(hv[2][1]), "v"(hv[3][1]), "v"(hv[4][1]));
    { const u4 v = {cvtpk(hv[1][8], hv[1][9]), cvtpk(hv[1][10], hv[1][11]), cvtpk(hv[1][12], hv[1][13]), cvtpk(hv[1][14], hv[1][15])};
      asm volatile("ds_write_b128 %0, %1 offset:0" :: "v"(hl[1][1]), "v"(v) : "memory");
      st128(v, rS, hg1, hso + ((5*wn + 1) >> 1)*8192 + ((wn + 1) & 1)*256, SC1); }
    asm volatile("v_fmac_f32 %0, %3, %9\n\tv_fmac_f32 %1, %4, %10\n\tv_fmac_f32 %2, %5, %11\n\tv_fmac_f32_dpp %0, %9, %6 quad_perm:[1,0,3,2] row_mask:0xf bank_mask:0xf\n\tv_fmac_f32_dpp %1, %10, %7 quad_perm:[1,0,3,2] row_mask:0xf bank_mask:0xf\n\tv_fmac_f32_dpp %2, %11, %8 quad_perm:[1,0,3,2] row_mask:0xf bank_mask:0xf" : "+v"(hv[2][3]), "+v"(hv[3][3]), "+v"(hv[4][3]) : "v"(la[2]), "v"(la[3]), "v"(la[4]), "v"(lb[2]), "v"(lb[3]), "v"(lb[4]), "v"(hv[2][2]), "v"(hv[3][2]), "v"(hv[4][2]));
    asm volatile("v_fmac_f32 %0, %3, %9\n\tv_fmac_f32 %1, %4, %10\n\tv_fmac_f32 %2, %5, %11\n\tv_fmac_f32_dpp %0, %9, %6 quad_perm:[1,0,3,2] row_mask:0xf bank_mask:0xf\n\tv_fmac_f32_dpp %1, %10, %7 quad_perm:[1,0,3,2] row_mask:0xf bank_mask:0xf\n\tv_fmac_f32_dpp %2, %11, %8 quad_perm:[1,0,3,2] row_mask:0xf bank_mask:0xf" : "+v"(hv[2][4]), "+v"(hv[3][4]), "+v"(hv[4][4]) : "v"(la[2]), "v"(la[3]), "v"(la[4]), "v"(lb[2]), "v"(lb[3]), "v"(lb[4]), "v"(hv[2][3]), "v"(hv[3][3]), "v"(hv[4][3]));
    asm volatile("v_fmac_f32 %0, %3, %9\n\tv_fmac_f32 %1, %4, %10\n\tv_fmac_f32 %2, %5, %11\n\tv_fmac_f32_dpp %0, %9, %6 quad_perm:[1,0,3,2] row_mask:0xf bank_mask:0xf\n\tv_fmac_f32_dpp %1, %10, %7 quad_perm:[1,0,3,2] row_mask:0xf bank_mask:0xf\n\tv_fmac_f32_dpp %2, %11, %8 quad_perm:[1,0,3,2] row_mask:0xf bank_mask:0xf" : "+v"(hv[2][5]), "+v"(hv[3][5]), "+v"(hv[4][5]) : "v"(la[2]), "v"(la[3]), "v"(la[4]), "v"(lb[2]), "v"(lb[3]), "v"(lb[4]), "v"(hv[2][4]), "v"(hv[3][4]), "v"(hv[4][4]));
    asm volatile("v_fmac_f32 %0, %3, %9\n\tv_fmac_f32 %1, %4, %10\n\tv_fmac_f32 %2, %5, %11\n\tv_fmac_f32_dpp %0, %9, %6 quad_perm:[1,0,3,2] row_mask:0xf bank_mask:0xf\n\tv_fmac_f32_dpp %1, %10, %7 quad_perm:[1,0,3,2] row_mask:0xf bank_mask:0xf\n\tv_fmac_f32_dpp %2, %11, %8 quad_perm:[1,0,3,2] row_mask:0xf bank_mask:0xf" : "+v"(hv[2][6]), "+v"(hv[3][6]), "+v"(hv[4][6]) : "v"(la[2]), "v"(la[3]), "v"(la[4]), "v"(lb[2]), "v"(lb[3]), "v"(lb[4]), "v"(hv[2][5]), "v"(hv[3][5]), "v"(hv[4][5]));
    asm volatile("v_fmac_f32 %0, %3, %9\n\tv_fmac_f32 %1, %4, %10\n\tv_fmac_f32 %2, %5, %11\n\tv_fmac_f32_dpp %0, %9, %6 quad_perm:[1,0,3,2] row_mask:0xf bank_mask:0xf\n\tv_fmac_f32_dpp %1, %10, %7 quad_perm:[1,0,3,2] row_mask:0xf bank_mask:0xf\n\tv_fmac_f32_dpp %2, %11, %8 quad_perm:[1,0,3,2] row_mask:0xf bank_mask:0xf" : "+v"(hv[2][7]), "+v"(hv[3][7]), "+v"(hv[4][7]) : "v"(la[2]), "v"(la[3]), "v"(la[4]), "v"(lb[2]), "v"(lb[3]), "v"(lb[4]), "v"(hv[2][6]), "v"(hv[3][6]), "v"(hv[4][6]));
    asm volatile("v_fmac_f32 %0, %3, %9\n\tv_fmac_f32 %1, %4, %10\n\tv_fmac_f32 %2, %5, %11\n\tv_fmac_f32_dpp %0, %9, %6 quad_perm:[1,0,3,2] row_mask:0xf bank_mask:0xf\n\tv_fmac_f32_dpp %1, %10, %7 quad_perm:[1,0,3,2] row_mask:0xf bank_mask:0xf\n\tv_fmac_f32_dpp %2, %11, %8 quad_perm:[1,0,3,2] row_mask:0xf bank_mask:0xf" : "+v"(hv[2][8]), "+v"(hv[3][8]), "+v"(hv[4][8]) : "v"(la[2]), "v"(la[3]), "v"(la[4]), "v"(lb[2]), "v"(lb[3]), "v"(lb[4]), "v"(hv[2][7]), "v"(hv[3][7]), "v"(hv[4][7]));
    { const u4 v = {cvtpk(hv[2][0], hv[2][1]), cvtpk(hv[2][2], hv[2][3]), cvtpk(hv[2][4], hv[2][5]), cvtpk(hv[2][6], hv[2][7])};
      asm volatile("ds_write_b128 %0, %1 offset:8192" :: "v"(hl[0][0]), "v"(v) : "memory");
      st128(v, rS, hg0, hso + ((5*wn + 2) >> 1)*8192 + ((wn + 2) & 1)*256, SC1); }
    asm volatile("v_fmac_f32 %0, %3, %9\n\tv_fmac_f32 %1, %4, %10\n\tv_fmac_f32 %2, %5, %11\n\tv_fmac_f32_dpp %0, %9, %6 quad_perm:[1,0,3,2] row_mask:0xf bank_mask:0xf\n\tv_fmac_f32_dpp %1, %10, %7 quad_perm:[1,0,3,2] row_mask:0xf bank_mask:0xf\n\tv_fmac_f32_dpp %2, %11, %8 quad_perm:[1,0,3,2] row_mask:0xf bank_mask:0xf" : "+v"(hv[2][9]), "+v"(hv[3][9]), "+v"(hv[4][9]) : "v"(la[2]), "v"(la[3]), "v"(la[4]), "v"(lb[2]), "v"(lb[3]), "v"(lb[4]), "v"(hv[2][8]), "v"(hv[3][8]), "v"(hv[4][8]));
    asm volatile("v_fmac_f32 %0, %3, %9\n\tv_fmac_f32 %1, %4, %10\n\tv_fmac_f32 %2, %5, %11\n\tv_fmac_f32_dpp %0, %9, %6 quad_perm:[1,0,3,2] row_mask:0xf bank_mask:0xf\n\tv_fmac_f32_dpp %1, %10, %7 quad_perm:[1,0,3,2] row_mask:0xf bank_mask:0xf\n\tv_fmac_f32_dpp %2, %11, %8 quad_perm:[1,0,3,2] row_mask:0xf bank_mask:0xf" : "+v"(hv[2][10]), "+v"(hv[3][10]), "+v"(hv[4][10]) : "v"(la[2]), "v"(la[3]), "v"(la[4]), "v"(lb[2]), "v"(lb[3]), "v"(lb[4]), "v"(hv[2][9]), "v"(hv[3][9]), "v"(hv[4][9]));
    { const u4 v = {cvtpk(hv[3][0], hv[3][1]), cvtpk(hv[3][2], hv[3][3]), cvtpk(hv[3][4], hv[3][5]), cvtpk(hv[3][6], hv[3][7])};
      asm volatile("ds_write_b128 %0, %1 offset:8192" :: "v"(hl[0][1]), "v"(v) : "memory");
      st128(v, rS, hg0, hso + ((5*wn + 3) >> 1)*8192 + ((wn + 3) & 1)*256, SC1); }
    asm volatile("v_fmac_f32 %0, %3, %9\n\tv_fmac_f32 %1, %4, %10\n\tv_fmac_f32 %2, %5, %11\n\tv_fmac_f32_dpp %0, %9, %6 quad_perm:[1,0,3,2] row_mask:0xf bank_mask:0xf\n\tv_fmac_f32_dpp %1, %10, %7 quad_perm:[1,0,3,2] row_mask:0xf bank_mask:0xf\n\tv_fmac_f32_dpp %2, %11, %8 quad_perm:[1,0,3,2] row_mask:0xf bank_mask:0xf" : "+v"(hv[2][11]), "+v"(hv[3][11]), "+v"(hv[4][11]) : "v"(la[2]), "v"(la[3]), "v"(la[4]), "v"(lb[2]), "v"(lb[3]), "v"(lb[4]), "v"(hv[2][10]), "v"(hv[3][10]), "v"(hv[4][10]));
    asm volatile("v_fmac_f32 %0, %3, %9\n\tv_fmac_f32 %1, %4, %10\n\tv_fmac_f32 %2, %5, %11\n\tv_fmac_f32_dpp %0, %9, %6 quad_perm:[1,0,3,2] row_mask:0xf bank_mask:0xf\n\tv_fmac_f32_dpp %1, %10, %7 quad_perm:[1,0,3,2] row_mask:0xf bank_mask:0xf\n\tv_fmac_f32_dpp %2, %11, %8 quad_perm:[1,0,3,2] row_mask:0xf bank_mask:0xf" : "+v"(hv[2][12]), "+v"(hv[3][12]), "+v"(hv[4][12]) : "v"(la[2]), "v"(la[3]), "v"(la[4]), "v"(lb[2]), "v"(lb[3]), "v"(lb[4]), "v"(hv[2][11]), "v"(hv[3][11]), "v"(hv[4][11]));
    { const u4 v = {cvtpk(hv[4][0], hv[4][1]), cvtpk(hv[4][2], hv[4][3]), cvtpk(hv[4][4], hv[4][5]), cvtpk(hv[4][6], hv[4][7])};
      asm volatile("ds_write_b128 %0, %1 offset:16384" :: "v"(hl[0][0]), "v"(v) : "memory");
      st128(v, rS, hg0, hso + ((5*wn + 4) >> 1)*8192 + ((wn + 4) & 1)*256, SC1); }
    asm volatile("v_fmac_f32 %0, %3, %9\n\tv_fmac_f32 %1, %4, %10\n\tv_fmac_f32 %2, %5, %11\n\tv_fmac_f32_dpp %0, %9, %6 quad_perm:[1,0,3,2] row_mask:0xf bank_mask:0xf\n\tv_fmac_f32_dpp %1, %10, %7 quad_perm:[1,0,3,2] row_mask:0xf bank_mask:0xf\n\tv_fmac_f32_dpp %2, %11, %8 quad_perm:[1,0,3,2] row_mask:0xf bank_mask:0xf" : "+v"(hv[2][13]), "+v"(hv[3][13]), "+v"(hv[4][13]) : "v"(la[2]), "v"(la[3]), "v"(la[4]), "v"(lb[2]), "v"(lb[3]), "v"(lb[4]), "v"(hv[2][12]), "v"(hv[3][12]), "v"(hv[4][12]));
    asm volatile("v_fmac_f32 %0, %3, %9\n\tv_fmac_f32 %1, %4, %10\n\tv_fmac_f32 %2, %5, %11\n\tv_fmac_f32_dpp %0, %9, %6 quad_perm:[1,0,3,2] row_mask:0xf bank_mask:0xf\n\tv_fmac_f32_dpp %1, %10, %7 quad_perm:[1,0,3,2] row_mask:0xf bank_mask:0xf\n\tv_fmac_f32_dpp %2, %11, %8 quad_perm:[1,0,3,2] row_mask:0xf bank_mask:0xf" : "+v"(hv[2][14]), "+v"(hv[3][14]), "+v"(hv[4][14]) : "v"(la[2]), "v"(la[3]), "v"(la[4]), "v"(lb[2]), "v"(lb[3]), "v"(lb[4]), "v"(hv[2][13]), "v"(hv[3][13]), "v"(hv[4][13]));
    asm volatile("v_fmac_f32 %0, %3, %9\n\tv_fmac_f32 %1, %4, %10\n\tv_fmac_f32 %2, %5, %11\n\tv_fmac_f32_dpp %0, %9, %6 quad_perm:[1,0,3,2] row_mask:0xf bank_mask:0xf\n\tv_fmac_f32_dpp %1, %10, %7 quad_perm:[1,0,3,2] row_mask:0xf bank_mask:0xf\n\tv_fmac_f32_dpp %2, %11, %8 quad_perm:[1,0,3,2] row_mask:0xf bank_mask:0xf" : "+v"(hv[2][15]), "+v"(hv[3][15]), "+v"(hv[4][15]) : "v"(la[2]), "v"(la[3]), "v"(la[4]), "v"(lb[2]), "v"(lb[3]), "v"(lb[4]), "v"(hv[2][14]), "v"(hv[3][14]), "v"(hv[4][14]));
    { const u4 v = {cvtpk(hv[2][8], hv[2][9]), cvtpk(hv[2][10], hv[2][11]), cvtpk(hv[2][12], hv[2][13]), cvtpk(hv[2][14], hv[2][15])};
      asm volatile("ds_write_b128 %0, %1 offset:8192" :: "v"(hl[1][0]), "v"(v) : "memory");
      st128(v, rS, hg1, hso + ((5*wn + 2) >> 1)*8192 + ((wn + 2) & 1)*256, SC1); }
    { const u4 v = {cvtpk(hv[3][8], hv[3][9]), cvtpk(hv[3][10], hv[3][11]), cvtpk(hv[3][12], hv[3][13]), cvtpk(hv[3][14], hv[3][15])};
      asm volatile("ds_write_b128 %0, %1 offset:8192" :: "v"(hl[1][1]), "v"(v) : "memory");
      st128(v, rS, hg1, hso + ((5*wn + 3) >> 1)*8192 + ((wn + 3) & 1)*256, SC1); }
    { const u4 v = {cvtpk(hv[4][8], hv[4][9]), cvtpk(hv[4][10], hv[4][11]), cvtpk(hv[4][12], hv[4][13]), cvtpk(hv[4][14], hv[4][15])};
      asm volatile("ds_write_b128 %0, %1 offset:16384" :: "v"(hl[1][0]), "v"(v) : "memory");
      st128(v, rS, hg1, hso + ((5*wn + 4) >> 1)*8192 + ((wn + 4) & 1)*256, SC1); }
    asm volatile("s_waitcnt vmcnt(13)" ::: "memory");
    asm volatile("" : "+v"(f2v));
    const bool ok0 = __all(flev > 0 || f2v == tag);
    if (ok0) {
    sh = 4194304 + (c*88 + ksa + 0)*8192;
    asm volatile("" : "+s"(sh));
    asm volatile("s_add_u32 m0, %0, 53248" :: "s"(lh) : "scc", "memory");
    asm volatile("s_nop 0");
    asm volatile("buffer_load_dwordx4 %0, %1, %2 offen offset:0 sc1 lds" :: "v"(hv0), "s"(rS), "s"(sh) : "memory");
    asm volatile("s_addk_i32 m0, 0x400" ::: "scc", "memory");
    asm volatile("s_nop 0");
    asm volatile("buffer_load_dwordx4 %0, %1, %2 offen offset:0 sc1 lds" :: "v"(hv1), "s"(rS), "s"(sh) : "memory");
    }
    __builtin_amdgcn_sched_barrier(0);
    f4 o[5][4]; u4 HX0[4], HX1[4], WX0[5], WX1[5]; u2 tl[4], th[4];
    const unsigned hb = lds0 + 12288 + wm*4096 + vl16, wlo = lds0 + 61440 + wn*5120 + vl16, whi = wlo + 51200;
    unsigned ht[2]; _Pragma("unroll") for (int t = 0; t < 2; ++t) { const unsigned h = (r16 >> 1) & 1;
      ht[t] = lds0 + 12288 + wm*4096 + (32*h + ((8*g + 4*t + (r16 >> 2)) ^ (4*h)))*16 + 8*(r16 & 1); }
    __builtin_amdgcn_sched_barrier(0);
    u4 WA[22][5], HK0[22], HK1[22];
    u2 ex[5], es; const unsigned tok = 128*c + 64*wm + 16*kq + r16, xvo = tok*1280 + (160*cq + 80*wn + 4*g)*2;
    asm volatile("s_waitcnt lgkmcnt(0)\n\ts_barrier" ::: "memory");
    asm volatile("ds_read_b64_tr_b16 %0, %1 offset:3072" : "=v"(tl[3]) : "v"(ht[0]) : "memory");
    asm volatile("ds_read_b64_tr_b16 %0, %1 offset:3072" : "=v"(th[3]) : "v"(ht[1]) : "memory");
    HX0[3] = cat(tl[3], th[3]);
    asm volatile("ds_read_b64_tr_b16 %0, %1 offset:2048" : "=v"(tl[2]) : "v"(ht[0]) : "memory");
    asm volatile("ds_read_b64_tr_b16 %0, %1 offset:2048" : "=v"(th[2]) : "v"(ht[1]) : "memory");
    HX0[2] = cat(tl[2], th[2]);
    asm volatile("ds_read_b128 %0, %1 offset:40960" : "=v"(WX0[0]) : "v"(whi) : "memory");
    asm volatile("ds_read_b128 %0, %1 offset:41984" : "=v"(WX0[1]) : "v"(whi) : "memory");
    asm volatile("ds_read_b128 %0, %1 offset:43008" : "=v"(WX0[2]) : "v"(whi) : "memory");
    asm volatile("ds_read_b128 %0, %1 offset:44032" : "=v"(WX0[3]) : "v"(whi) : "memory");
    asm volatile("ds_read_b128 %0, %1 offset:45056" : "=v"(WX0[4]) : "v"(whi) : "memory");
    asm volatile("ds_read_b64_tr_b16 %0, %1 offset:1024" : "=v"(tl[1]) : "v"(ht[0]) : "memory");
    asm volatile("ds_read_b64_tr_b16 %0, %1 offset:1024" : "=v"(th[1]) : "v"(ht[1]) : "memory");
    HX0[1] = cat(tl[1], th[1]);
    asm volatile("ds_read_b64_tr_b16 %0, %1 offset:0" : "=v"(tl[0]) : "v"(ht[0]) : "memory");
    asm volatile("ds_read_b64_tr_b16 %0, %1 offset:0" : "=v"(th[0]) : "v"(ht[1]) : "memory");
    HX0[0] = cat(tl[0], th[0]);
    asm volatile("s_waitcnt lgkmcnt(0)" ::: "memory");
    asm volatile("v_mfma_f32_16x16x32_bf16 %0, %1, %2, 0" : "=&v"(o[0][3]) : "v"(WX0[0]), "v"(HX0[3]));
    asm volatile("ds_read_b64_tr_b16 %0, %1 offset:11264" : "=v"(tl[3]) : "v"(ht[0]) : "memory");
    asm volatile("v_mfma_f32_16x16x32_bf16 %0, %1, %2, 0" : "=&v"(o[0][2]) : "v"(WX0[0]), "v"(HX0[2]));
    asm volatile("ds_read_b64_tr_b16 %0, %1 offset:11264" : "=v"(th[3]) : "v"(ht[1]) : "memory");
    HX1[3] = cat(tl[3], th[3]);
    asm volatile("v_mfma_f32_16x16x32_bf16 %0, %1, %2, 0" : "=&v"(o[0][1]) : "v"(WX0[0]), "v"(HX0[1]));
    asm volatile("ds_read_b64_tr_b16 %0, %1 offset:10240" : "=v"(tl[2]) : "v"(ht[0]) : "memory");
    asm volatile("v_mfma_f32_16x16x32_bf16 %0, %1, %2, 0" : "=&v"(o[0][0]) : "v"(WX0[0]), "v"(HX0[0]));
    asm volatile("ds_read_b64_tr_b16 %0, %1 offset:10240" : "=v"(th[2]) : "v"(ht[1]) : "memory");
    HX1[2] = cat(tl[2], th[2]);
    asm volatile("v_mfma_f32_16x16x32_bf16 %0, %1, %2, 0" : "=&v"(o[1][3]) : "v"(WX0[1]), "v"(HX0[3]));
    asm volatile("ds_read_b128 %0, %1 offset:30720" : "=v"(WX1[0]) : "v"(whi) : "memory");
    asm volatile("v_mfma_f32_16x16x32_bf16 %0, %1, %2, 0" : "=&v"(o[1][2]) : "v"(WX0[1]), "v"(HX0[2]));
    asm volatile("ds_read_b128 %0, %1 offset:31744" : "=v"(WX1[1]) : "v"(whi) : "memory");
    asm volatile("v_mfma_f32_16x16x32_bf16 %0, %1, %2, 0" : "=&v"(o[1][1]) : "v"(WX0[1]), "v"(HX0[1]));
    asm volatile("ds_read_b128 %0, %1 offset:32768" : "=v"(WX1[2]) : "v"(whi) : "memory");
    asm volatile("v_mfma_f32_16x16x32_bf16 %0, %1, %2, 0" : "=&v"(o[1][0]) : "v"(WX0[1]), "v"(HX0[0]));
    asm volatile("ds_read_b128 %0, %1 offset:33792" : "=v"(WX1[3]) : "v"(whi) : "memory");
    asm volatile("v_mfma_f32_16x16x32_bf16 %0, %1, %2, 0" : "=&v"(o[2][3]) : "v"(WX0[2]), "v"(HX0[3]));
    asm volatile("ds_read_b128 %0, %1 offset:34816" : "=v"(WX1[4]) : "v"(whi) : "memory");
    asm volatile("v_mfma_f32_16x16x32_bf16 %0, %1, %2, 0" : "=&v"(o[2][2]) : "v"(WX0[2]), "v"(HX0[2]));
    asm volatile("ds_read_b64_tr_b16 %0, %1 offset:9216" : "=v"(tl[1]) : "v"(ht[0]) : "memory");
    asm volatile("v_mfma_f32_16x16x32_bf16 %0, %1, %2, 0" : "=&v"(o[2][1]) : "v"(WX0[2]), "v"(HX0[1]));
    asm volatile("ds_read_b64_tr_b16 %0, %1 offset:9216" : "=v"(th[1]) : "v"(ht[1]) : "memory");
    HX1[1] = cat(tl[1], th[1]);
    asm volatile("v_mfma_f32_16x16x32_bf16 %0, %1, %2, 0" : "=&v"(o[2][0]) : "v"(WX0[2]), "v"(HX0[0]));
    asm volatile("ds_read_b64_tr_b16 %0, %1 offset:8192" : "=v"(tl[0]) : "v"(ht[0]) : "memory");
    asm volatile("v_mfma_f32_16x16x32_bf16 %0, %1, %2, 0" : "=&v"(o[3][3]) : "v"(WX0[3]), "v"(HX0[3]));
    asm volatile("ds_read_b64_tr_b16 %0, %1 offset:8192" : "=v"(th[0]) : "v"(ht[1]) : "memory");
    HX1[0] = cat(tl[0], th[0]);
    asm volatile("v_mfma_f32_16x16x32_bf16 %0, %1, %2, 0" : "=&v"(o[3][2]) : "v"(WX0[3]), "v"(HX0[2]));
    asm volatile("v_mfma_f32_16x16x32_bf16 %0, %1, %2, 0" : "=&v"(o[3][1]) : "v"(WX0[3]), "v"(HX0[1]));
    asm volatile("v_mfma_f32_16x16x32_bf16 %0, %1, %2, 0" : "=&v"(o[3][0]) : "v"(WX0[3]), "v"(HX0[0]));
    asm volatile("v_mfma_f32_16x16x32_bf16 %0, %1, %2, 0" : "=&v"(o[4][3]) : "v"(WX0[4]), "v"(HX0[3]));
    asm volatile("v_mfma_f32_16x16x32_bf16 %0, %1, %2, 0" : "=&v"(o[4][2]) : "v"(WX0[4]), "v"(HX0[2]));
    asm volatile("v_mfma_f32_16x16x32_bf16 %0, %1, %2, 0" : "=&v"(o[4][1]) : "v"(WX0[4]), "v"(HX0[1]));
    asm volatile("v_mfma_f32_16x16x32_bf16 %0, %1, %2, 0" : "=&v"(o[4][0]) : "v"(WX0[4]), "v"(HX0[0]));
    asm volatile("s_waitcnt lgkmcnt(0)\n\ts_barrier" ::: "memory");
    if (!ok0) { spin1(rS, fo, flev > 0, tag);
    sh = 4194304 + (c*88 + ksa + 0)*8192;
    asm volatile("" : "+s"(sh));
    asm volatile("s_add_u32 m0, %0, 53248" :: "s"(lh) : "scc", "memory");
    asm volatile("s_nop 0");
    asm volatile("buffer_load_dwordx4 %0, %1, %2 offen offset:0 sc1 lds" :: "v"(hv0), "s"(rS), "s"(sh) : "memory");
    asm volatile("s_addk_i32 m0, 0x400" ::: "scc", "memory");
    asm volatile("s_nop 0");
    asm volatile("buffer_load_dwordx4 %0, %1, %2 offen offset:0 sc1 lds" :: "v"(hv1), "s"(rS), "s"(sh) : "memory");
    }
    __builtin_amdgcn_sched_barrier(0);
    asm volatile("v_mfma_f32_16x16x32_bf16 %0, %1, %2, %0" : "+v"(o[0][3]) : "v"(WX1[0]), "v"(HX1[3]));
    asm volatile("ds_read_b64_tr_b16 %0, %1 offset:19456" : "=v"(tl[3]) : "v"(ht[0]) : "memory");
    sh = 4194304 + (c*88 + ksa + 3)*8192;
    asm volatile("" : "+s"(sh));
    asm volatile("s_add_u32 m0, %0, 12288" :: "s"(lh) : "scc", "memory");
    asm volatile("v_mfma_f32_16x16x32_bf16 %0, %1, %2, %0" : "+v"(o[0][2]) : "v"(WX1[0]), "v"(HX1[2]));
    asm volatile("ds_read_b64_tr_b16 %0, %1 offset:19456" : "=v"(th[3]) : "v"(ht[1]) : "memory");
    HX0[3] = cat(tl[3], th[3]);
    asm volatile("buffer_load_dwordx4 %0, %1, %2 offen offset:0 sc1 lds" :: "v"(hv0), "s"(rS), "s"(sh) : "memory");
    asm volatile("v_mfma_f32_16x16x32_bf16 %0, %1, %2, %0" : "+v"(o[0][1]) : "v"(WX1[0]), "v"(HX1[1]));
    asm volatile("ds_read_b64_tr_b16 %0, %1 offset:18432" : "=v"(tl[2]) : "v"(ht[0]) : "memory");
    asm volatile("v_mfma_f32_16x16x32_bf16 %0, %1, %2, %0" : "+v"(o[0][0]) : "v"(WX1[0]), "v"(HX1[0]));
    asm volatile("ds_read_b64_tr_b16 %0, %1 offset:18432" : "=v"(th[2]) : "v"(ht[1]) : "memory");
    HX0[2] = cat(tl[2], th[2]);
    asm volatile("s_addk_i32 m0, 0x400" ::: "scc", "memory");
    asm volatile("v_mfma_f32_16x16x32_bf16 %0, %1, %2, %0" : "+v"(o[1][3]) : "v"(WX1[1]), "v"(HX1[3]));
    asm volatile("ds_read_b128 %0, %1 offset:20480" : "=v"(WX0[0]) : "v"(whi) : "memory");
    asm volatile("buffer_load_dwordx4 %0, %1, %2 offen offset:0 sc1 lds" :: "v"(hv1), "s"(rS), "s"(sh) : "memory");
    asm volatile("v_mfma_f32_16x16x32_bf16 %0, %1, %2, %0" : "+v"(o[1][2]) : "v"(WX1[1]), "v"(HX1[2]));
    asm volatile("ds_read_b128 %0, %1 offset:21504" : "=v"(WX0[1]) : "v"(whi) : "memory");
    asm volatile("v_mfma_f32_16x16x32_bf16 %0, %1, %2, %0" : "+v"(o[1][1]) : "v"(WX1[1]), "v"(HX1[1]));
    asm volatile("ds_read_b128 %0, %1 offset:22528" : "=v"(WX0[2]) : "v"(whi) : "memory");
    asm volatile("buffer_load_dword %0, %1, %2, 0 offen sc1" : "=v"(f2v) : "v"(fo), "s"(rS) : "memory");
    asm volatile("v_mfma_f32_16x16x32_bf16 %0, %1, %2, %0" : "+v"(o[1][0]) : "v"(WX1[1]), "v"(HX1[0]));
    asm volatile("ds_read_b128 %0, %1 offset:23552" : "=v"(WX0[3]) : "v"(whi) : "memory");
    sh = 4194304 + (c*88 + ksa + 6)*8192;
    asm volatile("" : "+s"(sh));
    asm volatile("s_add_u32 m0, %0, 20480" :: "s"(lh) : "scc", "memory");
    asm volatile("v_mfma_f32_16x16x32_bf16 %0, %1, %2, %0" : "+v"(o[2][3]) : "v"(WX1[2]), "v"(HX1[3]));
    asm volatile("ds_read_b128 %0, %1 offset:24576" : "=v"(WX0[4]) : "v"(whi) : "memory");
    asm volatile("buffer_load_dwordx4 %0, %1, %2 offen offset:0 sc1 lds" :: "v"(hv0), "s"(rS), "s"(sh) : "memory");
    asm volatile("v_mfma_f32_16x16x32_bf16 %0, %1, %2, %0" : "+v"(o[2][2]) : "v"(WX1[2]), "v"(HX1[2]));
    asm volatile("ds_read_b64_tr_b16 %0, %1 offset:17408" : "=v"(tl[1]) : "v"(ht[0]) : "memory");
    asm volatile("v_mfma_f32_16x16x32_bf16 %0, %1, %2, %0" : "+v"(o[2][1]) : "v"(WX1[2]), "v"(HX1[1]));
    asm volatile("ds_read_b64_tr_b16 %0, %1 offset:17408" : "=v"(th[1]) : "v"(ht[1]) : "memory");
    HX0[1] = cat(tl[1], th[1]);
    asm volatile("s_addk_i32 m0, 0x400" ::: "scc", "memory");
    asm volatile("v_mfma_f32_16x16x32_bf16 %0, %1, %2, %0" : "+v"(o[2][0]) : "v"(WX1[2]), "v"(HX1[0]));
    asm volatile("ds_read_b64_tr_b16 %0, %1 offset:16384" : "=v"(tl[0]) : "v"(ht[0]) : "memory");
    asm volatile("buffer_load_dwordx4 %0, %1, %2 offen offset:0 sc1 lds" :: "v"(hv1), "s"(rS), "s"(sh) : "memory");
    asm volatile("v_mfma_f32_16x16x32_bf16 %0, %1, %2, %0" : "+v"(o[3][3]) : "v"(WX1[3]), "v"(HX1[3]));
    asm volatile("ds_read_b64_tr_b16 %0, %1 offset:16384" : "=v"(th[0]) : "v"(ht[1]) : "memory");
    HX0[0] = cat(tl[0], th[0]);
    asm volatile("v_mfma_f32_16x16x32_bf16 %0, %1, %2, %0" : "+v"(o[3][2]) : "v"(WX1[3]), "v"(HX1[2]));
    sw = swb + (ksa + 4 + wm*(3u))*40960;
    asm volatile("" : "+s"(sw));
    asm volatile("s_add_u32 m0, %0, 153600" :: "s"(lw2) : "scc", "memory");
    asm volatile("v_mfma_f32_16x16x32_bf16 %0, %1, %2, %0" : "+v"(o[3][1]) : "v"(WX1[3]), "v"(HX1[1]));
    asm volatile("buffer_load_dwordx4 %0, %1, %2 offen offset:0 sc1 lds" :: "v"(vl16), "s"(rS), "s"(sw) : "memory");
    asm volatile("v_mfma_f32_16x16x32_bf16 %0, %1, %2, %0" : "+v"(o[3][0]) : "v"(WX1[3]), "v"(HX1[0]));
    asm volatile("v_mfma_f32_16x16x32_bf16 %0, %1, %2, %0" : "+v"(o[4][3]) : "v"(WX1[4]), "v"(HX1[3]));
    asm volatile("v_mfma_f32_16x16x32_bf16 %0, %1, %2, %0" : "+v"(o[4][2]) : "v"(WX1[4]), "v"(HX1[2]));
    asm volatile("buffer_load_dwordx4 %0, %1, %2 offen offset:1024 sc1 lds" :: "v"(vl16), "s"(rS), "s"(sw) : "memory");
    asm volatile("v_mfma_f32_16x16x32_bf16 %0, %1, %2, %0" : "+v"(o[4][1]) : "v"(WX1[4]), "v"(HX1[1]));
    asm volatile("v_mfma_f32_16x16x32_bf16 %0, %1, %2, %0" : "+v"(o[4][0]) : "v"(WX1[4]), "v"(HX1[0]));
    asm volatile("s_waitcnt lgkmcnt(0)" ::: "memory");
    asm volatile("v_mfma_f32_16x16x32_bf16 %0, %1, %2, %0" : "+v"(o[0][3]) : "v"(WX0[0]), "v"(HX0[3]));
    asm volatile("ds_read_b64_tr_b16 %0, %1 offset:27648" : "=v"(tl[3]) : "v"(ht[0]) : "memory");
    asm volatile("buffer_load_dwordx4 %0, %1, %2 offen offset:2048 sc1 lds" :: "v"(vl16), "s"(rS), "s"(sw) : "memory");
    asm volatile("v_mfma_f32_16x16x32_bf16 %0, %1, %2, %0" : "+v"(o[0][2]) : "v"(WX0[0]), "v"(HX0[2]));
    asm volatile("ds_read_b64_tr_b16 %0, %1 offset:27648" : "=v"(th[3]) : "v"(ht[1]) : "memory");
    HX1[3] = cat(tl[3], th[3]);
    asm volatile("v_mfma_f32_16x16x32_bf16 %0, %1, %2, %0" : "+v"(o[0][1]) : "v"(WX0[0]), "v"(HX0[1]));
    asm volatile("ds_read_b64_tr_b16 %0, %1 offset:26624" : "=v"(tl[2]) : "v"(ht[0]) : "memory");
    asm volatile("v_mfma_f32_16x16x32_bf16 %0, %1, %2, %0" : "+v"(o[0][0]) : "v"(WX0[0]), "v"(HX0[0]));
    asm volatile("ds_read_b64_tr_b16 %0, %1 offset:26624" : "=v"(th[2]) : "v"(ht[1]) : "memory");
    HX1[2] = cat(tl[2], th[2]);
    asm volatile("buffer_load_dwordx4 %0, %1, %2 offen offset:3072 sc1 lds" :: "v"(vl16), "s"(rS), "s"(sw) : "memory");
    asm volatile("v_mfma_f32_16x16x32_bf16 %0, %1, %2, %0" : "+v"(o[1][3]) : "v"(WX0[1]), "v"(HX0[3]));
    asm volatile("ds_read_b128 %0, %1 offset:10240" : "=v"(WX1[0]) : "v"(whi) : "memory");
    asm volatile("v_mfma_f32_16x16x32_bf16 %0, %1, %2, %0" : "+v"(o[1][2]) : "v"(WX0[1]), "v"(HX0[2]));
    asm volatile("ds_read_b128 %0, %1 offset:11264" : "=v"(WX1[1]) : "v"(whi) : "memory");
    asm volatile("s_addk_i32 m0, 0x1000" ::: "scc", "memory");
    asm volatile("v_mfma_f32_16x16x32_bf16 %0, %1, %2, %0" : "+v"(o[1][1]) : "v"(WX0[1]), "v"(HX0[1]));
    asm volatile("ds_read_b128 %0, %1 offset:12288" : "=v"(WX1[2]) : "v"(whi) : "memory");
    asm volatile("buffer_load_dwordx4 %0, %1, %2 offen offset:0 sc1 lds" :: "v"(vl16h), "s"(rS), "s"(sw) : "memory");
    asm volatile("v_mfma_f32_16x16x32_bf16 %0, %1, %2, %0" : "+v"(o[1][0]) : "v"(WX0[1]), "v"(HX0[0]));
    asm volatile("ds_read_b128 %0, %1 offset:13312" : "=v"(WX1[3]) : "v"(whi) : "memory");
    asm volatile("v_mfma_f32_16x16x32_bf16 %0, %1, %2, %0" : "+v"(o[2][3]) : "v"(WX0[2]), "v"(HX0[3]));
    asm volatile("ds_read_b128 %0, %1 offset:14336" : "=v"(WX1[4]) : "v"(whi) : "memory");
    asm volatile("v_mfma_f32_16x16x32_bf16 %0, %1, %2, %0" : "+v"(o[2][2]) : "v"(WX0[2]), "v"(HX0[2]));
    asm volatile("ds_read_b64_tr_b16 %0, %1 offset:25600" : "=v"(tl[1]) : "v"(ht[0]) : "memory");
    asm volatile("v_mfma_f32_16x16x32_bf16 %0, %1, %2, %0" : "+v"(o[2][1]) : "v"(WX0[2]), "v"(HX0[1]));
    asm volatile("ds_read_b64_tr_b16 %0, %1 offset:25600" : "=v"(th[1]) : "v"(ht[1]) : "memory");
    HX1[1] = cat(tl[1], th[1]);
    asm volatile("v_mfma_f32_16x16x32_bf16 %0, %1, %2, %0" : "+v"(o[2][0]) : "v"(WX0[2]), "v"(HX0[0]));
    asm volatile("ds_read_b64_tr_b16 %0, %1 offset:24576" : "=v"(tl[0]) : "v"(ht[0]) : "memory");
    asm volatile("v_mfma_f32_16x16x32_bf16 %0, %1, %2, %0" : "+v"(o[3][3]) : "v"(WX0[3]), "v"(HX0[3]));
    asm volatile("ds_read_b64_tr_b16 %0, %1 offset:24576" : "=v"(th[0]) : "v"(ht[1]) : "memory");
    HX1[0] = cat(tl[0], th[0]);
    asm volatile("v_mfma_f32_16x16x32_bf16 %0, %1, %2, %0" : "+v"(o[3][2]) : "v"(WX0[3]), "v"(HX0[2]));
    asm volatile("v_mfma_f32_16x16x32_bf16 %0, %1, %2, %0" : "+v"(o[3][1]) : "v"(WX0[3]), "v"(HX0[1]));
    asm volatile("v_mfma_f32_16x16x32_bf16 %0, %1, %2, %0" : "+v"(o[3][0]) : "v"(WX0[3]), "v"(HX0[0]));
    asm volatile("v_mfma_f32_16x16x32_bf16 %0, %1, %2, %0" : "+v"(o[4][3]) : "v"(WX0[4]), "v"(HX0[3]));
    asm volatile("v_mfma_f32_16x16x32_bf16 %0, %1, %2, %0" : "+v"(o[4][2]) : "v"(WX0[4]), "v"(HX0[2]));
    asm volatile("v_mfma_f32_16x16x32_bf16 %0, %1, %2, %0" : "+v"(o[4][1]) : "v"(WX0[4]), "v"(HX0[1]));
    asm volatile("v_mfma_f32_16x16x32_bf16 %0, %1, %2, %0" : "+v"(o[4][0]) : "v"(WX0[4]), "v"(HX0[0]));
    asm volatile("s_waitcnt vmcnt(10) lgkmcnt(0)\n\ts_barrier" ::: "memory");
    __builtin_amdgcn_raw_buffer_store_b32(tag, rS, tid == 0 ? 4096 + (c*16 + 0 + j)*128 + 0 : OOB, 0, SC1);
    __builtin_amdgcn_sched_barrier(0);
    asm volatile("s_waitcnt vmcnt(8)" ::: "memory");
    asm volatile("" : "+v"(f2v));
    if (!__all(flev > 1 || fown || f2v == tag)) spin1(rS, fo, flev > 1 || fown, tag);
    __builtin_amdgcn_sched_barrier(0);
    asm volatile("v_mfma_f32_16x16x32_bf16 %0, %1, %2, %0" : "+v"(o[0][3]) : "v"(WX1[0]), "v"(HX1[3]));
    asm volatile("ds_read_b64_tr_b16 %0, %1 offset:35840" : "=v"(tl[3]) : "v"(ht[0]) : "memory");
    sh = 4194304 + (c*88 + ksa + 9)*8192;
    asm volatile("" : "+s"(sh));
    asm volatile("s_add_u32 m0, %0, 28672" :: "s"(lh) : "scc", "memory");
    asm volatile("v_mfma_f32_16x16x32_bf16 %0, %1, %2, %0" : "+v"(o[0][2]) : "v"(WX1[0]), "v"(HX1[2]));
    asm volatile("ds_read_b64_tr_b16 %0, %1 offset:35840" : "=v"(th[3]) : "v"(ht[1]) : "memory");
    HX0[3] = cat(tl[3], th[3]);
    asm volatile("buffer_load_dwordx4 %0, %1, %2 offen offset:0 sc1 lds" :: "v"(hv0), "s"(rS), "s"(sh) : "memory");
    asm volatile("v_mfma_f32_16x16x32_bf16 %0, %1, %2, %0" : "+v"(o[0][1]) : "v"(WX1[0]), "v"(HX1[1]));
    asm volatile("ds_read_b64_tr_b16 %0, %1 offset:34816" : "=v"(tl[2]) : "v"(ht[0]) : "memory");
    asm volatile("v_mfma_f32_16x16x32_bf16 %0, %1, %2, %0" : "+v"(o[0][0]) : "v"(WX1[0]), "v"(HX1[0]));
    asm volatile("ds_read_b64_tr_b16 %0, %1 offset:34816" : "=v"(th[2]) : "v"(ht[1]) : "memory");
    HX0[2] = cat(tl[2], th[2]);
    asm volatile("s_addk_i32 m0, 0x400" ::: "scc", "memory");
    asm volatile("v_mfma_f32_16x16x32_bf16 %0, %1, %2, %0" : "+v"(o[1][3]) : "v"(WX1[1]), "v"(HX1[3]));
    asm volatile("ds_read_b128 %0, %1 offset:0" : "=v"(WX0[0]) : "v"(whi) : "memory");
    asm volatile("buffer_load_dwordx4 %0, %1, %2 offen offset:0 sc1 lds" :: "v"(hv1), "s"(rS), "s"(sh) : "memory");
    asm volatile("v_mfma_f32_16x16x32_bf16 %0, %1, %2, %0" : "+v"(o[1][2]) : "v"(WX1[1]), "v"(HX1[2]));
    asm volatile("ds_read_b128 %0, %1 offset:1024" : "=v"(WX0[1]) : "v"(whi) : "memory");
    asm volatile("v_mfma_f32_16x16x32_bf16 %0, %1, %2, %0" : "+v"(o[1][1]) : "v"(WX1[1]), "v"(HX1[1]));
    asm volatile("ds_read_b128 %0, %1 offset:2048" : "=v"(WX0[2]) : "v"(whi) : "memory");
    sh = 4194304 + (c*88 + ksa + 1)*8192;
    asm volatile("" : "+s"(sh));
    asm volatile("s_add_u32 m0, %0, 36864" :: "s"(lh) : "scc", "memory");
    asm volatile("v_mfma_f32_16x16x32_bf16 %0, %1, %2, %0" : "+v"(o[1][0]) : "v"(WX1[1]), "v"(HX1[0]));
    asm volatile("ds_read_b128 %0, %1 offset:3072" : "=v"(WX0[3]) : "v"(whi) : "memory");
    asm volatile("buffer_load_dwordx4 %0, %1, %2 offen offset:0 sc1 lds" :: "v"(hv0), "s"(rS), "s"(sh) : "memory");
    asm volatile("v_mfma_f32_16x16x32_bf16 %0, %1, %2, %0" : "+v"(o[2][3]) : "v"(WX1[2]), "v"(HX1[3]));
    asm volatile("ds_read_b128 %0, %1 offset:4096" : "=v"(WX0[4]) : "v"(whi) : "memory");
    asm volatile("v_mfma_f32_16x16x32_bf16 %0, %1, %2, %0" : "+v"(o[2][2]) : "v"(WX1[2]), "v"(HX1[2]));
    asm volatile("ds_read_b64_tr_b16 %0, %1 offset:33792" : "=v"(tl[1]) : "v"(ht[0]) : "memory");
    asm volatile("s_addk_i32 m0, 0x400" ::: "scc", "memory");
    asm volatile("v_mfma_f32_16x16x32_bf16 %0, %1, %2, %0" : "+v"(o[2][1]) : "v"(WX1[2]), "v"(HX1[1]));
    asm volatile("ds_read_b64_tr_b16 %0, %1 offset:33792" : "=v"(th[1]) : "v"(ht[1]) : "memory");
    HX0[1] = cat(tl[1], th[1]);
    asm volatile("buffer_load_dwordx4 %0, %1, %2 offen offset:0 sc1 lds" :: "v"(hv1), "s"(rS), "s"(sh) : "memory");
    asm volatile("v_mfma_f32_16x16x32_bf16 %0, %1, %2, %0" : "+v"(o[2][0]) : "v"(WX1[2]), "v"(HX1[0]));
    asm volatile("ds_read_b64_tr_b16 %0, %1 offset:32768" : "=v"(tl[0]) : "v"(ht[0]) : "memory");
    asm volatile("v_mfma_f32_16x16x32_bf16 %0, %1, %2, %0" : "+v"(o[3][3]) : "v"(WX1[3]), "v"(HX1[3]));
    asm volatile("ds_read_b64_tr_b16 %0, %1 offset:32768" : "=v"(th[0]) : "v"(ht[1]) : "memory");
    HX0[0] = cat(tl[0], th[0]);
    sw = swb + (ksa + 10 + wm*(4294967288u))*40960;
    asm volatile("" : "+s"(sw));
    asm volatile("s_add_u32 m0, %0, 133120" :: "s"(lw2) : "scc", "memory");
    asm volatile("v_mfma_f32_16x16x32_bf16 %0, %1, %2, %0" : "+v"(o[3][2]) : "v"(WX1[3]), "v"(HX1[2]));
    asm volatile("buffer_load_dwordx4 %0, %1, %2 offen offset:0 sc1 lds" :: "v"(vl16), "s"(rS), "s"(sw) : "memory");
    asm volatile("v_mfma_f32_16x16x32_bf16 %0, %1, %2, %0" : "+v"(o[3][1]) : "v"(WX1[3]), "v"(HX1[1]));
    asm volatile("v_mfma_f32_16x16x32_bf16 %0, %1, %2, %0" : "+v"(o[3][0]) : "v"(WX1[3]), "v"(HX1[0]));
    asm volatile("v_mfma_f32_16x16x32_bf16 %0, %1, %2, %0" : "+v"(o[4][3]) : "v"(WX1[4]), "v"(HX1[3]));
    asm volatile("buffer_load_dwordx4 %0, %1, %2 offen offset:1024 sc1 lds" :: "v"(vl16), "s"(rS), "s"(sw) : "memory");
    asm volatile("v_mfma_f32_16x16x32_bf16 %0, %1, %2, %0" : "+v"(o[4][2]) : "v"(WX1[4]), "v"(HX1[2]));
    asm volatile("v_mfma_f32_16x16x32_bf16 %0, %1, %2, %0" : "+v"(o[4][1]) : "v"(WX1[4]), "v"(HX1[1]));
    asm volatile("v_mfma_f32_16x16x32_bf16 %0, %1, %2, %0" : "+v"(o[4][0]) : "v"(WX1[4]), "v"(HX1[0]));
    asm volatile("buffer_load_dwordx4 %0, %1, %2 offen offset:2048 sc1 lds" :: "v"(vl16), "s"(rS), "s"(sw) : "memory");
    asm volatile("s_waitcnt lgkmcnt(0)" ::: "memory");
    asm volatile("v_mfma_f32_16x16x32_bf16 %0, %1, %2, %0" : "+v"(o[0][3]) : "v"(WX0[0]), "v"(HX0[3]));
    asm volatile("ds_read_b128 %0, %1 offset:44032" : "=v"(HX1[3]) : "v"(hb) : "memory");
    asm volatile("v_mfma_f32_16x16x32_bf16 %0, %1, %2, %0" : "+v"(o[0][2]) : "v"(WX0[0]), "v"(HX0[2]));
    asm volatile("ds_read_b128 %0, %1 offset:43008" : "=v"(HX1[2]) : "v"(hb) : "memory");
    asm volatile("v_mfma_f32_16x16x32_bf16 %0, %1, %2, %0" : "+v"(o[0][1]) : "v"(WX0[0]), "v"(HX0[1]));
    asm volatile("ds_read_b128 %0, %1 offset:40960" : "=v"(WX1[0]) : "v"(wlo) : "memory");
    asm volatile("buffer_load_dwordx4 %0, %1, %2 offen offset:3072 sc1 lds" :: "v"(vl16), "s"(rS), "s"(sw) : "memory");
    asm volatile("v_mfma_f32_16x16x32_bf16 %0, %1, %2, %0" : "+v"(o[0][0]) : "v"(WX0[0]), "v"(HX0[0]));
    asm volatile("ds_read_b128 %0, %1 offset:41984" : "=v"(WX1[1]) : "v"(wlo) : "memory");
    asm volatile("v_mfma_f32_16x16x32_bf16 %0, %1, %2, %0" : "+v"(o[1][3]) : "v"(WX0[1]), "v"(HX0[3]));
    asm volatile("ds_read_b128 %0, %1 offset:43008" : "=v"(WX1[2]) : "v"(wlo) : "memory");
    asm volatile("s_addk_i32 m0, 0x1000" ::: "scc", "memory");
    asm volatile("v_mfma_f32_16x16x32_bf16 %0, %1, %2, %0" : "+v"(o[1][2]) : "v"(WX0[1]), "v"(HX0[2]));
    asm volatile("ds_read_b128 %0, %1 offset:44032" : "=v"(WX1[3]) : "v"(wlo) : "memory");
    asm volatile("buffer_load_dwordx4 %0, %1, %2 offen offset:0 sc1 lds" :: "v"(vl16h), "s"(rS), "s"(sw) : "memory");
    asm volatile("v_mfma_f32_16x16x32_bf16 %0, %1, %2, %0" : "+v"(o[1][1]) : "v"(WX0[1]), "v"(HX0[1]));
    asm volatile("ds_read_b128 %0, %1 offset:45056" : "=v"(WX1[4]) : "v"(wlo) : "memory");
    asm volatile("v_mfma_f32_16x16x32_bf16 %0, %1, %2, %0" : "+v"(o[1][0]) : "v"(WX0[1]), "v"(HX0[0]));
    asm volatile("ds_read_b128 %0, %1 offset:41984" : "=v"(HX1[1]) : "v"(hb) : "memory");
    asm volatile("v_mfma_f32_16x16x32_bf16 %0, %1, %2, %0" : "+v"(o[2][3]) : "v"(WX0[2]), "v"(HX0[3]));
    asm volatile("ds_read_b128 %0, %1 offset:40960" : "=v"(HX1[0]) : "v"(hb) : "memory");
    asm volatile("v_mfma_f32_16x16x32_bf16 %0, %1, %2, %0" : "+v"(o[2][2]) : "v"(WX0[2]), "v"(HX0[2]));
    asm volatile("v_mfma_f32_16x16x32_bf16 %0, %1, %2, %0" : "+v"(o[2][1]) : "v"(WX0[2]), "v"(HX0[1]));
    asm volatile("v_mfma_f32_16x16x32_bf16 %0, %1, %2, %0" : "+v"(o[2][0]) : "v"(WX0[2]), "v"(HX0[0]));
    asm volatile("v_mfma_f32_16x16x32_bf16 %0, %1, %2, %0" : "+v"(o[3][3]) : "v"(WX0[3]), "v"(HX0[3]));
    asm volatile("v_mfma_f32_16x16x32_bf16 %0, %1, %2, %0" : "+v"(o[3][2]) : "v"(WX0[3]), "v"(HX0[2]));
    asm volatile("v_mfma_f32_16x16x32_bf16 %0, %1, %2, %0" : "+v"(o[3][1]) : "v"(WX0[3]), "v"(HX0[1]));
    asm volatile("v_mfma_f32_16x16x32_bf16 %0, %1, %2, %0" : "+v"(o[3][0]) : "v"(WX0[3]), "v"(HX0[0]));
    asm volatile("v_mfma_f32_16x16x32_bf16 %0, %1, %2, %0" : "+v"(o[4][3]) : "v"(WX0[4]), "v"(HX0[3]));
    asm volatile("v_mfma_f32_16x16x32_bf16 %0, %1, %2, %0" : "+v"(o[4][2]) : "v"(WX0[4]), "v"(HX0[2]));
    asm volatile("v_mfma_f32_16x16x32_bf16 %0, %1, %2, %0" : "+v"(o[4][1]) : "v"(WX0[4]), "v"(HX0[1]));
    asm volatile("v_mfma_f32_16x16x32_bf16 %0, %1, %2, %0" : "+v"(o[4][0]) : "v"(WX0[4]), "v"(HX0[0]));
    asm volatile("s_waitcnt vmcnt(15) lgkmcnt(0)\n\ts_barrier" ::: "memory");
    asm volatile("v_mfma_f32_16x16x32_bf16 %0, %1, %2, %0" : "+v"(o[0][3]) : "v"(WX1[0]), "v"(HX1[3]));
    asm volatile("ds_read_b128 %0, %1 offset:3072" : "=v"(HX0[3]) : "v"(hb) : "memory");
    sh = 4194304 + (c*88 + ksa + 4)*8192;
    asm volatile("" : "+s"(sh));
    asm volatile("s_add_u32 m0, %0, 45056" :: "s"(lh) : "scc", "memory");
    asm volatile("v_mfma_f32_16x16x32_bf16 %0, %1, %2, %0" : "+v"(o[0][2]) : "v"(WX1[0]), "v"(HX1[2]));
    asm volatile("ds_read_b128 %0, %1 offset:2048" : "=v"(HX0[2]) : "v"(hb) : "memory");
    asm volatile("buffer_load_dwordx4 %0, %1, %2 offen offset:0 sc1 lds" :: "v"(hv0), "s"(rS), "s"(sh) : "memory");
    asm volatile("v_mfma_f32_16x16x32_bf16 %0, %1, %2, %0" : "+v"(o[0][1]) : "v"(WX1[0]), "v"(HX1[1]));
    asm volatile("ds_read_b128 %0, %1 offset:30720" : "=v"(WX0[0]) : "v"(wlo) : "memory");
    asm volatile("v_mfma_f32_16x16x32_bf16 %0, %1, %2, %0" : "+v"(o[0][0]) : "v"(WX1[0]), "v"(HX1[0]));
    asm volatile("ds_read_b128 %0, %1 offset:31744" : "=v"(WX0[1]) : "v"(wlo) : "memory");
    asm volatile("s_addk_i32 m0, 0x400" ::: "scc", "memory");
    asm volatile("v_mfma_f32_16x16x32_bf16 %0, %1, %2, %0" : "+v"(o[1][3]) : "v"(WX1[1]), "v"(HX1[3]));
    asm volatile("ds_read_b128 %0, %1 offset:32768" : "=v"(WX0[2]) : "v"(wlo) : "memory");
    asm volatile("buffer_load_dwordx4 %0, %1, %2 offen offset:0 sc1 lds" :: "v"(hv1), "s"(rS), "s"(sh) : "memory");
    asm volatile("v_mfma_f32_16x16x32_bf16 %0, %1, %2, %0" : "+v"(o[1][2]) : "v"(WX1[1]), "v"(HX1[2]));
    asm volatile("ds_read_b128 %0, %1 offset:33792" : "=v"(WX0[3]) : "v"(wlo) : "memory");
    asm volatile("v_mfma_f32_16x16x32_bf16 %0, %1, %2, %0" : "+v"(o[1][1]) : "v"(WX1[1]), "v"(HX1[1]));
    asm volatile("ds_read_b128 %0, %1 offset:34816" : "=v"(WX0[4]) : "v"(wlo) : "memory");
    asm volatile("buffer_load_dword %0, %1, %2, 0 offen sc1" : "=v"(f2v) : "v"(fo), "s"(rS) : "memory");
    asm volatile("v_mfma_f32_16x16x32_bf16 %0, %1, %2, %0" : "+v"(o[1][0]) : "v"(WX1[1]), "v"(HX1[0]));
    asm volatile("ds_read_b128 %0, %1 offset:1024" : "=v"(HX0[1]) : "v"(hb) : "memory");
    sh = 4194304 + (c*88 + ksa + 7)*8192;
    asm volatile("" : "+s"(sh));
    asm volatile("s_add_u32 m0, %0, 53248" :: "s"(lh) : "scc", "memory");
    asm volatile("v_mfma_f32_16x16x32_bf16 %0, %1, %2, %0" : "+v"(o[2][3]) : "v"(WX1[2]), "v"(HX1[3]));
    asm volatile("ds_read_b128 %0, %1 offset:0" : "=v"(HX0[0]) : "v"(hb) : "memory");
    asm volatile("buffer_load_dwordx4 %0, %1, %2 offen offset:0 sc1 lds" :: "v"(hv0), "s"(rS), "s"(sh) : "memory");
    asm volatile("v_mfma_f32_16x16x32_bf16 %0, %1, %2, %0" : "+v"(o[2][2]) : "v"(WX1[2]), "v"(HX1[2]));
    asm volatile("v_mfma_f32_16x16x32_bf16 %0, %1, %2, %0" : "+v"(o[2][1]) : "v"(WX1[2]), "v"(HX1[1]));
    asm volatile("s_addk_i32 m0, 0x400" ::: "scc", "memory");
    asm volatile("v_mfma_f32_16x16x32_bf16 %0, %1, %2, %0" : "+v"(o[2][0]) : "v"(WX1[2]), "v"(HX1[0]));
    asm volatile("buffer_load_dwordx4 %0, %1, %2 offen offset:0 sc1 lds" :: "v"(hv1), "s"(rS), "s"(sh) : "memory");
    asm volatile("v_mfma_f32_16x16x32_bf16 %0, %1, %2, %0" : "+v"(o[3][3]) : "v"(WX1[3]), "v"(HX1[3]));
    asm volatile("v_mfma_f32_16x16x32_bf16 %0, %1, %2, %0" : "+v"(o[3][2]) : "v"(WX1[3]), "v"(HX1[2]));
    sw = swb + (ksa + 5 + wm*(3u))*40960;
    asm volatile("" : "+s"(sw));
    asm volatile("s_add_u32 m0, %0, 112640" :: "s"(lw2) : "scc", "memory");
    asm volatile("v_mfma_f32_16x16x32_bf16 %0, %1, %2, %0" : "+v"(o[3][1]) : "v"(WX1[3]), "v"(HX1[1]));
    asm volatile("buffer_load_dwordx4 %0, %1, %2 offen offset:0 sc1 lds" :: "v"(vl16), "s"(rS), "s"(sw) : "memory");
    asm volatile("v_mfma_f32_16x16x32_bf16 %0, %1, %2, %0" : "+v"(o[3][0]) : "v"(WX1[3]), "v"(HX1[0]));
    asm volatile("v_mfma_f32_16x16x32_bf16 %0, %1, %2, %0" : "+v"(o[4][3]) : "v"(WX1[4]), "v"(HX1[3]));
    asm volatile("v_mfma_f32_16x16x32_bf16 %0, %1, %2, %0" : "+v"(o[4][2]) : "v"(WX1[4]), "v"(HX1[2]));
    asm volatile("buffer_load_dwordx4 %0, %1, %2 offen offset:1024 sc1 lds" :: "v"(vl16), "s"(rS), "s"(sw) : "memory");
    asm volatile("v_mfma_f32_16x16x32_bf16 %0, %1, %2, %0" : "+v"(o[4][1]) : "v"(WX1[4]), "v"(HX1[1]));
    asm volatile("v_mfma_f32_16x16x32_bf16 %0, %1, %2, %0" : "+v"(o[4][0]) : "v"(WX1[4]), "v"(HX1[0]));
    asm volatile("s_waitcnt lgkmcnt(0)" ::: "memory");
    asm volatile("v_mfma_f32_16x16x32_bf16 %0, %1, %2, %0" : "+v"(o[0][3]) : "v"(WX0[0]), "v"(HX0[3]));
    asm volatile("ds_read_b128 %0, %1 offset:11264" : "=v"(HX1[3]) : "v"(hb) : "memory");
    asm volatile("buffer_load_dwordx4 %0, %1, %2 offen offset:2048 sc1 lds" :: "v"(vl16), "s"(rS), "s"(sw) : "memory");
    asm volatile("v_mfma_f32_16x16x32_bf16 %0, %1, %2, %0" : "+v"(o[0][2]) : "v"(WX0[0]), "v"(HX0[2]));
    asm volatile("ds_read_b128 %0, %1 offset:10240" : "=v"(HX1[2]) : "v"(hb) : "memory");
    asm volatile("v_mfma_f32_16x16x32_bf16 %0, %1, %2, %0" : "+v"(o[0][1]) : "v"(WX0[0]), "v"(HX0[1]));
    asm volatile("ds_read_b128 %0, %1 offset:20480" : "=v"(WX1[0]) : "v"(wlo) : "memory");
    asm volatile("v_mfma_f32_16x16x32_bf16 %0, %1, %2, %0" : "+v"(o[0][0]) : "v"(WX0[0]), "v"(HX0[0]));
    asm volatile("ds_read_b128 %0, %1 offset:21504" : "=v"(WX1[1]) : "v"(wlo) : "memory");
    asm volatile("buffer_load_dwordx4 %0, %1, %2 offen offset:3072 sc1 lds" :: "v"(vl16), "s"(rS), "s"(sw) : "memory");
    asm volatile("v_mfma_f32_16x16x32_bf16 %0, %1, %2, %0" : "+v"(o[1][3]) : "v"(WX0[1]), "v"(HX0[3]));
    asm volatile("ds_read_b128 %0, %1 offset:22528" : "=v"(WX1[2]) : "v"(wlo) : "memory");
    asm volatile("v_mfma_f32_16x16x32_bf16 %0, %1, %2, %0" : "+v"(o[1][2]) : "v"(WX0[1]), "v"(HX0[2]));
    asm volatile("ds_read_b128 %0, %1 offset:23552" : "=v"(WX1[3]) : "v"(wlo) : "memory");
    asm volatile("s_addk_i32 m0, 0x1000" ::: "scc", "memory");
    asm volatile("v_mfma_f32_16x16x32_bf16 %0, %1, %2, %0" : "+v"(o[1][1]) : "v"(WX0[1]), "v"(HX0[1]));
    asm volatile("ds_read_b128 %0, %1 offset:24576" : "=v"(WX1[4]) : "v"(wlo) : "memory");
    asm volatile("buffer_load_dwordx4 %0, %1, %2 offen offset:0 sc1 lds" :: "v"(vl16h), "s"(rS), "s"(sw) : "memory");
    asm volatile("v_mfma_f32_16x16x32_bf16 %0, %1, %2, %0" : "+v"(o[1][0]) : "v"(WX0[1]), "v"(HX0[0]));
    asm volatile("ds_read_b128 %0, %1 offset:9216" : "=v"(HX1[1]) : "v"(hb) : "memory");
    asm volatile("v_mfma_f32_16x16x32_bf16 %0, %1, %2, %0" : "+v"(o[2][3]) : "v"(WX0[2]), "v"(HX0[3]));
    asm volatile("ds_read_b128 %0, %1 offset:8192" : "=v"(HX1[0]) : "v"(hb) : "memory");
    asm volatile("v_mfma_f32_16x16x32_bf16 %0, %1, %2, %0" : "+v"(o[2][2]) : "v"(WX0[2]), "v"(HX0[2]));
    asm volatile("v_mfma_f32_16x16x32_bf16 %0, %1, %2, %0" : "+v"(o[2][1]) : "v"(WX0[2]), "v"(HX0[1]));
    asm volatile("v_mfma_f32_16x16x32_bf16 %0, %1, %2, %0" : "+v"(o[2][0]) : "v"(WX0[2]), "v"(HX0[0]));
    asm volatile("v_mfma_f32_16x16x32_bf16 %0, %1, %2, %0" : "+v"(o[3][3]) : "v"(WX0[3]), "v"(HX0[3]));
    asm volatile("v_mfma_f32_16x16x32_bf16 %0, %1, %2, %0" : "+v"(o[3][2]) : "v"(WX0[3]), "v"(HX0[2]));
    asm volatile("v_mfma_f32_16x16x32_bf16 %0, %1, %2, %0" : "+v"(o[3][1]) : "v"(WX0[3]), "v"(HX0[1]));
    asm volatile("v_mfma_f32_16x16x32_bf16 %0, %1, %2, %0" : "+v"(o[3][0]) : "v"(WX0[3]), "v"(HX0[0]));
    asm volatile("v_mfma_f32_16x16x32_bf16 %0, %1, %2, %0" : "+v"(o[4][3]) : "v"(WX0[4]), "v"(HX0[3]));
    asm volatile("v_mfma_f32_16x16x32_bf16 %0, %1, %2, %0" : "+v"(o[4][2]) : "v"(WX0[4]), "v"(HX0[2]));
    asm volatile("v_mfma_f32_16x16x32_bf16 %0, %1, %2, %0" : "+v"(o[4][1]) : "v"(WX0[4]), "v"(HX0[1]));
    asm volatile("v_mfma_f32_16x16x32_bf16 %0, %1, %2, %0" : "+v"(o[4][0]) : "v"(WX0[4]), "v"(HX0[0]));
    asm volatile("s_waitcnt vmcnt(15) lgkmcnt(0)\n\ts_barrier" ::: "memory");
    __builtin_amdgcn_sched_barrier(0);
    asm volatile("s_waitcnt vmcnt(7)" ::: "memory");
    asm volatile("" : "+v"(f2v));
    if (!__all(flev > 2 || fown || f2v == tag)) spin1(rS, fo, flev > 2 || fown, tag);
    __builtin_amdgcn_sched_barrier(0);
    asm volatile("v_mfma_f32_16x16x32_bf16 %0, %1, %2, %0" : "+v"(o[0][3]) : "v"(WX1[0]), "v"(HX1[3]));
    asm volatile("ds_read_b128 %0, %1 offset:19456" : "=v"(HX0[3]) : "v"(hb) : "memory");
    sh = 4194304 + (c*88 + ksa + 10)*8192;
    asm volatile("" : "+s"(sh));
    asm volatile("s_add_u32 m0, %0, 12288" :: "s"(lh) : "scc", "memory");
    asm volatile("v_mfma_f32_16x16x32_bf16 %0, %1, %2, %0" : "+v"(o[0][2]) : "v"(WX1[0]), "v"(HX1[2]));
    asm volatile("ds_read_b128 %0, %1 offset:18432" : "=v"(HX0[2]) : "v"(hb) : "memory");
    asm volatile("buffer_load_dwordx4 %0, %1, %2 offen offset:0 sc1 lds" :: "v"(hv0), "s"(rS), "s"(sh) : "memory");
    asm volatile("v_mfma_f32_16x16x32_bf16 %0, %1, %2, %0" : "+v"(o[0][1]) : "v"(WX1[0]), "v"(HX1[1]));
    asm volatile("ds_read_b128 %0, %1 offset:10240" : "=v"(WX0[0]) : "v"(wlo) : "memory");
    asm volatile("v_mfma_f32_16x16x32_bf16 %0, %1, %2, %0" : "+v"(o[0][0]) : "v"(WX1[0]), "v"(HX1[0]));
    asm volatile("ds_read_b128 %0, %1 offset:11264" : "=v"(WX0[1]) : "v"(wlo) : "memory");
    asm volatile("s_addk_i32 m0, 0x400" ::: "scc", "memory");
    asm volatile("v_mfma_f32_16x16x32_bf16 %0, %1, %2, %0" : "+v"(o[1][3]) : "v"(WX1[1]), "v"(HX1[3]));
    asm volatile("ds_read_b128 %0, %1 offset:12288" : "=v"(WX0[2]) : "v"(wlo) : "memory");
    asm volatile("buffer_load_dwordx4 %0, %1, %2 offen offset:0 sc1 lds" :: "v"(hv1), "s"(rS), "s"(sh) : "memory");
    asm volatile("v_mfma_f32_16x16x32_bf16 %0, %1, %2, %0" : "+v"(o[1][2]) : "v"(WX1[1]), "v"(HX1[2]));
    asm volatile("ds_read_b128 %0, %1 offset:13312" : "=v"(WX0[3]) : "v"(wlo) : "memory");
    asm volatile("v_mfma_f32_16x16x32_bf16 %0, %1, %2, %0" : "+v"(o[1][1]) : "v"(WX1[1]), "v"(HX1[1]));
    asm volatile("ds_read_b128 %0, %1 offset:14336" : "=v"(WX0[4]) : "v"(wlo) : "memory");
    sh = 4194304 + (c*88 + ksa + 2)*8192;
    asm volatile("" : "+s"(sh));
    asm volatile("s_add_u32 m0, %0, 20480" :: "s"(lh) : "scc", "memory");
    asm volatile("v_mfma_f32_16x16x32_bf16 %0, %1, %2, %0" : "+v"(o[1][0]) : "v"(WX1[1]), "v"(HX1[0]));
    asm volatile("ds_read_b128 %0, %1 offset:17408" : "=v"(HX0[1]) : "v"(hb) : "memory");
    asm volatile("buffer_load_dwordx4 %0, %1, %2 offen offset:0 sc1 lds" :: "v"(hv0), "s"(rS), "s"(sh) : "memory");
    asm volatile("v_mfma_f32_16x16x32_bf16 %0, %1, %2, %0" : "+v"(o[2][3]) : "v"(WX1[2]), "v"(HX1[3]));
    asm volatile("ds_read_b128 %0, %1 offset:16384" : "=v"(HX0[0]) : "v"(hb) : "memory");
    asm volatile("v_mfma_f32_16x16x32_bf16 %0, %1, %2, %0" : "+v"(o[2][2]) : "v"(WX1[2]), "v"(HX1[2]));
    asm volatile("s_addk_i32 m0, 0x400" ::: "scc", "memory");
    asm volatile("v_mfma_f32_16x16x32_bf16 %0, %1, %2, %0" : "+v"(o[2][1]) : "v"(WX1[2]), "v"(HX1[1]));
    asm volatile("buffer_load_dwordx4 %0, %1, %2 offen offset:0 sc1 lds" :: "v"(hv1), "s"(rS), "s"(sh) : "memory");
    asm volatile("v_mfma_f32_16x16x32_bf16 %0, %1, %2, %0" : "+v"(o[2][0]) : "v"(WX1[2]), "v"(HX1[0]));
    asm volatile("v_mfma_f32_16x16x32_bf16 %0, %1, %2, %0" : "+v"(o[3][3]) : "v"(WX1[3]), "v"(HX1[3]));
    sw = swb + (ksa + 11 + wm*(ksp + 0 - ksa - 11))*40960;
    asm volatile("" : "+s"(sw));
    asm volatile("s_add_u32 m0, %0, 92160" :: "s"(lw2) : "scc", "memory");
    asm volatile("v_mfma_f32_16x16x32_bf16 %0, %1, %2, %0" : "+v"(o[3][2]) : "v"(WX1[3]), "v"(HX1[2]));
    asm volatile("buffer_load_dwordx4 %0, %1, %2 offen offset:0 sc1 lds" :: "v"(vl16), "s"(rS), "s"(sw) : "memory");
    asm volatile("v_mfma_f32_16x16x32_bf16 %0, %1, %2, %0" : "+v"(o[3][1]) : "v"(WX1[3]), "v"(HX1[1]));
    asm volatile("v_mfma_f32_16x16x32_bf16 %0, %1, %2, %0" : "+v"(o[3][0]) : "v"(WX1[3]), "v"(HX1[0]));
    asm volatile("v_mfma_f32_16x16x32_bf16 %0, %1, %2, %0" : "+v"(o[4][3]) : "v"(WX1[4]), "v"(HX1[3]));
    asm volatile("buffer_load_dwordx4 %0, %1, %2 offen offset:1024 sc1 lds" :: "v"(vl16), "s"(rS), "s"(sw) : "memory");
    asm volatile("v_mfma_f32_16x16x32_bf16 %0, %1, %2, %0" : "+v"(o[4][2]) : "v"(WX1[4]), "v"(HX1[2]));
    asm volatile("v_mfma_f32_16x16x32_bf16 %0, %1, %2, %0" : "+v"(o[4][1]) : "v"(WX1[4]), "v"(HX1[1]));
    asm volatile("v_mfma_f32_16x16x32_bf16 %0, %1, %2, %0" : "+v"(o[4][0]) : "v"(WX1[4]), "v"(HX1[0]));
    asm volatile("buffer_load_dwordx4 %0, %1, %2 offen offset:2048 sc1 lds" :: "v"(vl16), "s"(rS), "s"(sw) : "memory");
    asm volatile("s_waitcnt lgkmcnt(0)" ::: "memory");
    asm volatile("v_mfma_f32_16x16x32_bf16 %0, %1, %2, %0" : "+v"(o[0][3]) : "v"(WX0[0]), "v"(HX0[3]));
    asm volatile("ds_read_b128 %0, %1 offset:27648" : "=v"(HX1[3]) : "v"(hb) : "memory");
    asm volatile("v_mfma_f32_16x16x32_bf16 %0, %1, %2, %0" : "+v"(o[0][2]) : "v"(WX0[0]), "v"(HX0[2]));
    asm volatile("ds_read_b128 %0, %1 offset:26624" : "=v"(HX1[2]) : "v"(hb) : "memory");
    asm volatile("v_mfma_f32_16x16x32_bf16 %0, %1, %2, %0" : "+v"(o[0][1]) : "v"(WX0[0]), "v"(HX0[1]));
    asm volatile("ds_read_b128 %0, %1 offset:0" : "=v"(WX1[0]) : "v"(wlo) : "memory");
    asm volatile("buffer_load_dwordx4 %0, %1, %2 offen offset:3072 sc1 lds" :: "v"(vl16), "s"(rS), "s"(sw) : "memory");
    asm volatile("v_mfma_f32_16x16x32_bf16 %0, %1, %2, %0" : "+v"(o[0][0]) : "v"(WX0[0]), "v"(HX0[0]));
    asm volatile("ds_read_b128 %0, %1 offset:1024" : "=v"(WX1[1]) : "v"(wlo) : "memory");
    asm volatile("v_mfma_f32_16x16x32_bf16 %0, %1, %2, %0" : "+v"(o[1][3]) : "v"(WX0[1]), "v"(HX0[3]));
    asm volatile("ds_read_b128 %0, %1 offset:2048" : "=v"(WX1[2]) : "v"(wlo) : "memory");
    asm volatile("s_addk_i32 m0, 0x1000" ::: "scc", "memory");
    asm volatile("v_mfma_f32_16x16x32_bf16 %0, %1, %2, %0" : "+v"(o[1][2]) : "v"(WX0[1]), "v"(HX0[2]));
    asm volatile("ds_read_b128 %0, %1 offset:3072" : "=v"(WX1[3]) : "v"(wlo) : "memory");
    asm volatile("buffer_load_dwordx4 %0, %1, %2 offen offset:0 sc1 lds" :: "v"(vl16h), "s"(rS), "s"(sw) : "memory");
    asm volatile("v_mfma_f32_16x16x32_bf16 %0, %1, %2, %0" : "+v"(o[1][1]) : "v"(WX0[1]), "v"(HX0[1]));
    asm volatile("ds_read_b128 %0, %1 offset:4096" : "=v"(WX1[4]) : "v"(wlo) : "memory");
    asm volatile("v_mfma_f32_16x16x32_bf16 %0, %1, %2, %0" : "+v"(o[1][0]) : "v"(WX0[1]), "v"(HX0[0]));
    asm volatile("ds_read_b128 %0, %1 offset:25600" : "=v"(HX1[1]) : "v"(hb) : "memory");
    asm volatile("v_mfma_f32_16x16x32_bf16 %0, %1, %2, %0" : "+v"(o[2][3]) : "v"(WX0[2]), "v"(HX0[3]));
    asm volatile("ds_read_b128 %0, %1 offset:24576" : "=v"(HX1[0]) : "v"(hb) : "memory");
    asm volatile("v_mfma_f32_16x16x32_bf16 %0, %1, %2, %0" : "+v"(o[2][2]) : "v"(WX0[2]), "v"(HX0[2]));
    asm volatile("v_mfma_f32_16x16x32_bf16 %0, %1, %2, %0" : "+v"(o[2][1]) : "v"(WX0[2]), "v"(HX0[1]));
    asm volatile("v_mfma_f32_16x16x32_bf16 %0, %1, %2, %0" : "+v"(o[2][0]) : "v"(WX0[2]), "v"(HX0[0]));
    asm volatile("v_mfma_f32_16x16x32_bf16 %0, %1, %2, %0" : "+v"(o[3][3]) : "v"(WX0[3]), "v"(HX0[3]));
    asm volatile("v_mfma_f32_16x16x32_bf16 %0, %1, %2, %0" : "+v"(o[3][2]) : "v"(WX0[3]), "v"(HX0[2]));
    asm volatile("v_mfma_f32_16x16x32_bf16 %0, %1, %2, %0" : "+v"(o[3][1]) : "v"(WX0[3]), "v"(HX0[1]));
    asm volatile("v_mfma_f32_16x16x32_bf16 %0, %1, %2, %0" : "+v"(o[3][0]) : "v"(WX0[3]), "v"(HX0[0]));
    asm volatile("v_mfma_f32_16x16x32_bf16 %0, %1, %2, %0" : "+v"(o[4][3]) : "v"(WX0[4]), "v"(HX0[3]));
    asm volatile("v_mfma_f32_16x16x32_bf16 %0, %1, %2, %0" : "+v"(o[4][2]) : "v"(WX0[4]), "v"(HX0[2]));
    asm volatile("v_mfma_f32_16x16x32_bf16 %0, %1, %2, %0" : "+v"(o[4][1]) : "v"(WX0[4]), "v"(HX0[1]));
    asm volatile("v_mfma_f32_16x16x32_bf16 %0, %1, %2, %0" : "+v"(o[4][0]) : "v"(WX0[4]), "v"(HX0[0]));
    asm volatile("s_waitcnt vmcnt(14) lgkmcnt(0)\n\ts_barrier" ::: "memory");
    asm volatile("v_mfma_f32_16x16x32_bf16 %0, %1, %2, %0" : "+v"(o[0][3]) : "v"(WX1[0]), "v"(HX1[3]));
    asm volatile("ds_read_b128 %0, %1 offset:35840" : "=v"(HX0[3]) : "v"(hb) : "memory");
    sh = 4194304 + (c*88 + ksa + 5)*8192;
    asm volatile("" : "+s"(sh));
    asm volatile("s_add_u32 m0, %0, 28672" :: "s"(lh) : "scc", "memory");
    asm volatile("v_mfma_f32_16x16x32_bf16 %0, %1, %2, %0" : "+v"(o[0][2]) : "v"(WX1[0]), "v"(HX1[2]));
    asm volatile("ds_read_b128 %0, %1 offset:34816" : "=v"(HX0[2]) : "v"(hb) : "memory");
    asm volatile("buffer_load_dwordx4 %0, %1, %2 offen offset:0 sc1 lds" :: "v"(hv0), "s"(rS), "s"(sh) : "memory");
    asm volatile("v_mfma_f32_16x16x32_bf16 %0, %1, %2, %0" : "+v"(o[0][1]) : "v"(WX1[0]), "v"(HX1[1]));
    asm volatile("ds_read_b128 %0, %1 offset:40960" : "=a"(WA[10][0]) : "v"(whi) : "memory");
    asm volatile("s_addk_i32 m0, 0x400" ::: "scc", "memory");
    asm volatile("v_mfma_f32_16x16x32_bf16 %0, %1, %2, %0" : "+v"(o[0][0]) : "v"(WX1[0]), "v"(HX1[0]));
    asm volatile("ds_read_b128 %0, %1 offset:41984" : "=a"(WA[10][1]) : "v"(whi) : "memory");
    asm volatile("buffer_load_dwordx4 %0, %1, %2 offen offset:0 sc1 lds" :: "v"(hv1), "s"(rS), "s"(sh) : "memory");
    asm volatile("v_mfma_f32_16x16x32_bf16 %0, %1, %2, %0" : "+v"(o[1][3]) : "v"(WX1[1]), "v"(HX1[3]));
    asm volatile("ds_read_b128 %0, %1 offset:43008" : "=a"(WA[10][2]) : "v"(whi) : "memory");
    asm volatile("buffer_load_dword %0, %1, %2, 0 offen sc1" : "=v"(f2v) : "v"(fo), "s"(rS) : "memory");
    asm volatile("v_mfma_f32_16x16x32_bf16 %0, %1, %2, %0" : "+v"(o[1][2]) : "v"(WX1[1]), "v"(HX1[2]));
    asm volatile("ds_read_b128 %0, %1 offset:44032" : "=a"(WA[10][3]) : "v"(whi) : "memory");
    sh = 4194304 + (c*88 + ksa + 8)*8192;
    asm volatile("" : "+s"(sh));
    asm volatile("s_add_u32 m0, %0, 36864" :: "s"(lh) : "scc", "memory");
    asm volatile("v_mfma_f32_16x16x32_bf16 %0, %1, %2, %0" : "+v"(o[1][1]) : "v"(WX1[1]), "v"(HX1[1]));
    asm volatile("ds_read_b128 %0, %1 offset:45056" : "=a"(WA[10][4]) : "v"(whi) : "memory");
    asm volatile("buffer_load_dwordx4 %0, %1, %2 offen offset:0 sc1 lds" :: "v"(hv0), "s"(rS), "s"(sh) : "memory");
    asm volatile("v_mfma_f32_16x16x32_bf16 %0, %1, %2, %0" : "+v"(o[1][0]) : "v"(WX1[1]), "v"(HX1[0]));
    asm volatile("ds_read_b128 %0, %1 offset:33792" : "=v"(HK1[10]) : "v"(hb) : "memory");
    asm volatile("s_addk_i32 m0, 0x400" ::: "scc", "memory");
    asm volatile("v_mfma_f32_16x16x32_bf16 %0, %1, %2, %0" : "+v"(o[2][3]) : "v"(WX1[2]), "v"(HX1[3]));
    asm volatile("ds_read_b128 %0, %1 offset:32768" : "=v"(HK0[10]) : "v"(hb) : "memory");
    asm volatile("buffer_load_dwordx4 %0, %1, %2 offen offset:0 sc1 lds" :: "v"(hv1), "s"(rS), "s"(sh) : "memory");
    asm volatile("v_mfma_f32_16x16x32_bf16 %0, %1, %2, %0" : "+v"(o[2][2]) : "v"(WX1[2]), "v"(HX1[2]));
    sw = swb + (ksp + 1 + wm*(1u))*40960;
    asm volatile("" : "+s"(sw));
    asm volatile("s_add_u32 m0, %0, 71680" :: "s"(lw2) : "scc", "memory");
    asm volatile("v_mfma_f32_16x16x32_bf16 %0, %1, %2, %0" : "+v"(o[2][1]) : "v"(WX1[2]), "v"(HX1[1]));
    asm volatile("buffer_load_dwordx4 %0, %1, %2 offen offset:0 sc1 lds" :: "v"(vl16), "s"(rS), "s"(sw) : "memory");
    asm volatile("v_mfma_f32_16x16x32_bf16 %0, %1, %2, %0" : "+v"(o[2][0]) : "v"(WX1[2]), "v"(HX1[0]));
    asm volatile("v_mfma_f32_16x16x32_bf16 %0, %1, %2, %0" : "+v"(o[3][3]) : "v"(WX1[3]), "v"(HX1[3]));
    asm volatile("buffer_load_dwordx4 %0, %1, %2 offen offset:1024 sc1 lds" :: "v"(vl16), "s"(rS), "s"(sw) : "memory");
    asm volatile("v_mfma_f32_16x16x32_bf16 %0, %1, %2, %0" : "+v"(o[3][2]) : "v"(WX1[3]), "v"(HX1[2]));
    asm volatile("v_mfma_f32_16x16x32_bf16 %0, %1, %2, %0" : "+v"(o[3][1]) : "v"(WX1[3]), "v"(HX1[1]));
    asm volatile("buffer_load_dwordx4 %0, %1, %2 offen offset:2048 sc1 lds" :: "v"(vl16), "s"(rS), "s"(sw) : "memory");
    asm volatile("v_mfma_f32_16x16x32_bf16 %0, %1, %2, %0" : "+v"(o[3][0]) : "v"(WX1[3]), "v"(HX1[0]));
    asm volatile("v_mfma_f32_16x16x32_bf16 %0, %1, %2, %0" : "+v"(o[4][3]) : "v"(WX1[4]), "v"(HX1[3]));
    asm volatile("buffer_load_dwordx4 %0, %1, %2 offen offset:3072 sc1 lds" :: "v"(vl16), "s"(rS), "s"(sw) : "memory");
    asm volatile("v_mfma_f32_16x16x32_bf16 %0, %1, %2, %0" : "+v"(o[4][2]) : "v"(WX1[4]), "v"(HX1[2]));
    asm volatile("s_addk_i32 m0, 0x1000" ::: "scc", "memory");
    asm volatile("v_mfma_f32_16x16x32_bf16 %0, %1, %2, %0" : "+v"(o[4][1]) : "v"(WX1[4]), "v"(HX1[1]));
    asm volatile("buffer_load_dwordx4 %0, %1, %2 offen offset:0 sc1 lds" :: "v"(vl16h), "s"(rS), "s"(sw) : "memory");
    asm volatile("v_mfma_f32_16x16x32_bf16 %0, %1, %2, %0" : "+v"(o[4][0]) : "v"(WX1[4]), "v"(HX1[0]));
    asm volatile("s_waitcnt lgkmcnt(0)" ::: "memory");
    asm volatile("v_mfma_f32_16x16x32_bf16 %0, %1, %2, %0" : "+v"(o[0][3]) : "a"(WA[10][0]), "v"(HX0[3]));
    asm volatile("ds_read_b128 %0, %1 offset:44032" : "=v"(HX1[3]) : "v"(hb) : "memory");
    asm volatile("ds_read_b128 %0, %1 offset:43008" : "=v"(HX1[2]) : "v"(hb) : "memory");
    asm volatile("v_mfma_f32_16x16x32_bf16 %0, %1, %2, %0" : "+v"(o[0][2]) : "a"(WA[10][0]), "v"(HX0[2]));
    asm volatile("ds_read_b128 %0, %1 offset:30720" : "=a"(WA[11][0]) : "v"(whi) : "memory");
    asm volatile("v_mfma_f32_16x16x32_bf16 %0, %1, %2, %0" : "+v"(o[1][3]) : "a"(WA[10][1]), "v"(HX0[3]));
    asm volatile("ds_read_b128 %0, %1 offset:31744" : "=a"(WA[11][1]) : "v"(whi) : "memory");
    asm volatile("ds_read_b128 %0, %1 offset:32768" : "=a"(WA[11][2]) : "v"(whi) : "memory");
    asm volatile("v_mfma_f32_16x16x32_bf16 %0, %1, %2, %0" : "+v"(o[1][2]) : "a"(WA[10][1]), "v"(HX0[2]));
    asm volatile("ds_read_b128 %0, %1 offset:33792" : "=a"(WA[11][3]) : "v"(whi) : "memory");
    asm volatile("v_mfma_f32_16x16x32_bf16 %0, %1, %2, %0" : "+v"(o[2][3]) : "a"(WA[10][2]), "v"(HX0[3]));
    asm volatile("ds_read_b128 %0, %1 offset:34816" : "=a"(WA[11][4]) : "v"(whi) : "memory");
    asm volatile("ds_read_b128 %0, %1 offset:41984" : "=v"(HK1[11]) : "v"(hb) : "memory");
    asm volatile("v_mfma_f32_16x16x32_bf16 %0, %1, %2, %0" : "+v"(o[2][2]) : "a"(WA[10][2]), "v"(HX0[2]));
    asm volatile("ds_read_b128 %0, %1 offset:40960" : "=v"(HK0[11]) : "v"(hb) : "memory");
    asm volatile("v_mfma_f32_16x16x32_bf16 %0, %1, %2, %0" : "+v"(o[3][3]) : "a"(WA[10][3]), "v"(HX0[3]));
    asm volatile("v_mfma_f32_16x16x32_bf16 %0, %1, %2, %0" : "+v"(o[3][2]) : "a"(WA[10][3]), "v"(HX0[2]));
    asm volatile("v_mfma_f32_16x16x32_bf16 %0, %1, %2, %0" : "+v"(o[4][3]) : "a"(WA[10][4]), "v"(HX0[3]));
    asm volatile("v_mfma_f32_16x16x32_bf16 %0, %1, %2, %0" : "+v"(o[4][2]) : "a"(WA[10][4]), "v"(HX0[2]));
    asm volatile("s_waitcnt vmcnt(15) lgkmcnt(0)\n\ts_barrier" ::: "memory");
    __builtin_amdgcn_sched_barrier(0);
    asm volatile("s_waitcnt vmcnt(7)" ::: "memory");
    asm volatile("" : "+v"(f2v));
    if (!__all(flev > 3 || fown || f2v == tag)) spin1(rS, fo, flev > 3 || fown, tag);
    __builtin_amdgcn_sched_barrier(0);
    asm volatile("v_mfma_f32_16x16x32_bf16 %0, %1, %2, %0" : "+v"(o[0][3]) : "a"(WA[11][0]), "v"(HX1[3]));
    asm volatile("ds_read_b128 %0, %1 offset:3072" : "=v"(HX0[3]) : "v"(hb) : "memory");
    asm volatile("ds_read_b128 %0, %1 offset:2048" : "=v"(HX0[2]) : "v"(hb) : "memory");
    sh = 4194304 + (c*88 + ksa + 11)*8192;
    asm volatile("" : "+s"(sh));
    asm volatile("s_add_u32 m0, %0, 45056" :: "s"(lh) : "scc", "memory");
    asm volatile("v_mfma_f32_16x16x32_bf16 %0, %1, %2, %0" : "+v"(o[0][2]) : "a"(WA[11][0]), "v"(HX1[2]));
    asm volatile("ds_read_b128 %0, %1 offset:20480" : "=a"(WA[12][0]) : "v"(whi) : "memory");
    asm volatile("buffer_load_dwordx4 %0, %1, %2 offen offset:0 sc1 lds" :: "v"(hv0), "s"(rS), "s"(sh) : "memory");
    asm volatile("s_addk_i32 m0, 0x400" ::: "scc", "memory");
    asm volatile("v_mfma_f32_16x16x32_bf16 %0, %1, %2, %0" : "+v"(o[1][3]) : "a"(WA[11][1]), "v"(HX1[3]));
    asm volatile("ds_read_b128 %0, %1 offset:21504" : "=a"(WA[12][1]) : "v"(whi) : "memory");
    asm volatile("ds_read_b128 %0, %1 offset:22528" : "=a"(WA[12][2]) : "v"(whi) : "memory");
    asm volatile("buffer_load_dwordx4 %0, %1, %2 offen offset:0 sc1 lds" :: "v"(hv1), "s"(rS), "s"(sh) : "memory");
    sh = 4194304 + (c*88 + ksp + 0)*8192;
    asm volatile("" : "+s"(sh));
    asm volatile("s_add_u32 m0, %0, 53248" :: "s"(lh) : "scc", "memory");
    asm volatile("v_mfma_f32_16x16x32_bf16 %0, %1, %2, %0" : "+v"(o[1][2]) : "a"(WA[11][1]), "v"(HX1[2]));
    asm volatile("ds_read_b128 %0, %1 offset:23552" : "=a"(WA[12][3]) : "v"(whi) : "memory");
    asm volatile("buffer_load_dwordx4 %0, %1, %2 offen offset:0 sc1 lds" :: "v"(hv0), "s"(rS), "s"(sh) : "memory");
    asm volatile("s_addk_i32 m0, 0x400" ::: "scc", "memory");
    asm volatile("v_mfma_f32_16x16x32_bf16 %0, %1, %2, %0" : "+v"(o[2][3]) : "a"(WA[11][2]), "v"(HX1[3]));
    asm volatile("ds_read_b128 %0, %1 offset:24576" : "=a"(WA[12][4]) : "v"(whi) : "memory");
    asm volatile("ds_read_b128 %0, %1 offset:1024" : "=v"(HK1[12]) : "v"(hb) : "memory");
    asm volatile("buffer_load_dwordx4 %0, %1, %2 offen offset:0 sc1 lds" :: "v"(hv1), "s"(rS), "s"(sh) : "memory");
    sw = swb + (ksp + 3 + wm*(1u))*40960;
    asm volatile("" : "+s"(sw));
    asm volatile("s_add_u32 m0, %0, 153600" :: "s"(lw2) : "scc", "memory");
    asm volatile("v_mfma_f32_16x16x32_bf16 %0, %1, %2, %0" : "+v"(o[2][2]) : "a"(WA[11][2]), "v"(HX1[2]));
    asm volatile("ds_read_b128 %0, %1 offset:0" : "=v"(HK0[12]) : "v"(hb) : "memory");
    asm volatile("buffer_load_dwordx4 %0, %1, %2 offen offset:0 sc1 lds" :: "v"(vl16), "s"(rS), "s"(sw) : "memory");
    asm volatile("v_mfma_f32_16x16x32_bf16 %0, %1, %2, %0" : "+v"(o[3][3]) : "a"(WA[11][3]), "v"(HX1[3]));
    asm volatile("buffer_load_dwordx4 %0, %1, %2 offen offset:1024 sc1 lds" :: "v"(vl16), "s"(rS), "s"(sw) : "memory");
    asm volatile("v_mfma_f32_16x16x32_bf16 %0, %1, %2, %0" : "+v"(o[3][2]) : "a"(WA[11][3]), "v"(HX1[2]));
    asm volatile("buffer_load_dwordx4 %0, %1, %2 offen offset:2048 sc1 lds" :: "v"(vl16), "s"(rS), "s"(sw) : "memory");
    asm volatile("v_mfma_f32_16x16x32_bf16 %0, %1, %2, %0" : "+v"(o[4][3]) : "a"(WA[11][4]), "v"(HX1[3]));
    asm volatile("buffer_load_dwordx4 %0, %1, %2 offen offset:3072 sc1 lds" :: "v"(vl16), "s"(rS), "s"(sw) : "memory");
    asm volatile("s_addk_i32 m0, 0x1000" ::: "scc", "memory");
    asm volatile("v_mfma_f32_16x16x32_bf16 %0, %1, %2, %0" : "+v"(o[4][2]) : "a"(WA[11][4]), "v"(HX1[2]));
    asm volatile("buffer_load_dwordx4 %0, %1, %2 offen offset:0 sc1 lds" :: "v"(vl16h), "s"(rS), "s"(sw) : "memory");
    asm volatile("s_waitcnt lgkmcnt(0)" ::: "memory");
    asm volatile("v_mfma_f32_16x16x32_bf16 %0, %1, %2, %0" : "+v"(o[0][3]) : "a"(WA[12][0]), "v"(HX0[3]));
    asm volatile("ds_read_b128 %0, %1 offset:11264" : "=v"(HX1[3]) : "v"(hb) : "memory");
    asm volatile("ds_read_b128 %0, %1 offset:10240" : "=v"(HX1[2]) : "v"(hb) : "memory");
    asm volatile("v_mfma_f32_16x16x32_bf16 %0, %1, %2, %0" : "+v"(o[0][2]) : "a"(WA[12][0]), "v"(HX0[2]));
    asm volatile("ds_read_b128 %0, %1 offset:10240" : "=a"(WA[13][0]) : "v"(whi) : "memory");
    asm volatile("v_mfma_f32_16x16x32_bf16 %0, %1, %2, %0" : "+v"(o[1][3]) : "a"(WA[12][1]), "v"(HX0[3]));
    asm volatile("ds_read_b128 %0, %1 offset:11264" : "=a"(WA[13][1]) : "v"(whi) : "memory");
    asm volatile("ds_read_b128 %0, %1 offset:12288" : "=a"(WA[13][2]) : "v"(whi) : "memory");
    asm volatile("v_mfma_f32_16x16x32_bf16 %0, %1, %2, %0" : "+v"(o[1][2]) : "a"(WA[12][1]), "v"(HX0[2]));
    asm volatile("ds_read_b128 %0, %1 offset:13312" : "=a"(WA[13][3]) : "v"(whi) : "memory");
    asm volatile("v_mfma_f32_16x16x32_bf16 %0, %1, %2, %0" : "+v"(o[2][3]) : "a"(WA[12][2]), "v"(HX0[3]));
    asm volatile("ds_read_b128 %0, %1 offset:14336" : "=a"(WA[13][4]) : "v"(whi) : "memory");
    asm volatile("ds_read_b128 %0, %1 offset:9216" : "=v"(HK1[13]) : "v"(hb) : "memory");
    asm volatile("v_mfma_f32_16x16x32_bf16 %0, %1, %2, %0" : "+v"(o[2][2]) : "a"(WA[12][2]), "v"(HX0[2]));
    asm volatile("ds_read_b128 %0, %1 offset:8192" : "=v"(HK0[13]) : "v"(hb) : "memory");
    asm volatile("v_mfma_f32_16x16x32_bf16 %0, %1, %2, %0" : "+v"(o[3][3]) : "a"(WA[12][3]), "v"(HX0[3]));
    asm volatile("v_mfma_f32_16x16x32_bf16 %0, %1, %2, %0" : "+v"(o[3][2]) : "a"(WA[12][3]), "v"(HX0[2]));
    asm volatile("v_mfma_f32_16x16x32_bf16 %0, %1, %2, %0" : "+v"(o[4][3]) : "a"(WA[12][4]), "v"(HX0[3]));
    asm volatile("v_mfma_f32_16x16x32_bf16 %0, %1, %2, %0" : "+v"(o[4][2]) : "a"(WA[12][4]), "v"(HX0[2]));
    asm volatile("s_waitcnt vmcnt(14) lgkmcnt(0)\n\ts_barrier" ::: "memory");
    asm volatile("v_mfma_f32_16x16x32_bf16 %0, %1, %2, %0" : "+v"(o[0][3]) : "a"(WA[13][0]), "v"(HX1[3]));
    asm volatile("ds_read_b128 %0, %1 offset:19456" : "=v"(HX0[3]) : "v"(hb) : "memory");
    asm volatile("ds_read_b128 %0, %1 offset:18432" : "=v"(HX0[2]) : "v"(hb) : "memory");
    sh = 4194304 + (c*88 + ksp + 1)*8192;
    asm volatile("" : "+s"(sh));
    asm volatile("s_add_u32 m0, %0, 12288" :: "s"(lh) : "scc", "memory");
    asm volatile("v_mfma_f32_16x16x32_bf16 %0, %1, %2, %0" : "+v"(o[0][2]) : "a"(WA[13][0]), "v"(HX1[2]));
    asm volatile("ds_read_b128 %0, %1 offset:0" : "=a"(WA[14][0]) : "v"(whi) : "memory");
    asm volatile("buffer_load_dwordx4 %0, %1, %2 offen offset:0 sc1 lds" :: "v"(hv0), "s"(rS), "s"(sh) : "memory");
    asm volatile("v_mfma_f32_16x16x32_bf16 %0, %1, %2, %0" : "+v"(o[1][3]) : "a"(WA[13][1]), "v"(HX1[3]));
    asm volatile("ds_read_b128 %0, %1 offset:1024" : "=a"(WA[14][1]) : "v"(whi) : "memory");
    asm volatile("ds_read_b128 %0, %1 offset:2048" : "=a"(WA[14][2]) : "v"(whi) : "memory");
    asm volatile("v_mfma_f32_16x16x32_bf16 %0, %1, %2, %0" : "+v"(o[1][2]) : "a"(WA[13][1]), "v"(HX1[2]));
    asm volatile("ds_read_b128 %0, %1 offset:3072" : "=a"(WA[14][3]) : "v"(whi) : "memory");
    asm volatile("s_addk_i32 m0, 0x400" ::: "scc", "memory");
    asm volatile("v_mfma_f32_16x16x32_bf16 %0, %1, %2, %0" : "+v"(o[2][3]) : "a"(WA[13][2]), "v"(HX1[3]));
    asm volatile("ds_read_b128 %0, %1 offset:4096" : "=a"(WA[14][4]) : "v"(whi) : "memory");
    asm volatile("ds_read_b128 %0, %1 offset:17408" : "=v"(HK1[14]) : "v"(hb) : "memory");
    asm volatile("buffer_load_dwordx4 %0, %1, %2 offen offset:0 sc1 lds" :: "v"(hv1), "s"(rS), "s"(sh) : "memory");
    asm volatile("v_mfma_f32_16x16x32_bf16 %0, %1, %2, %0" : "+v"(o[2][2]) : "a"(WA[13][2]), "v"(HX1[2]));
    asm volatile("ds_read_b128 %0, %1 offset:16384" : "=v"(HK0[14]) : "v"(hb) : "memory");
    asm volatile("v_mfma_f32_16x16x32_bf16 %0, %1, %2, %0" : "+v"(o[3][3]) : "a"(WA[13][3]), "v"(HX1[3]));
    sh = 4194304 + (c*88 + ksp + 2)*8192;
    asm volatile("" : "+s"(sh));
    asm volatile("s_add_u32 m0, %0, 20480" :: "s"(lh) : "scc", "memory");
    asm volatile("v_mfma_f32_16x16x32_bf16 %0, %1, %2, %0" : "+v"(o[3][2]) : "a"(WA[13][3]), "v"(HX1[2]));
    asm volatile("buffer_load_dwordx4 %0, %1, %2 offen offset:0 sc1 lds" :: "v"(hv0), "s"(rS), "s"(sh) : "memory");
    asm volatile("v_mfma_f32_16x16x32_bf16 %0, %1, %2, %0" : "+v"(o[4][3]) : "a"(WA[13][4]), "v"(HX1[3]));
    asm volatile("v_mfma_f32_16x16x32_bf16 %0, %1, %2, %0" : "+v"(o[4][2]) : "a"(WA[13][4]), "v"(HX1[2]));
    asm volatile("s_addk_i32 m0, 0x400" ::: "scc", "memory");
    asm volatile("s_waitcnt lgkmcnt(0)" ::: "memory");
    asm volatile("v_mfma_f32_16x16x32_bf16 %0, %1, %2, %0" : "+v"(o[0][3]) : "a"(WA[14][0]), "v"(HX0[3]));
    asm volatile("ds_read_b128 %0, %1 offset:27648" : "=v"(HX1[3]) : "v"(hb) : "memory");
    asm volatile("ds_read_b128 %0, %1 offset:26624" : "=v"(HX1[2]) : "v"(hb) : "memory");
    asm volatile("buffer_load_dwordx4 %0, %1, %2 offen offset:0 sc1 lds" :: "v"(hv1), "s"(rS), "s"(sh) : "memory");
    asm volatile("v_mfma_f32_16x16x32_bf16 %0, %1, %2, %0" : "+v"(o[0][2]) : "a"(WA[14][0]), "v"(HX0[2]));
    asm volatile("ds_read_b128 %0, %1 offset:40960" : "=a"(WA[15][0]) : "v"(wlo) : "memory");
    asm volatile("v_mfma_f32_16x16x32_bf16 %0, %1, %2, %0" : "+v"(o[1][3]) : "a"(WA[14][1]), "v"(HX0[3]));
    asm volatile("ds_read_b128 %0, %1 offset:41984" : "=a"(WA[15][1]) : "v"(wlo) : "memory");
    asm volatile("ds_read_b128 %0, %1 offset:43008" : "=a"(WA[15][2]) : "v"(wlo) : "memory");
    asm volatile("v_mfma_f32_16x16x32_bf16 %0, %1, %2, %0" : "+v"(o[1][2]) : "a"(WA[14][1]), "v"(HX0[2]));
    asm volatile("ds_read_b128 %0, %1 offset:44032" : "=a"(WA[15][3]) : "v"(wlo) : "memory");
    asm volatile("v_mfma_f32_16x16x32_bf16 %0, %1, %2, %0" : "+v"(o[2][3]) : "a"(WA[14][2]), "v"(HX0[3]));
    asm volatile("ds_read_b128 %0, %1 offset:45056" : "=a"(WA[15][4]) : "v"(wlo) : "memory");
    asm volatile("ds_read_b128 %0, %1 offset:25600" : "=v"(HK1[15]) : "v"(hb) : "memory");
    asm volatile("v_mfma_f32_16x16x32_bf16 %0, %1, %2, %0" : "+v"(o[2][2]) : "a"(WA[14][2]), "v"(HX0[2]));
    asm volatile("ds_read_b128 %0, %1 offset:24576" : "=v"(HK0[15]) : "v"(hb) : "memory");
    asm volatile("v_mfma_f32_16x16x32_bf16 %0, %1, %2, %0" : "+v"(o[3][3]) : "a"(WA[14][3]), "v"(HX0[3]));
    asm volatile("v_mfma_f32_16x16x32_bf16 %0, %1, %2, %0" : "+v"(o[3][2]) : "a"(WA[14][3]), "v"(HX0[2]));
    asm volatile("v_mfma_f32_16x16x32_bf16 %0, %1, %2, %0" : "+v"(o[4][3]) : "a"(WA[14][4]), "v"(HX0[3]));
    asm volatile("v_mfma_f32_16x16x32_bf16 %0, %1, %2, %0" : "+v"(o[4][2]) : "a"(WA[14][4]), "v"(HX0[2]));
    asm volatile("s_waitcnt vmcnt(9) lgkmcnt(0)\n\ts_barrier" ::: "memory");
    asm volatile("v_mfma_f32_16x16x32_bf16 %0, %1, %2, %0" : "+v"(o[0][3]) : "a"(WA[15][0]), "v"(HX1[3]));
    asm volatile("ds_read_b128 %0, %1 offset:35840" : "=v"(HX0[3]) : "v"(hb) : "memory");
    asm volatile("ds_read_b128 %0, %1 offset:34816" : "=v"(HX0[2]) : "v"(hb) : "memory");
    sh = 4194304 + (c*88 + ksp + 3)*8192;
    asm volatile("" : "+s"(sh));
    asm volatile("s_add_u32 m0, %0, 28672" :: "s"(lh) : "scc", "memory");
    asm volatile("v_mfma_f32_16x16x32_bf16 %0, %1, %2, %0" : "+v"(o[0][2]) : "a"(WA[15][0]), "v"(HX1[2]));
    asm volatile("ds_read_b128 %0, %1 offset:30720" : "=a"(WA[16][0]) : "v"(wlo) : "memory");
    asm volatile("buffer_load_dwordx4 %0, %1, %2 offen offset:0 sc1 lds" :: "v"(hv0), "s"(rS), "s"(sh) : "memory");
    asm volatile("v_mfma_f32_16x16x32_bf16 %0, %1, %2, %0" : "+v"(o[1][3]) : "a"(WA[15][1]), "v"(HX1[3]));
    asm volatile("ds_read_b128 %0, %1 offset:31744" : "=a"(WA[16][1]) : "v"(wlo) : "memory");
    asm volatile("ds_read_b128 %0, %1 offset:32768" : "=a"(WA[16][2]) : "v"(wlo) : "memory");
    asm volatile("v_mfma_f32_16x16x32_bf16 %0, %1, %2, %0" : "+v"(o[1][2]) : "a"(WA[15][1]), "v"(HX1[2]));
    asm volatile("ds_read_b128 %0, %1 offset:33792" : "=a"(WA[16][3]) : "v"(wlo) : "memory");
    asm volatile("s_addk_i32 m0, 0x400" ::: "scc", "memory");
    asm volatile("v_mfma_f32_16x16x32_bf16 %0, %1, %2, %0" : "+v"(o[2][3]) : "a"(WA[15][2]), "v"(HX1[3]));
    asm volatile("ds_read_b128 %0, %1 offset:34816" : "=a"(WA[16][4]) : "v"(wlo) : "memory");
    asm volatile("ds_read_b128 %0, %1 offset:33792" : "=v"(HK1[16]) : "v"(hb) : "memory");
    asm volatile("buffer_load_dwordx4 %0, %1, %2 offen offset:0 sc1 lds" :: "v"(hv1), "s"(rS), "s"(sh) : "memory");
    asm volatile("v_mfma_f32_16x16x32_bf16 %0, %1, %2, %0" : "+v"(o[2][2]) : "a"(WA[15][2]), "v"(HX1[2]));
    asm volatile("ds_read_b128 %0, %1 offset:32768" : "=v"(HK0[16]) : "v"(hb) : "memory");
    asm volatile("v_mfma_f32_16x16x32_bf16 %0, %1, %2, %0" : "+v"(o[3][3]) : "a"(WA[15][3]), "v"(HX1[3]));
    sh = 4194304 + (c*88 + ksp + 4)*8192;
    asm volatile("" : "+s"(sh));
    asm volatile("s_add_u32 m0, %0, 36864" :: "s"(lh) : "scc", "memory");
    asm volatile("v_mfma_f32_16x16x32_bf16 %0, %1, %2, %0" : "+v"(o[3][2]) : "a"(WA[15][3]), "v"(HX1[2]));
    asm volatile("buffer_load_dwordx4 %0, %1, %2 offen offset:0 sc1 lds" :: "v"(hv0), "s"(rS), "s"(sh) : "memory");
    asm volatile("v_mfma_f32_16x16x32_bf16 %0, %1, %2, %0" : "+v"(o[4][3]) : "a"(WA[15][4]), "v"(HX1[3]));
    asm volatile("v_mfma_f32_16x16x32_bf16 %0, %1, %2, %0" : "+v"(o[4][2]) : "a"(WA[15][4]), "v"(HX1[2]));
    asm volatile("s_addk_i32 m0, 0x400" ::: "scc", "memory");
    asm volatile("s_waitcnt lgkmcnt(0)" ::: "memory");
    asm volatile("v_mfma_f32_16x16x32_bf16 %0, %1, %2, %0" : "+v"(o[0][3]) : "a"(WA[16][0]), "v"(HX0[3]));
    asm volatile("ds_read_b64_tr_b16 %0, %1 offset:44032" : "=v"(tl[3]) : "v"(ht[0]) : "memory");
    asm volatile("ds_read_b64_tr_b16 %0, %1 offset:44032" : "=v"(th[3]) : "v"(ht[1]) : "memory");
    HX1[3] = cat(tl[3], th[3]);
    asm volatile("ds_read_b64_tr_b16 %0, %1 offset:43008" : "=v"(tl[2]) : "v"(ht[0]) : "memory");
    asm volatile("buffer_load_dwordx4 %0, %1, %2 offen offset:0 sc1 lds" :: "v"(hv1), "s"(rS), "s"(sh) : "memory");
    asm volatile("v_mfma_f32_16x16x32_bf16 %0, %1, %2, %0" : "+v"(o[0][2]) : "a"(WA[16][0]), "v"(HX0[2]));
    asm volatile("ds_read_b64_tr_b16 %0, %1 offset:43008" : "=v"(th[2]) : "v"(ht[1]) : "memory");
    HX1[2] = cat(tl[2], th[2]);
    asm volatile("ds_read_b128 %0, %1 offset:20480" : "=a"(WA[17][0]) : "v"(wlo) : "memory");
    asm volatile("v_mfma_f32_16x16x32_bf16 %0, %1, %2, %0" : "+v"(o[1][3]) : "a"(WA[16][1]), "v"(HX0[3]));
    asm volatile("ds_read_b128 %0, %1 offset:21504" : "=a"(WA[17][1]) : "v"(wlo) : "memory");
    asm volatile("ds_read_b128 %0, %1 offset:22528" : "=a"(WA[17][2]) : "v"(wlo) : "memory");
    asm volatile("v_mfma_f32_16x16x32_bf16 %0, %1, %2, %0" : "+v"(o[1][2]) : "a"(WA[16][1]), "v"(HX0[2]));
    asm volatile("ds_read_b128 %0, %1 offset:23552" : "=a"(WA[17][3]) : "v"(wlo) : "memory");
    asm volatile("ds_read_b128 %0, %1 offset:24576" : "=a"(WA[17][4]) : "v"(wlo) : "memory");
    asm volatile("v_mfma_f32_16x16x32_bf16 %0, %1, %2, %0" : "+v"(o[2][3]) : "a"(WA[16][2]), "v"(HX0[3]));
    asm volatile("ds_read_b64_tr_b16 %0, %1 offset:41984" : "=v"(tl[1]) : "v"(ht[0]) : "memory");
    asm volatile("ds_read_b64_tr_b16 %0, %1 offset:41984" : "=v"(th[1]) : "v"(ht[1]) : "memory");
    HK1[17] = cat(tl[1], th[1]);
    asm volatile("v_mfma_f32_16x16x32_bf16 %0, %1, %2, %0" : "+v"(o[2][2]) : "a"(WA[16][2]), "v"(HX0[2]));
    asm volatile("ds_read_b64_tr_b16 %0, %1 offset:40960" : "=v"(tl[0]) : "v"(ht[0]) : "memory");
    asm volatile("ds_read_b64_tr_b16 %0, %1 offset:40960" : "=v"(th[0]) : "v"(ht[1]) : "memory");
    HK0[17] = cat(tl[0], th[0]);
    asm volatile("v_mfma_f32_16x16x32_bf16 %0, %1, %2, %0" : "+v"(o[3][3]) : "a"(WA[16][3]), "v"(HX0[3]));
    asm volatile("v_mfma_f32_16x16x32_bf16 %0, %1, %2, %0" : "+v"(o[3][2]) : "a"(WA[16][3]), "v"(HX0[2]));
    asm volatile("v_mfma_f32_16x16x32_bf16 %0, %1, %2, %0" : "+v"(o[4][3]) : "a"(WA[16][4]), "v"(HX0[3]));
    asm volatile("v_mfma_f32_16x16x32_bf16 %0, %1, %2, %0" : "+v"(o[4][2]) : "a"(WA[16][4]), "v"(HX0[2]));
    asm volatile("s_waitcnt vmcnt(4) lgkmcnt(0)\n\ts_barrier" ::: "memory");
    asm volatile("v_mfma_f32_16x16x32_bf16 %0, %1, %2, %0" : "+v"(o[0][3]) : "a"(WA[17][0]), "v"(HX1[3]));
    asm volatile("ds_read_b64_tr_b16 %0, %1 offset:3072" : "=v"(tl[3]) : "v"(ht[0]) : "memory");
    asm volatile("ds_read_b64_tr_b16 %0, %1 offset:3072" : "=v"(th[3]) : "v"(ht[1]) : "memory");
    HX0[3] = cat(tl[3], th[3]);
    asm volatile("ds_read_b64_tr_b16 %0, %1 offset:2048" : "=v"(tl[2]) : "v"(ht[0]) : "memory");
    asm volatile("v_mfma_f32_16x16x32_bf16 %0, %1, %2, %0" : "+v"(o[0][2]) : "a"(WA[17][0]), "v"(HX1[2]));
    asm volatile("ds_read_b64_tr_b16 %0, %1 offset:2048" : "=v"(th[2]) : "v"(ht[1]) : "memory");
    HX0[2] = cat(tl[2], th[2]);
    asm volatile("ds_read_b128 %0, %1 offset:10240" : "=a"(WA[18][0]) : "v"(wlo) : "memory");
    asm volatile("buffer_load_dwordx2 %0, %1, %2, 0 offen sc1" : "=v"(es) : "v"(40960 + tok*8), "s"(rS) : "memory");
    asm volatile("v_mfma_f32_16x16x32_bf16 %0, %1, %2, %0" : "+v"(o[1][3]) : "a"(WA[17][1]), "v"(HX1[3]));
    asm volatile("ds_read_b128 %0, %1 offset:11264" : "=a"(WA[18][1]) : "v"(wlo) : "memory");
    asm volatile("ds_read_b128 %0, %1 offset:12288" : "=a"(WA[18][2]) : "v"(wlo) : "memory");
    asm volatile("v_mfma_f32_16x16x32_bf16 %0, %1, %2, %0" : "+v"(o[1][2]) : "a"(WA[17][1]), "v"(HX1[2]));
    asm volatile("ds_read_b128 %0, %1 offset:13312" : "=a"(WA[18][3]) : "v"(wlo) : "memory");
    asm volatile("ds_read_b128 %0, %1 offset:14336" : "=a"(WA[18][4]) : "v"(wlo) : "memory");
    asm volatile("buffer_load_dwordx2 %0, %1, %2, 0 offen offset:0" : "=v"(ex[0]) : "v"(xvo), "s"(rX) : "memory");
    asm volatile("v_mfma_f32_16x16x32_bf16 %0, %1, %2, %0" : "+v"(o[2][3]) : "a"(WA[17][2]), "v"(HX1[3]));
    asm volatile("ds_read_b64_tr_b16 %0, %1 offset:1024" : "=v"(tl[1]) : "v"(ht[0]) : "memory");
    asm volatile("ds_read_b64_tr_b16 %0, %1 offset:1024" : "=v"(th[1]) : "v"(ht[1]) : "memory");
    HK1[18] = cat(tl[1], th[1]);
    asm volatile("v_mfma_f32_16x16x32_bf16 %0, %1, %2, %0" : "+v"(o[2][2]) : "a"(WA[17][2]), "v"(HX1[2]));
    asm volatile("ds_read_b64_tr_b16 %0, %1 offset:0" : "=v"(tl[0]) : "v"(ht[0]) : "memory");
    asm volatile("ds_read_b64_tr_b16 %0, %1 offset:0" : "=v"(th[0]) : "v"(ht[1]) : "memory");
    HK0[18] = cat(tl[0], th[0]);
    asm volatile("buffer_load_dwordx2 %0, %1, %2, 0 offen offset:32" : "=v"(ex[1]) : "v"(xvo), "s"(rX) : "memory");
    asm volatile("v_mfma_f32_16x16x32_bf16 %0, %1, %2, %0" : "+v"(o[3][3]) : "a"(WA[17][3]), "v"(HX1[3]));
    asm volatile("v_mfma_f32_16x16x32_bf16 %0, %1, %2, %0" : "+v"(o[3][2]) : "a"(WA[17][3]), "v"(HX1[2]));
    asm volatile("buffer_load_dwordx2 %0, %1, %2, 0 offen offset:64" : "=v"(ex[2]) : "v"(xvo), "s"(rX) : "memory");
    asm volatile("v_mfma_f32_16x16x32_bf16 %0, %1, %2, %0" : "+v"(o[4][3]) : "a"(WA[17][4]), "v"(HX1[3]));
    asm volatile("v_mfma_f32_16x16x32_bf16 %0, %1, %2, %0" : "+v"(o[4][2]) : "a"(WA[17][4]), "v"(HX1[2]));
    asm volatile("buffer_load_dwordx2 %0, %1, %2, 0 offen offset:96" : "=v"(ex[3]) : "v"(xvo), "s"(rX) : "memory");
    asm volatile("s_waitcnt lgkmcnt(0)" ::: "memory");
    asm volatile("v_mfma_f32_16x16x32_bf16 %0, %1, %2, %0" : "+v"(o[0][3]) : "a"(WA[18][0]), "v"(HX0[3]));
    asm volatile("ds_read_b64_tr_b16 %0, %1 offset:11264" : "=v"(tl[3]) : "v"(ht[0]) : "memory");
    asm volatile("ds_read_b64_tr_b16 %0, %1 offset:11264" : "=v"(th[3]) : "v"(ht[1]) : "memory");
    HX1[3] = cat(tl[3], th[3]);
    asm volatile("ds_read_b64_tr_b16 %0, %1 offset:10240" : "=v"(tl[2]) : "v"(ht[0]) : "memory");
    asm volatile("v_mfma_f32_16x16x32_bf16 %0, %1, %2, %0" : "+v"(o[0][2]) : "a"(WA[18][0]), "v"(HX0[2]));
    asm volatile("ds_read_b64_tr_b16 %0, %1 offset:10240" : "=v"(th[2]) : "v"(ht[1]) : "memory");
    HX1[2] = cat(tl[2], th[2]);
    asm volatile("ds_read_b128 %0, %1 offset:0" : "=a"(WA[19][0]) : "v"(wlo) : "memory");
    asm volatile("buffer_load_dwordx2 %0, %1, %2, 0 offen offset:128" : "=v"(ex[4]) : "v"(xvo), "s"(rX) : "memory");
    asm volatile("v_mfma_f32_16x16x32_bf16 %0, %1, %2, %0" : "+v"(o[1][3]) : "a"(WA[18][1]), "v"(HX0[3]));
    asm volatile("ds_read_b128 %0, %1 offset:1024" : "=a"(WA[19][1]) : "v"(wlo) : "memory");
    asm volatile("ds_read_b128 %0, %1 offset:2048" : "=a"(WA[19][2]) : "v"(wlo) : "memory");
    asm volatile("v_mfma_f32_16x16x32_bf16 %0, %1, %2, %0" : "+v"(o[1][2]) : "a"(WA[18][1]), "v"(HX0[2]));
    asm volatile("ds_read_b128 %0, %1 offset:3072" : "=a"(WA[19][3]) : "v"(wlo) : "memory");
    asm volatile("ds_read_b128 %0, %1 offset:4096" : "=a"(WA[19][4]) : "v"(wlo) : "memory");
    asm volatile("v_mfma_f32_16x16x32_bf16 %0, %1, %2, %0" : "+v"(o[2][3]) : "a"(WA[18][2]), "v"(HX0[3]));
    asm volatile("ds_read_b64_tr_b16 %0, %1 offset:9216" : "=v"(tl[1]) : "v"(ht[0]) : "memory");
    asm volatile("ds_read_b64_tr_b16 %0, %1 offset:9216" : "=v"(th[1]) : "v"(ht[1]) : "memory");
    HK1[19] = cat(tl[1], th[1]);
    asm volatile("v_mfma_f32_16x16x32_bf16 %0, %1, %2, %0" : "+v"(o[2][2]) : "a"(WA[18][2]), "v"(HX0[2]));
    asm volatile("ds_read_b64_tr_b16 %0, %1 offset:8192" : "=v"(tl[0]) : "v"(ht[0]) : "memory");
    asm volatile("ds_read_b64_tr_b16 %0, %1 offset:8192" : "=v"(th[0]) : "v"(ht[1]) : "memory");
    HK0[19] = cat(tl[0], th[0]);
    asm volatile("v_mfma_f32_16x16x32_bf16 %0, %1, %2, %0" : "+v"(o[3][3]) : "a"(WA[18][3]), "v"(HX0[3]));
    asm volatile("v_mfma_f32_16x16x32_bf16 %0, %1, %2, %0" : "+v"(o[3][2]) : "a"(WA[18][3]), "v"(HX0[2]));
    asm volatile("v_mfma_f32_16x16x32_bf16 %0, %1, %2, %0" : "+v"(o[4][3]) : "a"(WA[18][4]), "v"(HX0[3]));
    asm volatile("v_mfma_f32_16x16x32_bf16 %0, %1, %2, %0" : "+v"(o[4][2]) : "a"(WA[18][4]), "v"(HX0[2]));
    asm volatile("s_waitcnt vmcnt(6) lgkmcnt(0)\n\ts_barrier" ::: "memory");
    asm volatile("v_mfma_f32_16x16x32_bf16 %0, %1, %2, %0" : "+v"(o[0][3]) : "a"(WA[19][0]), "v"(HX1[3]));
    asm volatile("ds_read_b64_tr_b16 %0, %1 offset:19456" : "=v"(tl[3]) : "v"(ht[0]) : "memory");
    asm volatile("ds_read_b64_tr_b16 %0, %1 offset:19456" : "=v"(th[3]) : "v"(ht[1]) : "memory");
    HX0[3] = cat(tl[3], th[3]);
    asm volatile("ds_read_b64_tr_b16 %0, %1 offset:18432" : "=v"(tl[2]) : "v"(ht[0]) : "memory");
    asm volatile("v_mfma_f32_16x16x32_bf16 %0, %1, %2, %0" : "+v"(o[0][2]) : "a"(WA[19][0]), "v"(HX1[2]));
    asm volatile("ds_read_b64_tr_b16 %0, %1 offset:18432" : "=v"(th[2]) : "v"(ht[1]) : "memory");
    HX0[2] = cat(tl[2], th[2]);
    asm volatile("ds_read_b128 %0, %1 offset:40960" : "=a"(WA[20][0]) : "v"(whi) : "memory");
    asm volatile("v_mfma_f32_16x16x32_bf16 %0, %1, %2, %0" : "+v"(o[1][3]) : "a"(WA[19][1]), "v"(HX1[3]));
    asm volatile("ds_read_b128 %0, %1 offset:41984" : "=a"(WA[20][1]) : "v"(whi) : "memory");
    asm volatile("ds_read_b128 %0, %1 offset:43008" : "=a"(WA[20][2]) : "v"(whi) : "memory");
    asm volatile("v_mfma_f32_16x16x32_bf16 %0, %1, %2, %0" : "+v"(o[1][2]) : "a"(WA[19][1]), "v"(HX1[2]));
    asm volatile("ds_read_b128 %0, %1 offset:44032" : "=a"(WA[20][3]) : "v"(whi) : "memory");
    asm volatile("ds_read_b128 %0, %1 offset:45056" : "=a"(WA[20][4]) : "v"(whi) : "memory");
    asm volatile("v_mfma_f32_16x16x32_bf16 %0, %1, %2, %0" : "+v"(o[2][3]) : "a"(WA[19][2]), "v"(HX1[3]));
    asm volatile("ds_read_b64_tr_b16 %0, %1 offset:17408" : "=v"(tl[1]) : "v"(ht[0]) : "memory");
    asm volatile("ds_read_b64_tr_b16 %0, %1 offset:17408" : "=v"(th[1]) : "v"(ht[1]) : "memory");
    HK1[20] = cat(tl[1], th[1]);
    asm volatile("v_mfma_f32_16x16x32_bf16 %0, %1, %2, %0" : "+v"(o[2][2]) : "a"(WA[19][2]), "v"(HX1[2]));
    asm volatile("ds_read_b64_tr_b16 %0, %1 offset:16384" : "=v"(tl[0]) : "v"(ht[0]) : "memory");
    asm volatile("ds_read_b64_tr_b16 %0, %1 offset:16384" : "=v"(th[0]) : "v"(ht[1]) : "memory");
    HK0[20] = cat(tl[0], th[0]);
    asm volatile("v_mfma_f32_16x16x32_bf16 %0, %1, %2, %0" : "+v"(o[3][3]) : "a"(WA[19][3]), "v"(HX1[3]));
    asm volatile("v_mfma_f32_16x16x32_bf16 %0, %1, %2, %0" : "+v"(o[3][2]) : "a"(WA[19][3]), "v"(HX1[2]));
    asm volatile("v_mfma_f32_16x16x32_bf16 %0, %1, %2, %0" : "+v"(o[4][3]) : "a"(WA[19][4]), "v"(HX1[3]));
    asm volatile("v_mfma_f32_16x16x32_bf16 %0, %1, %2, %0" : "+v"(o[4][2]) : "a"(WA[19][4]), "v"(HX1[2]));
    asm volatile("s_waitcnt lgkmcnt(0)" ::: "memory");
    const unsigned pd = (idx << 3) | (xc & 1);
    asm volatile("v_mfma_f32_16x16x32_bf16 %0, %1, %2, %0" : "+v"(o[0][3]) : "a"(WA[20][0]), "v"(HX0[3]));
    asm volatile("ds_read_b64_tr_b16 %0, %1 offset:27648" : "=v"(tl[3]) : "v"(ht[0]) : "memory");
    asm volatile("ds_read_b64_tr_b16 %0, %1 offset:27648" : "=v"(th[3]) : "v"(ht[1]) : "memory");
    HX1[3] = cat(tl[3], th[3]);
    asm volatile("ds_read_b64_tr_b16 %0, %1 offset:26624" : "=v"(tl[2]) : "v"(ht[0]) : "memory");
    asm volatile("v_mfma_f32_16x16x32_bf16 %0, %1, %2, %0" : "+v"(o[1][3]) : "a"(WA[20][1]), "v"(HX0[3]));
    asm volatile("ds_read_b64_tr_b16 %0, %1 offset:26624" : "=v"(th[2]) : "v"(ht[1]) : "memory");
    HX1[2] = cat(tl[2], th[2]);
    asm volatile("ds_read_b128 %0, %1 offset:30720" : "=a"(WA[21][0]) : "v"(whi) : "memory");
    asm volatile("v_mfma_f32_16x16x32_bf16 %0, %1, %2, %0" : "+v"(o[2][3]) : "a"(WA[20][2]), "v"(HX0[3]));
    asm volatile("ds_read_b128 %0, %1 offset:31744" : "=a"(WA[21][1]) : "v"(whi) : "memory");
    asm volatile("ds_read_b128 %0, %1 offset:32768" : "=a"(WA[21][2]) : "v"(whi) : "memory");
    asm volatile("v_mfma_f32_16x16x32_bf16 %0, %1, %2, %0" : "+v"(o[3][3]) : "a"(WA[20][3]), "v"(HX0[3]));
    asm volatile("ds_read_b128 %0, %1 offset:33792" : "=a"(WA[21][3]) : "v"(whi) : "memory");
    asm volatile("ds_read_b128 %0, %1 offset:34816" : "=a"(WA[21][4]) : "v"(whi) : "memory");
    asm volatile("v_mfma_f32_16x16x32_bf16 %0, %1, %2, %0" : "+v"(o[4][3]) : "a"(WA[20][4]), "v"(HX0[3]));
    asm volatile("ds_read_b64_tr_b16 %0, %1 offset:25600" : "=v"(tl[1]) : "v"(ht[0]) : "memory");
    asm volatile("ds_read_b64_tr_b16 %0, %1 offset:25600" : "=v"(th[1]) : "v"(ht[1]) : "memory");
    HK1[21] = cat(tl[1], th[1]);
    asm volatile("v_mfma_f32_16x16x32_bf16 %0, %1, %2, %0" : "+v"(o[0][2]) : "a"(WA[20][0]), "v"(HX0[2]));
    asm volatile("ds_read_b64_tr_b16 %0, %1 offset:24576" : "=v"(tl[0]) : "v"(ht[0]) : "memory");
    asm volatile("ds_read_b64_tr_b16 %0, %1 offset:24576" : "=v"(th[0]) : "v"(ht[1]) : "memory");
    HK0[21] = cat(tl[0], th[0]);
    asm volatile("v_mfma_f32_16x16x32_bf16 %0, %1, %2, %0" : "+v"(o[1][2]) : "a"(WA[20][1]), "v"(HX0[2]));
    asm volatile("v_mfma_f32_16x16x32_bf16 %0, %1, %2, %0" : "+v"(o[2][2]) : "a"(WA[20][2]), "v"(HX0[2]));
    asm volatile("v_mfma_f32_16x16x32_bf16 %0, %1, %2, %0" : "+v"(o[3][2]) : "a"(WA[20][3]), "v"(HX0[2]));
    asm volatile("v_mfma_f32_16x16x32_bf16 %0, %1, %2, %0" : "+v"(o[4][2]) : "a"(WA[20][4]), "v"(HX0[2]));
    asm volatile("s_waitcnt lgkmcnt(0)" ::: "memory");
    unsigned f3v; const unsigned f3 = 3964928 + (b*4 + w)*128 + (lane & 3)*4;
    asm volatile("v_mfma_f32_16x16x32_bf16 %0, %1, %2, %0" : "+v"(o[0][3]) : "a"(WA[21][0]), "v"(HX1[3]));
    asm volatile("v_mfma_f32_16x16x32_bf16 %0, %1, %2, %0" : "+v"(o[1][3]) : "a"(WA[21][1]), "v"(HX1[3]));
    asm volatile("v_mfma_f32_16x16x32_bf16 %0, %1, %2, %0" : "+v"(o[2][3]) : "a"(WA[21][2]), "v"(HX1[3]));
    asm volatile("v_mfma_f32_16x16x32_bf16 %0, %1, %2, %0" : "+v"(o[3][3]) : "a"(WA[21][3]), "v"(HX1[3]));
    asm volatile("v_mfma_f32_16x16x32_bf16 %0, %1, %2, %0" : "+v"(o[4][3]) : "a"(WA[21][4]), "v"(HX1[3]));
    asm volatile("v_mfma_f32_16x16x32_bf16 %0, %1, %2, %0" : "+v"(o[0][2]) : "a"(WA[21][0]), "v"(HX1[2]));
    asm volatile("v_mfma_f32_16x16x32_bf16 %0, %1, %2, %0" : "+v"(o[1][2]) : "a"(WA[21][1]), "v"(HX1[2]));
    asm volatile("v_mfma_f32_16x16x32_bf16 %0, %1, %2, %0" : "+v"(o[2][2]) : "a"(WA[21][2]), "v"(HX1[2]));
    asm volatile("v_mfma_f32_16x16x32_bf16 %0, %1, %2, %0" : "+v"(o[3][2]) : "a"(WA[21][3]), "v"(HX1[2]));
    unsigned q3_0, q3_1, q3_2, q3_3, q3_4, q3_5, q3_6, q3_7, q3_8, q3_9; const unsigned ps3 = 15728640 + (((pd + 2*((kq + 3) & 3))*3 + 2)*4 + w)*2560;
    asm volatile("v_cvt_pk_bf16_f32 %0, %1, %2" : "=v"(q3_0) : "v"(o[0][3].x), "v"(o[0][3].y));
    asm volatile("v_mfma_f32_16x16x32_bf16 %0, %1, %2, %0" : "+v"(o[4][2]) : "a"(WA[21][4]), "v"(HX1[2]));
    asm volatile("v_cvt_pk_bf16_f32 %0, %1, %2" : "=v"(q3_1) : "v"(o[0][3].z), "v"(o[0][3].w));
    asm volatile("v_mfma_f32_16x16x32_bf16 %0, %1, %2, %0" : "+v"(o[0][1]) : "a"(WA[10][0]), "v"(HK1[10]));
    asm volatile("v_cvt_pk_bf16_f32 %0, %1, %2" : "=v"(q3_2) : "v"(o[1][3].x), "v"(o[1][3].y));
    asm volatile("v_mfma_f32_16x16x32_bf16 %0, %1, %2, %0" : "+v"(o[1][1]) : "a"(WA[10][1]), "v"(HK1[10]));
    asm volatile("v_cvt_pk_bf16_f32 %0, %1, %2" : "=v"(q3_3) : "v"(o[1][3].z), "v"(o[1][3].w));
    st128((u4{q3_0, q3_1, q3_2, q3_3}), rS, vl16, ps3, SC1);
    asm volatile("v_mfma_f32_16x16x32_bf16 %0, %1, %2, %0" : "+v"(o[2][1]) : "a"(WA[10][2]), "v"(HK1[10]));
    asm volatile("v_cvt_pk_bf16_f32 %0, %1, %2" : "=v"(q3_4) : "v"(o[2][3].x), "v"(o[2][3].y));
    asm volatile("v_mfma_f32_16x16x32_bf16 %0, %1, %2, %0" : "+v"(o[3][1]) : "a"(WA[10][3]), "v"(HK1[10]));
    asm volatile("v_cvt_pk_bf16_f32 %0, %1, %2" : "=v"(q3_5) : "v"(o[2][3].z), "v"(o[2][3].w));
    asm volatile("v_mfma_f32_16x16x32_bf16 %0, %1, %2, %0" : "+v"(o[4][1]) : "a"(WA[10][4]), "v"(HK1[10]));
    asm volatile("v_cvt_pk_bf16_f32 %0, %1, %2" : "=v"(q3_6) : "v"(o[3][3].x), "v"(o[3][3].y));
    asm volatile("v_mfma_f32_16x16x32_bf16 %0, %1, %2, %0" : "+v"(o[0][1]) : "a"(WA[11][0]), "v"(HK1[11]));
    asm volatile("v_cvt_pk_bf16_f32 %0, %1, %2" : "=v"(q3_7) : "v"(o[3][3].z), "v"(o[3][3].w));
    st128((u4{q3_4, q3_5, q3_6, q3_7}), rS, vl16 + 1024, ps3, SC1);
    asm volatile("v_mfma_f32_16x16x32_bf16 %0, %1, %2, %0" : "+v"(o[1][1]) : "a"(WA[11][1]), "v"(HK1[11]));
    asm volatile("v_cvt_pk_bf16_f32 %0, %1, %2" : "=v"(q3_8) : "v"(o[4][3].x), "v"(o[4][3].y));
    asm volatile("v_mfma_f32_16x16x32_bf16 %0, %1, %2, %0" : "+v"(o[2][1]) : "a"(WA[11][2]), "v"(HK1[11]));
    asm volatile("v_cvt_pk_bf16_f32 %0, %1, %2" : "=v"(q3_9) : "v"(o[4][3].z), "v"(o[4][3].w));
    __builtin_amdgcn_raw_buffer_store_b64(u2{q3_8, q3_9}, rS, lane*8 + 2048, ps3, SC1);
    asm volatile("v_mfma_f32_16x16x32_bf16 %0, %1, %2, %0" : "+v"(o[3][1]) : "a"(WA[11][3]), "v"(HK1[11]));
    unsigned q2_0, q2_1, q2_2, q2_3, q2_4, q2_5, q2_6, q2_7, q2_8, q2_9; const unsigned ps2 = 15728640 + (((pd + 2*((kq + 2) & 3))*3 + 1)*4 + w)*2560;
    asm volatile("v_cvt_pk_bf16_f32 %0, %1, %2" : "=v"(q2_0) : "v"(o[0][2].x), "v"(o[0][2].y));
    asm volatile("v_mfma_f32_16x16x32_bf16 %0, %1, %2, %0" : "+v"(o[4][1]) : "a"(WA[11][4]), "v"(HK1[11]));
    asm volatile("v_cvt_pk_bf16_f32 %0, %1, %2" : "=v"(q2_1) : "v"(o[0][2].z), "v"(o[0][2].w));
    asm volatile("v_mfma_f32_16x16x32_bf16 %0, %1, %2, %0" : "+v"(o[0][1]) : "a"(WA[12][0]), "v"(HK1[12]));
    asm volatile("v_cvt_pk_bf16_f32 %0, %1, %2" : "=v"(q2_2) : "v"(o[1][2].x), "v"(o[1][2].y));
    asm volatile("v_mfma_f32_16x16x32_bf16 %0, %1, %2, %0" : "+v"(o[1][1]) : "a"(WA[12][1]), "v"(HK1[12]));
    asm volatile("v_cvt_pk_bf16_f32 %0, %1, %2" : "=v"(q2_3) : "v"(o[1][2].z), "v"(o[1][2].w));
    st128((u4{q2_0, q2_1, q2_2, q2_3}), rS, vl16, ps2, SC1);
    asm volatile("v_mfma_f32_16x16x32_bf16 %0, %1, %2, %0" : "+v"(o[2][1]) : "a"(WA[12][2]), "v"(HK1[12]));
    asm volatile("v_cvt_pk_bf16_f32 %0, %1, %2" : "=v"(q2_4) : "v"(o[2][2].x), "v"(o[2][2].y));
    asm volatile("v_mfma_f32_16x16x32_bf16 %0, %1, %2, %0" : "+v"(o[3][1]) : "a"(WA[12][3]), "v"(HK1[12]));
    asm volatile("v_cvt_pk_bf16_f32 %0, %1, %2" : "=v"(q2_5) : "v"(o[2][2].z), "v"(o[2][2].w));
    asm volatile("v_mfma_f32_16x16x32_bf16 %0, %1, %2, %0" : "+v"(o[4][1]) : "a"(WA[12][4]), "v"(HK1[12]));
    asm volatile("v_cvt_pk_bf16_f32 %0, %1, %2" : "=v"(q2_6) : "v"(o[3][2].x), "v"(o[3][2].y));
    asm volatile("v_mfma_f32_16x16x32_bf16 %0, %1, %2, %0" : "+v"(o[0][1]) : "a"(WA[13][0]), "v"(HK1[13]));
    asm volatile("v_cvt_pk_bf16_f32 %0, %1, %2" : "=v"(q2_7) : "v"(o[3][2].z), "v"(o[3][2].w));
    st128((u4{q2_4, q2_5, q2_6, q2_7}), rS, vl16 + 1024, ps2, SC1);
    asm volatile("v_mfma_f32_16x16x32_bf16 %0, %1, %2, %0" : "+v"(o[1][1]) : "a"(WA[13][1]), "v"(HK1[13]));
    asm volatile("v_cvt_pk_bf16_f32 %0, %1, %2" : "=v"(q2_8) : "v"(o[4][2].x), "v"(o[4][2].y));
    asm volatile("v_mfma_f32_16x16x32_bf16 %0, %1, %2, %0" : "+v"(o[2][1]) : "a"(WA[13][2]), "v"(HK1[13]));
    asm volatile("v_cvt_pk_bf16_f32 %0, %1, %2" : "=v"(q2_9) : "v"(o[4][2].z), "v"(o[4][2].w));
    __builtin_amdgcn_raw_buffer_store_b64(u2{q2_8, q2_9}, rS, lane*8 + 2048, ps2, SC1);
    asm volatile("v_mfma_f32_16x16x32_bf16 %0, %1, %2, %0" : "+v"(o[3][1]) : "a"(WA[13][3]), "v"(HK1[13]));
    asm volatile("v_mfma_f32_16x16x32_bf16 %0, %1, %2, %0" : "+v"(o[4][1]) : "a"(WA[13][4]), "v"(HK1[13]));
    asm volatile("v_mfma_f32_16x16x32_bf16 %0, %1, %2, %0" : "+v"(o[0][1]) : "a"(WA[14][0]), "v"(HK1[14]));
    asm volatile("v_mfma_f32_16x16x32_bf16 %0, %1, %2, %0" : "+v"(o[1][1]) : "a"(WA[14][1]), "v"(HK1[14]));
    asm volatile("v_mfma_f32_16x16x32_bf16 %0, %1, %2, %0" : "+v"(o[2][1]) : "a"(WA[14][2]), "v"(HK1[14]));
    asm volatile("v_mfma_f32_16x16x32_bf16 %0, %1, %2, %0" : "+v"(o[3][1]) : "a"(WA[14][3]), "v"(HK1[14]));
    asm volatile("v_mfma_f32_16x16x32_bf16 %0, %1, %2, %0" : "+v"(o[4][1]) : "a"(WA[14][4]), "v"(HK1[14]));
    asm volatile("v_mfma_f32_16x16x32_bf16 %0, %1, %2, %0" : "+v"(o[0][1]) : "a"(WA[15][0]), "v"(HK1[15]));
    asm volatile("v_mfma_f32_16x16x32_bf16 %0, %1, %2, %0" : "+v"(o[1][1]) : "a"(WA[15][1]), "v"(HK1[15]));
    asm volatile("v_mfma_f32_16x16x32_bf16 %0, %1, %2, %0" : "+v"(o[2][1]) : "a"(WA[15][2]), "v"(HK1[15]));
    asm volatile("v_mfma_f32_16x16x32_bf16 %0, %1, %2, %0" : "+v"(o[3][1]) : "a"(WA[15][3]), "v"(HK1[15]));
    asm volatile("v_mfma_f32_16x16x32_bf16 %0, %1, %2, %0" : "+v"(o[4][1]) : "a"(WA[15][4]), "v"(HK1[15]));
    asm volatile("v_mfma_f32_16x16x32_bf16 %0, %1, %2, %0" : "+v"(o[0][1]) : "a"(WA[16][0]), "v"(HK1[16]));
    asm volatile("v_mfma_f32_16x16x32_bf16 %0, %1, %2, %0" : "+v"(o[1][1]) : "a"(WA[16][1]), "v"(HK1[16]));
    asm volatile("v_mfma_f32_16x16x32_bf16 %0, %1, %2, %0" : "+v"(o[2][1]) : "a"(WA[16][2]), "v"(HK1[16]));
    asm volatile("v_mfma_f32_16x16x32_bf16 %0, %1, %2, %0" : "+v"(o[3][1]) : "a"(WA[16][3]), "v"(HK1[16]));
    asm volatile("v_mfma_f32_16x16x32_bf16 %0, %1, %2, %0" : "+v"(o[4][1]) : "a"(WA[16][4]), "v"(HK1[16]));
    asm volatile("v_mfma_f32_16x16x32_bf16 %0, %1, %2, %0" : "+v"(o[0][1]) : "a"(WA[17][0]), "v"(HK1[17]));
    asm volatile("v_mfma_f32_16x16x32_bf16 %0, %1, %2, %0" : "+v"(o[1][1]) : "a"(WA[17][1]), "v"(HK1[17]));
    asm volatile("v_mfma_f32_16x16x32_bf16 %0, %1, %2, %0" : "+v"(o[2][1]) : "a"(WA[17][2]), "v"(HK1[17]));
    asm volatile("v_mfma_f32_16x16x32_bf16 %0, %1, %2, %0" : "+v"(o[3][1]) : "a"(WA[17][3]), "v"(HK1[17]));
    asm volatile("v_mfma_f32_16x16x32_bf16 %0, %1, %2, %0" : "+v"(o[4][1]) : "a"(WA[17][4]), "v"(HK1[17]));
    asm volatile("v_mfma_f32_16x16x32_bf16 %0, %1, %2, %0" : "+v"(o[0][1]) : "a"(WA[18][0]), "v"(HK1[18]));
    asm volatile("v_mfma_f32_16x16x32_bf16 %0, %1, %2, %0" : "+v"(o[1][1]) : "a"(WA[18][1]), "v"(HK1[18]));
    asm volatile("v_mfma_f32_16x16x32_bf16 %0, %1, %2, %0" : "+v"(o[2][1]) : "a"(WA[18][2]), "v"(HK1[18]));
    asm volatile("v_mfma_f32_16x16x32_bf16 %0, %1, %2, %0" : "+v"(o[3][1]) : "a"(WA[18][3]), "v"(HK1[18]));
    asm volatile("v_mfma_f32_16x16x32_bf16 %0, %1, %2, %0" : "+v"(o[4][1]) : "a"(WA[18][4]), "v"(HK1[18]));
    asm volatile("v_mfma_f32_16x16x32_bf16 %0, %1, %2, %0" : "+v"(o[0][1]) : "a"(WA[19][0]), "v"(HK1[19]));
    asm volatile("v_mfma_f32_16x16x32_bf16 %0, %1, %2, %0" : "+v"(o[1][1]) : "a"(WA[19][1]), "v"(HK1[19]));
    asm volatile("v_mfma_f32_16x16x32_bf16 %0, %1, %2, %0" : "+v"(o[2][1]) : "a"(WA[19][2]), "v"(HK1[19]));
    asm volatile("v_mfma_f32_16x16x32_bf16 %0, %1, %2, %0" : "+v"(o[3][1]) : "a"(WA[19][3]), "v"(HK1[19]));
    asm volatile("v_mfma_f32_16x16x32_bf16 %0, %1, %2, %0" : "+v"(o[4][1]) : "a"(WA[19][4]), "v"(HK1[19]));
    asm volatile("v_mfma_f32_16x16x32_bf16 %0, %1, %2, %0" : "+v"(o[0][1]) : "a"(WA[20][0]), "v"(HK1[20]));
    asm volatile("v_mfma_f32_16x16x32_bf16 %0, %1, %2, %0" : "+v"(o[1][1]) : "a"(WA[20][1]), "v"(HK1[20]));
    asm volatile("v_mfma_f32_16x16x32_bf16 %0, %1, %2, %0" : "+v"(o[2][1]) : "a"(WA[20][2]), "v"(HK1[20]));
    asm volatile("v_mfma_f32_16x16x32_bf16 %0, %1, %2, %0" : "+v"(o[3][1]) : "a"(WA[20][3]), "v"(HK1[20]));
    asm volatile("v_mfma_f32_16x16x32_bf16 %0, %1, %2, %0" : "+v"(o[4][1]) : "a"(WA[20][4]), "v"(HK1[20]));
    asm volatile("v_mfma_f32_16x16x32_bf16 %0, %1, %2, %0" : "+v"(o[0][1]) : "a"(WA[21][0]), "v"(HK1[21]));
    asm volatile("v_mfma_f32_16x16x32_bf16 %0, %1, %2, %0" : "+v"(o[1][1]) : "a"(WA[21][1]), "v"(HK1[21]));
    asm volatile("v_mfma_f32_16x16x32_bf16 %0, %1, %2, %0" : "+v"(o[2][1]) : "a"(WA[21][2]), "v"(HK1[21]));
    asm volatile("v_mfma_f32_16x16x32_bf16 %0, %1, %2, %0" : "+v"(o[3][1]) : "a"(WA[21][3]), "v"(HK1[21]));
    asm volatile("v_mfma_f32_16x16x32_bf16 %0, %1, %2, %0" : "+v"(o[4][1]) : "a"(WA[21][4]), "v"(HK1[21]));
    asm volatile("v_mfma_f32_16x16x32_bf16 %0, %1, %2, %0" : "+v"(o[0][0]) : "a"(WA[10][0]), "v"(HK0[10]));
    asm volatile("v_mfma_f32_16x16x32_bf16 %0, %1, %2, %0" : "+v"(o[1][0]) : "a"(WA[10][1]), "v"(HK0[10]));
    asm volatile("v_mfma_f32_16x16x32_bf16 %0, %1, %2, %0" : "+v"(o[2][0]) : "a"(WA[10][2]), "v"(HK0[10]));
    asm volatile("v_mfma_f32_16x16x32_bf16 %0, %1, %2, %0" : "+v"(o[3][0]) : "a"(WA[10][3]), "v"(HK0[10]));
    unsigned q1_0, q1_1, q1_2, q1_3, q1_4, q1_5, q1_6, q1_7, q1_8, q1_9; const unsigned ps1 = 15728640 + (((pd + 2*((kq + 1) & 3))*3 + 0)*4 + w)*2560;
    asm volatile("v_cvt_pk_bf16_f32 %0, %1, %2" : "=v"(q1_0) : "v"(o[0][1].x), "v"(o[0][1].y));
    asm volatile("v_mfma_f32_16x16x32_bf16 %0, %1, %2, %0" : "+v"(o[4][0]) : "a"(WA[10][4]), "v"(HK0[10]));
    asm volatile("v_cvt_pk_bf16_f32 %0, %1, %2" : "=v"(q1_1) : "v"(o[0][1].z), "v"(o[0][1].w));
    asm volatile("v_mfma_f32_16x16x32_bf16 %0, %1, %2, %0" : "+v"(o[0][0]) : "a"(WA[11][0]), "v"(HK0[11]));
    asm volatile("v_cvt_pk_bf16_f32 %0, %1, %2" : "=v"(q1_2) : "v"(o[1][1].x), "v"(o[1][1].y));
    asm volatile("v_mfma_f32_16x16x32_bf16 %0, %1, %2, %0" : "+v"(o[1][0]) : "a"(WA[11][1]), "v"(HK0[11]));
    asm volatile("v_cvt_pk_bf16_f32 %0, %1, %2" : "=v"(q1_3) : "v"(o[1][1].z), "v"(o[1][1].w));
    st128((u4{q1_0, q1_1, q1_2, q1_3}), rS, vl16, ps1, SC1);
    asm volatile("v_mfma_f32_16x16x32_bf16 %0, %1, %2, %0" : "+v"(o[2][0]) : "a"(WA[11][2]), "v"(HK0[11]));
    asm volatile("v_cvt_pk_bf16_f32 %0, %1, %2" : "=v"(q1_4) : "v"(o[2][1].x), "v"(o[2][1].y));
    asm volatile("v_mfma_f32_16x16x32_bf16 %0, %1, %2, %0" : "+v"(o[3][0]) : "a"(WA[11][3]), "v"(HK0[11]));
    asm volatile("v_cvt_pk_bf16_f32 %0, %1, %2" : "=v"(q1_5) : "v"(o[2][1].z), "v"(o[2][1].w));
    asm volatile("v_mfma_f32_16x16x32_bf16 %0, %1, %2, %0" : "+v"(o[4][0]) : "a"(WA[11][4]), "v"(HK0[11]));
    asm volatile("v_cvt_pk_bf16_f32 %0, %1, %2" : "=v"(q1_6) : "v"(o[3][1].x), "v"(o[3][1].y));
    asm volatile("v_mfma_f32_16x16x32_bf16 %0, %1, %2, %0" : "+v"(o[0][0]) : "a"(WA[12][0]), "v"(HK0[12]));
    asm volatile("v_cvt_pk_bf16_f32 %0, %1, %2" : "=v"(q1_7) : "v"(o[3][1].z), "v"(o[3][1].w));
    st128((u4{q1_4, q1_5, q1_6, q1_7}), rS, vl16 + 1024, ps1, SC1);
    asm volatile("s_waitcnt vmcnt(2)" ::: "memory");
    __builtin_amdgcn_raw_buffer_store_b32(tag, rS, (lane == 1 || lane == 2) ? 3964928 + ((pd + 2*((kq + 1 + lane) & 3))*4 + w)*128 + lane*4 : OOB, 0, SC1);
    asm volatile("v_mfma_f32_16x16x32_bf16 %0, %1, %2, %0" : "+v"(o[1][0]) : "a"(WA[12][1]), "v"(HK0[12]));
    asm volatile("v_cvt_pk_bf16_f32 %0, %1, %2" : "=v"(q1_8) : "v"(o[4][1].x), "v"(o[4][1].y));
    asm volatile("v_mfma_f32_16x16x32_bf16 %0, %1, %2, %0" : "+v"(o[2][0]) : "a"(WA[12][2]), "v"(HK0[12]));
    asm volatile("v_cvt_pk_bf16_f32 %0, %1, %2" : "=v"(q1_9) : "v"(o[4][1].z), "v"(o[4][1].w));
    __builtin_amdgcn_raw_buffer_store_b64(u2{q1_8, q1_9}, rS, lane*8 + 2048, ps1, SC1);
    asm volatile("v_mfma_f32_16x16x32_bf16 %0, %1, %2, %0" : "+v"(o[3][0]) : "a"(WA[12][3]), "v"(HK0[12]));
    asm volatile("v_mfma_f32_16x16x32_bf16 %0, %1, %2, %0" : "+v"(o[4][0]) : "a"(WA[12][4]), "v"(HK0[12]));
    asm volatile("v_mfma_f32_16x16x32_bf16 %0, %1, %2, %0" : "+v"(o[0][0]) : "a"(WA[13][0]), "v"(HK0[13]));
    asm volatile("v_mfma_f32_16x16x32_bf16 %0, %1, %2, %0" : "+v"(o[1][0]) : "a"(WA[13][1]), "v"(HK0[13]));
    asm volatile("v_mfma_f32_16x16x32_bf16 %0, %1, %2, %0" : "+v"(o[2][0]) : "a"(WA[13][2]), "v"(HK0[13]));
    asm volatile("v_mfma_f32_16x16x32_bf16 %0, %1, %2, %0" : "+v"(o[3][0]) : "a"(WA[13][3]), "v"(HK0[13]));
    asm volatile("v_mfma_f32_16x16x32_bf16 %0, %1, %2, %0" : "+v"(o[4][0]) : "a"(WA[13][4]), "v"(HK0[13]));
    asm volatile("v_mfma_f32_16x16x32_bf16 %0, %1, %2, %0" : "+v"(o[0][0]) : "a"(WA[14][0]), "v"(HK0[14]));
    asm volatile("v_mfma_f32_16x16x32_bf16 %0, %1, %2, %0" : "+v"(o[1][0]) : "a"(WA[14][1]), "v"(HK0[14]));
    asm volatile("v_mfma_f32_16x16x32_bf16 %0, %1, %2, %0" : "+v"(o[2][0]) : "a"(WA[14][2]), "v"(HK0[14]));
    asm volatile("v_mfma_f32_16x16x32_bf16 %0, %1, %2, %0" : "+v"(o[3][0]) : "a"(WA[14][3]), "v"(HK0[14]));
    asm volatile("v_mfma_f32_16x16x32_bf16 %0, %1, %2, %0" : "+v"(o[4][0]) : "a"(WA[14][4]), "v"(HK0[14]));
    asm volatile("v_mfma_f32_16x16x32_bf16 %0, %1, %2, %0" : "+v"(o[0][0]) : "a"(WA[15][0]), "v"(HK0[15]));
    asm volatile("v_mfma_f32_16x16x32_bf16 %0, %1, %2, %0" : "+v"(o[1][0]) : "a"(WA[15][1]), "v"(HK0[15]));
    asm volatile("v_mfma_f32_16x16x32_bf16 %0, %1, %2, %0" : "+v"(o[2][0]) : "a"(WA[15][2]), "v"(HK0[15]));
    asm volatile("v_mfma_f32_16x16x32_bf16 %0, %1, %2, %0" : "+v"(o[3][0]) : "a"(WA[15][3]), "v"(HK0[15]));
    asm volatile("v_mfma_f32_16x16x32_bf16 %0, %1, %2, %0" : "+v"(o[4][0]) : "a"(WA[15][4]), "v"(HK0[15]));
    asm volatile("v_mfma_f32_16x16x32_bf16 %0, %1, %2, %0" : "+v"(o[0][0]) : "a"(WA[16][0]), "v"(HK0[16]));
    asm volatile("v_mfma_f32_16x16x32_bf16 %0, %1, %2, %0" : "+v"(o[1][0]) : "a"(WA[16][1]), "v"(HK0[16]));
    asm volatile("v_mfma_f32_16x16x32_bf16 %0, %1, %2, %0" : "+v"(o[2][0]) : "a"(WA[16][2]), "v"(HK0[16]));
    asm volatile("v_mfma_f32_16x16x32_bf16 %0, %1, %2, %0" : "+v"(o[3][0]) : "a"(WA[16][3]), "v"(HK0[16]));
    asm volatile("v_mfma_f32_16x16x32_bf16 %0, %1, %2, %0" : "+v"(o[4][0]) : "a"(WA[16][4]), "v"(HK0[16]));
    asm volatile("v_mfma_f32_16x16x32_bf16 %0, %1, %2, %0" : "+v"(o[0][0]) : "a"(WA[17][0]), "v"(HK0[17]));
    asm volatile("v_mfma_f32_16x16x32_bf16 %0, %1, %2, %0" : "+v"(o[1][0]) : "a"(WA[17][1]), "v"(HK0[17]));
    asm volatile("v_mfma_f32_16x16x32_bf16 %0, %1, %2, %0" : "+v"(o[2][0]) : "a"(WA[17][2]), "v"(HK0[17]));
    asm volatile("v_mfma_f32_16x16x32_bf16 %0, %1, %2, %0" : "+v"(o[3][0]) : "a"(WA[17][3]), "v"(HK0[17]));
    asm volatile("v_mfma_f32_16x16x32_bf16 %0, %1, %2, %0" : "+v"(o[4][0]) : "a"(WA[17][4]), "v"(HK0[17]));
    asm volatile("v_mfma_f32_16x16x32_bf16 %0, %1, %2, %0" : "+v"(o[0][0]) : "a"(WA[18][0]), "v"(HK0[18]));
    asm volatile("buffer_load_dword %0, %1, %2, 0 offen sc1" : "=v"(f3v) : "v"(f3), "s"(rS) : "memory");
    asm volatile("v_mfma_f32_16x16x32_bf16 %0, %1, %2, %0" : "+v"(o[1][0]) : "a"(WA[18][1]), "v"(HK0[18]));
    asm volatile("v_mfma_f32_16x16x32_bf16 %0, %1, %2, %0" : "+v"(o[2][0]) : "a"(WA[18][2]), "v"(HK0[18]));
    asm volatile("v_mfma_f32_16x16x32_bf16 %0, %1, %2, %0" : "+v"(o[3][0]) : "a"(WA[18][3]), "v"(HK0[18]));
    asm volatile("v_mfma_f32_16x16x32_bf16 %0, %1, %2, %0" : "+v"(o[4][0]) : "a"(WA[18][4]), "v"(HK0[18]));
    asm volatile("v_mfma_f32_16x16x32_bf16 %0, %1, %2, %0" : "+v"(o[0][0]) : "a"(WA[19][0]), "v"(HK0[19]));
    asm volatile("v_mfma_f32_16x16x32_bf16 %0, %1, %2, %0" : "+v"(o[1][0]) : "a"(WA[19][1]), "v"(HK0[19]));
    asm volatile("v_mfma_f32_16x16x32_bf16 %0, %1, %2, %0" : "+v"(o[2][0]) : "a"(WA[19][2]), "v"(HK0[19]));
    asm volatile("v_mfma_f32_16x16x32_bf16 %0, %1, %2, %0" : "+v"(o[3][0]) : "a"(WA[19][3]), "v"(HK0[19]));
    asm volatile("v_mfma_f32_16x16x32_bf16 %0, %1, %2, %0" : "+v"(o[4][0]) : "a"(WA[19][4]), "v"(HK0[19]));
    asm volatile("v_mfma_f32_16x16x32_bf16 %0, %1, %2, %0" : "+v"(o[0][0]) : "a"(WA[20][0]), "v"(HK0[20]));
    asm volatile("v_mfma_f32_16x16x32_bf16 %0, %1, %2, %0" : "+v"(o[1][0]) : "a"(WA[20][1]), "v"(HK0[20]));
    asm volatile("v_mfma_f32_16x16x32_bf16 %0, %1, %2, %0" : "+v"(o[2][0]) : "a"(WA[20][2]), "v"(HK0[20]));
    asm volatile("v_mfma_f32_16x16x32_bf16 %0, %1, %2, %0" : "+v"(o[3][0]) : "a"(WA[20][3]), "v"(HK0[20]));
    asm volatile("v_mfma_f32_16x16x32_bf16 %0, %1, %2, %0" : "+v"(o[4][0]) : "a"(WA[20][4]), "v"(HK0[20]));
    asm volatile("v_mfma_f32_16x16x32_bf16 %0, %1, %2, %0" : "+v"(o[0][0]) : "a"(WA[21][0]), "v"(HK0[21]));
    asm volatile("v_mfma_f32_16x16x32_bf16 %0, %1, %2, %0" : "+v"(o[1][0]) : "a"(WA[21][1]), "v"(HK0[21]));
    asm volatile("v_mfma_f32_16x16x32_bf16 %0, %1, %2, %0" : "+v"(o[2][0]) : "a"(WA[21][2]), "v"(HK0[21]));
    asm volatile("v_mfma_f32_16x16x32_bf16 %0, %1, %2, %0" : "+v"(o[3][0]) : "a"(WA[21][3]), "v"(HK0[21]));
    asm volatile("v_mfma_f32_16x16x32_bf16 %0, %1, %2, %0" : "+v"(o[4][0]) : "a"(WA[21][4]), "v"(HK0[21]));
    __builtin_amdgcn_sched_barrier(0);
    asm volatile("s_waitcnt vmcnt(1)" ::: "memory");
    __builtin_amdgcn_raw_buffer_store_b32(tag, rS, lane == 0 ? 3964928 + ((pd + 2*((kq + 1 + lane) & 3))*4 + w)*128 + lane*4 : OOB, 0, SC1);
    { u4 ra[3], rb[3]; u2 rc[3]; const unsigned pb = 15728640 + (b*12 + w)*2560;
    asm volatile("s_waitcnt vmcnt(1)" ::: "memory");
    asm volatile("" : "+v"(f3v));
      if (!__all((lane & 3) == 0 || (lane & 3) == 3 || f3v == tag)) spin1(rS, f3, (lane & 3) == 0 || (lane & 3) == 3, tag);
      asm volatile("buffer_load_dwordx4 %0, %1, %2, %3 offen offset:0 sc1" : "=v"(ra[2]) : "v"(vl16), "s"(rS), "s"(pb + 20480) : "memory");
      asm volatile("buffer_load_dwordx4 %0, %1, %2, %3 offen offset:1024 sc1" : "=v"(rb[2]) : "v"(vl16), "s"(rS), "s"(pb + 20480) : "memory");
      asm volatile("buffer_load_dwordx2 %0, %1, %2, %3 offen offset:2048 sc1" : "=v"(rc[2]) : "v"(lane*8), "s"(rS), "s"(pb + 20480) : "memory");
      asm volatile("buffer_load_dwordx4 %0, %1, %2, %3 offen offset:0 sc1" : "=v"(ra[1]) : "v"(vl16), "s"(rS), "s"(pb + 10240) : "memory");
      asm volatile("buffer_load_dwordx4 %0, %1, %2, %3 offen offset:1024 sc1" : "=v"(rb[1]) : "v"(vl16), "s"(rS), "s"(pb + 10240) : "memory");
      asm volatile("buffer_load_dwordx2 %0, %1, %2, %3 offen offset:2048 sc1" : "=v"(rc[1]) : "v"(lane*8), "s"(rS), "s"(pb + 10240) : "memory");
    asm volatile("buffer_load_dword %0, %1, %2, 0 offen sc1" : "=v"(f3v) : "v"(f3), "s"(rS) : "memory");
    asm volatile("s_nop 7");
    asm volatile("" : "+v"(es), "+v"(ex[0]), "+v"(ex[1]), "+v"(ex[2]), "+v"(ex[3]), "+v"(ex[4]), "+v"(o[0][0]), "+v"(o[1][0]), "+v"(o[2][0]), "+v"(o[3][0]), "+v"(o[4][0]));
    { const float mean = rbf(__uint_as_float(es.x)), rs = ln_rstd(__uint_as_float(es.y));
      { const f4 xx = unpk4(ex[0]); o[0][0] += xx + (xx - mean)*rs*unpk4(LDS(u2, 10112 + (80*wn + 4*g)*2)) + unpk4(LDS(u2, 10496 + (80*wn + 4*g)*2)); }
      { const f4 xx = unpk4(ex[1]); o[1][0] += xx + (xx - mean)*rs*unpk4(LDS(u2, 10144 + (80*wn + 4*g)*2)) + unpk4(LDS(u2, 10528 + (80*wn + 4*g)*2)); }
      { const f4 xx = unpk4(ex[2]); o[2][0] += xx + (xx - mean)*rs*unpk4(LDS(u2, 10176 + (80*wn + 4*g)*2)) + unpk4(LDS(u2, 10560 + (80*wn + 4*g)*2)); }
      { const f4 xx = unpk4(ex[3]); o[3][0] += xx + (xx - mean)*rs*unpk4(LDS(u2, 10208 + (80*wn + 4*g)*2)) + unpk4(LDS(u2, 10592 + (80*wn + 4*g)*2)); }
      { const f4 xx = unpk4(ex[4]); o[4][0] += xx + (xx - mean)*rs*unpk4(LDS(u2, 10240 + (80*wn + 4*g)*2)) + unpk4(LDS(u2, 10624 + (80*wn + 4*g)*2)); }
    }
    asm volatile("" : "+v"(o[0][0]), "+v"(o[1][0]), "+v"(o[2][0]), "+v"(o[3][0]), "+v"(o[4][0]));
    asm volatile("s_waitcnt vmcnt(0)" ::: "memory");
    asm volatile("" : "+v"(f3v));
      if (!__all((lane & 3) != 0 || f3v == tag)) spin1(rS, f3, (lane & 3) != 0, tag);
      asm volatile("buffer_load_dwordx4 %0, %1, %2, %3 offen offset:0 sc1" : "=v"(ra[0]) : "v"(vl16), "s"(rS), "s"(pb + 0) : "memory");
      asm volatile("buffer_load_dwordx4 %0, %1, %2, %3 offen offset:1024 sc1" : "=v"(rb[0]) : "v"(vl16), "s"(rS), "s"(pb + 0) : "memory");
      asm volatile("buffer_load_dwordx2 %0, %1, %2, %3 offen offset:2048 sc1" : "=v"(rc[0]) : "v"(lane*8), "s"(rS), "s"(pb + 0) : "memory");
    asm volatile("" : "+v"(ra[1]), "+v"(ra[2]), "+v"(rb[1]), "+v"(rb[2]), "+v"(rc[1]), "+v"(rc[2]));
      o[0][0] += unpk4(u2{ra[2].x, ra[2].y}) + unpk4(u2{ra[1].x, ra[1].y});
      o[1][0] += unpk4(u2{ra[2].z, ra[2].w}) + unpk4(u2{ra[1].z, ra[1].w});
      o[2][0] += unpk4(u2{rb[2].x, rb[2].y}) + unpk4(u2{rb[1].x, rb[1].y});
      o[3][0] += unpk4(u2{rb[2].z, rb[2].w}) + unpk4(u2{rb[1].z, rb[1].w});
      o[4][0] += unpk4(rc[2]) + unpk4(rc[1]);
    asm volatile("" : "+v"(o[0][0]), "+v"(o[1][0]), "+v"(o[2][0]), "+v"(o[3][0]), "+v"(o[4][0]));
    asm volatile("s_waitcnt vmcnt(0)" ::: "memory");
    asm volatile("" : "+v"(ra[0]), "+v"(rb[0]), "+v"(rc[0]));
      const rsrc_t rO = mk(out, 2048*640*4);
      { const f4 a = o[0][0] + unpk4(u2{ra[0].x, ra[0].y}); st128(__builtin_bit_cast(u4, a), rO, 2*xvo + 0, 0, 16); }
      { const f4 a = o[1][0] + unpk4(u2{ra[0].z, ra[0].w}); st128(__builtin_bit_cast(u4, a), rO, 2*xvo + 64, 0, 16); }
      { const f4 a = o[2][0] + unpk4(u2{rb[0].x, rb[0].y}); st128(__builtin_bit_cast(u4, a), rO, 2*xvo + 128, 0, 16); }
      { const f4 a = o[3][0] + unpk4(u2{rb[0].z, rb[0].w}); st128(__builtin_bit_cast(u4, a), rO, 2*xvo + 192, 0, 16); }
      { const f4 a = o[4][0] + unpk4(rc[0]); st128(__builtin_bit_cast(u4, a), rO, 2*xvo + 256, 0, 16); }
    }
    asm volatile(".Lfk_end0:");
    asm volatile("" : "=a"(wtd));
    asm volatile("" : "=a"(sx[0]), "=a"(sx[1]), "=a"(sx[2]), "=a"(q_re), "=a"(q_im), "=a"(q_dt));
    asm volatile("" : "=v"(xv[0]), "=v"(xv[1]), "=v"(xv[2]), "=v"(xv[3]), "=v"(wv[0]), "=v"(wv[1]), "=v"(wv[2]), "=v"(wv[3]), "=v"(wv[4]), "=v"(wv[5]), "=v"(pfd), "=v"(tid2));
  }
  if (mlp) {
    const unsigned tid = tid2, lane = tid & 63, r16 = lane & 15, g = lane >> 4;
    const unsigned xa0 = lds0 + 12288 + (64*wm + r16)*128 + (((4*wm + g) ^ (r16 >> 1)) << 4), xa1 = xa0 ^ 64;
    unsigned wa0[4], wa1[4];
    { const unsigned q = r16 >> 2, h = (q >> 1) | ((g & 1) << 1);
      _Pragma("unroll") for (int m = 0; m < 4; ++m) { const unsigned a = lds0 + 61440 + (8*g + q)*384 + 128*((6*wn + m) >> 2) + 32*(((2*wn + m) & 3) ^ h) + 8*(r16 & 3);
        wa0[m] = a + 12288*wm; wa1[m] = a + 12288 - 12288*wm; } }
    u4 ones = {0x3f803f80u, 0x3f803f80u, 0x3f803f80u, 0x3f803f80u};
    u4 ones3 = ones*(xc & 1);
    asm volatile("" : "+v"(ones), "+v"(ones3));
    const unsigned vl16 = lane*16, vl16h = vl16 + 4096;
    const unsigned kso = 40 + 6*j, ksp = 40 + 6*(j ^ 1), kss = 10*kq;
    unsigned sw, sh;
    const unsigned lw2 = lds0 + wn*5120 - wm*10240, swb = 262144 + cq*10240 + wn*5120;
    u4 X0[4], X1[4], W0[6], W1[6]; f4 acc[6][4], cs[6];
    u2 wl[6], wh[6];
    unsigned fl; u2 st; f4 c4[2];
    asm volatile("s_waitcnt vmcnt(12) lgkmcnt(0)\n\ts_barrier" ::: "memory");
    asm volatile("ds_read_b128 %0, %1 offset:0" : "=v"(X0[0]) : "v"(xa0) : "memory");
    asm volatile("ds_read_b128 %0, %1 offset:2048" : "=v"(X0[1]) : "v"(xa0) : "memory");
    asm volatile("ds_read_b128 %0, %1 offset:4096" : "=v"(X0[2]) : "v"(xa0) : "memory");
    asm volatile("ds_read_b128 %0, %1 offset:6144" : "=v"(X0[3]) : "v"(xa0) : "memory");
    asm volatile("ds_read_b64_tr_b16 %0, %1 offset:0" : "=v"(wl[0]) : "v"(wa0[0]) : "memory");
    asm volatile("ds_read_b64_tr_b16 %0, %1 offset:1536" : "=v"(wh[0]) : "v"(wa0[0]) : "memory");
    W0[0] = cat(wl[0], wh[0]);
    asm volatile("ds_read_b64_tr_b16 %0, %1 offset:0" : "=v"(wl[1]) : "v"(wa0[1]) : "memory");
    asm volatile("ds_read_b64_tr_b16 %0, %1 offset:1536" : "=v"(wh[1]) : "v"(wa0[1]) : "memory");
    W0[1] = cat(wl[1], wh[1]);
    asm volatile("ds_read_b64_tr_b16 %0, %1 offset:0" : "=v"(wl[2]) : "v"(wa0[2]) : "memory");
    asm volatile("ds_read_b64_tr_b16 %0, %1 offset:1536" : "=v"(wh[2]) : "v"(wa0[2]) : "memory");
    W0[2] = cat(wl[2], wh[2]);
    asm volatile("ds_read_b64_tr_b16 %0, %1 offset:0" : "=v"(wl[3]) : "v"(wa0[3]) : "memory");
    asm volatile("ds_read_b64_tr_b16 %0, %1 offset:1536" : "=v"(wh[3]) : "v"(wa0[3]) : "memory");
    W0[3] = cat(wl[3], wh[3]);
    asm volatile("ds_read_b64_tr_b16 %0, %1 offset:128" : "=v"(wl[4]) : "v"(wa0[0]) : "memory");
    asm volatile("ds_read_b64_tr_b16 %0, %1 offset:1664" : "=v"(wh[4]) : "v"(wa0[0]) : "memory");
    W0[4] = cat(wl[4], wh[4]);
    asm volatile("ds_read_b64_tr_b16 %0, %1 offset:128" : "=v"(wl[5]) : "v"(wa0[1]) : "memory");
    asm volatile("ds_read_b64_tr_b16 %0, %1 offset:1664" : "=v"(wh[5]) : "v"(wa0[1]) : "memory");
    W0[5] = cat(wl[5], wh[5]);
    asm volatile("s_waitcnt lgkmcnt(0)" ::: "memory");
    asm volatile("v_mfma_f32_16x16x32_bf16 %0, %1, %2, 0" : "=&v"(acc[0][0]) : "v"(W0[0]), "v"(X0[0]));
    asm volatile("ds_read_b128 %0, %1 offset:0" : "=v"(X1[0]) : "v"(xa1) : "memory");
    asm volatile("s_add_u32 m0, %0, 45056" :: "s"(lw) : "scc", "memory");
    asm volatile("v_mfma_f32_16x16x32_bf16 %0, %1, %2, 0" : "=&v"(acc[0][1]) : "v"(W0[0]), "v"(X0[1]));
    asm volatile("ds_read_b128 %0, %1 offset:2048" : "=v"(X1[1]) : "v"(xa1) : "memory");
    asm volatile("buffer_load_dwordx4 %0, %1, %2 offen lds" :: "v"(xv[0]), "s"(rX), "s"(kx) : "memory");
    asm volatile("s_addk_i32 m0, 0x1000" ::: "scc", "memory");
    asm volatile("v_mfma_f32_16x16x32_bf16 %0, %1, %2, 0" : "=&v"(acc[0][2]) : "v"(W0[0]), "v"(X0[2]));
    asm volatile("ds_read_b128 %0, %1 offset:4096" : "=v"(X1[2]) : "v"(xa1) : "memory");
    asm volatile("buffer_load_dwordx4 %0, %1, %2 offen lds" :: "v"(xv[1]), "s"(rX), "s"(kx) : "memory");
    asm volatile("v_mfma_f32_16x16x32_bf16 %0, %1, %2, 0" : "=&v"(acc[0][3]) : "v"(W0[0]), "v"(X0[3]));
    asm volatile("ds_read_b128 %0, %1 offset:6144" : "=v"(X1[3]) : "v"(xa1) : "memory");
    asm volatile("s_addk_i32 m0, 0x1000" ::: "scc", "memory");
    asm volatile("v_mfma_f32_16x16x32_bf16 %0, %1, %2, 0" : "=&v"(cs[0]) : "v"(ones), "v"(W0[0]));
    asm volatile("ds_read_b64_tr_b16 %0, %1 offset:0" : "=v"(wl[0]) : "v"(wa1[0]) : "memory");
    asm volatile("buffer_load_dwordx4 %0, %1, %2 offen lds" :: "v"(xv[2]), "s"(rX), "s"(kx) : "memory");
    asm volatile("v_mfma_f32_16x16x32_bf16 %0, %1, %2, 0" : "=&v"(acc[1][0]) : "v"(W0[1]), "v"(X0[0]));
    asm volatile("ds_read_b64_tr_b16 %0, %1 offset:1536" : "=v"(wh[0]) : "v"(wa1[0]) : "memory");
    W1[0] = cat(wl[0], wh[0]);
    asm volatile("s_addk_i32 m0, 0x1000" ::: "scc", "memory");
    asm volatile("v_mfma_f32_16x16x32_bf16 %0, %1, %2, 0" : "=&v"(acc[1][1]) : "v"(W0[1]), "v"(X0[1]));
    asm volatile("ds_read_b64_tr_b16 %0, %1 offset:0" : "=v"(wl[1]) : "v"(wa1[1]) : "memory");
    asm volatile("buffer_load_dwordx4 %0, %1, %2 offen lds" :: "v"(xv[3]), "s"(rX), "s"(kx) : "memory");
    asm volatile("s_add_u32 %0, %0, 128\n\ts_cmp_eq_u32 %0, 1280\n\ts_cselect_b32 %0, 0, %0" : "+s"(kx) :: "scc");
    asm volatile("v_mfma_f32_16x16x32_bf16 %0, %1, %2, 0" : "=&v"(acc[1][2]) : "v"(W0[1]), "v"(X0[2]));
    asm volatile("ds_read_b64_tr_b16 %0, %1 offset:1536" : "=v"(wh[1]) : "v"(wa1[1]) : "memory");
    W1[1] = cat(wl[1], wh[1]);
    asm volatile("s_add_u32 m0, %0, 110592" :: "s"(lw) : "scc", "memory");
    asm volatile("v_mfma_f32_16x16x32_bf16 %0, %1, %2, 0" : "=&v"(acc[1][3]) : "v"(W0[1]), "v"(X0[3]));
    asm volatile("ds_read_b64_tr_b16 %0, %1 offset:0" : "=v"(wl[2]) : "v"(wa1[2]) : "memory");
    asm volatile("buffer_load_dwordx4 %0, %1, %2 offen lds" :: "v"(wv[0]), "s"(rW), "s"(kw) : "memory");
    asm volatile("v_mfma_f32_16x16x32_bf16 %0, %1, %2, 0" : "=&v"(cs[1]) : "v"(ones), "v"(W0[1]));
    asm volatile("ds_read_b64_tr_b16 %0, %1 offset:1536" : "=v"(wh[2]) : "v"(wa1[2]) : "memory");
    W1[2] = cat(wl[2], wh[2]);
    asm volatile("s_addk_i32 m0, 0x1000" ::: "scc", "memory");
    asm volatile("v_mfma_f32_16x16x32_bf16 %0, %1, %2, 0" : "=&v"(acc[2][0]) : "v"(W0[2]), "v"(X0[0]));
    asm volatile("ds_read_b64_tr_b16 %0, %1 offset:0" : "=v"(wl[3]) : "v"(wa1[3]) : "memory");
    asm volatile("buffer_load_dwordx4 %0, %1, %2 offen lds" :: "v"(wv[1]), "s"(rW), "s"(kw) : "memory");
    asm volatile("v_mfma_f32_16x16x32_bf16 %0, %1, %2, 0" : "=&v"(acc[2][1]) : "v"(W0[2]), "v"(X0[1]));
    asm volatile("ds_read_b64_tr_b16 %0, %1 offset:1536" : "=v"(wh[3]) : "v"(wa1[3]) : "memory");
    W1[3] = cat(wl[3], wh[3]);
    asm volatile("s_addk_i32 m0, 0x1000" ::: "scc", "memory");
    asm volatile("v_mfma_f32_16x16x32_bf16 %0, %1, %2, 0" : "=&v"(acc[2][2]) : "v"(W0[2]), "v"(X0[2]));
    asm volatile("ds_read_b64_tr_b16 %0, %1 offset:128" : "=v"(wl[4]) : "v"(wa1[0]) : "memory");
    asm volatile("buffer_load_dwordx4 %0, %1, %2 offen lds" :: "v"(wv[2]), "s"(rW), "s"(kw) : "memory");
    asm volatile("v_mfma_f32_16x16x32_bf16 %0, %1, %2, 0" : "=&v"(acc[2][3]) : "v"(W0[2]), "v"(X0[3]));
    asm volatile("ds_read_b64_tr_b16 %0, %1 offset:1664" : "=v"(wh[4]) : "v"(wa1[0]) : "memory");
    W1[4] = cat(wl[4], wh[4]);
    asm volatile("s_addk_i32 m0, 0x1000" ::: "scc", "memory");
    asm volatile("v_mfma_f32_16x16x32_bf16 %0, %1, %2, 0" : "=&v"(cs[2]) : "v"(ones), "v"(W0[2]));
    asm volatile("ds_read_b64_tr_b16 %0, %1 offset:128" : "=v"(wl[5]) : "v"(wa1[1]) : "memory");
    asm volatile("buffer_load_dwordx4 %0, %1, %2 offen lds" :: "v"(wv[3]), "s"(rW), "s"(kw) : "memory");
    asm volatile("v_mfma_f32_16x16x32_bf16 %0, %1, %2, 0" : "=&v"(acc[3][0]) : "v"(W0[3]), "v"(X0[0]));
    asm volatile("ds_read_b64_tr_b16 %0, %1 offset:1664" : "=v"(wh[5]) : "v"(wa1[1]) : "memory");
    W1[5] = cat(wl[5], wh[5]);
    asm volatile("s_addk_i32 m0, 0x1000" ::: "scc", "memory");
    asm volatile("v_mfma_f32_16x16x32_bf16 %0, %1, %2, 0" : "=&v"(acc[3][1]) : "v"(W0[3]), "v"(X0[1]));
    asm volatile("buffer_load_dwordx4 %0, %1, %2 offen lds" :: "v"(wv[4]), "s"(rW), "s"(kw) : "memory");
    asm volatile("v_mfma_f32_16x16x32_bf16 %0, %1, %2, 0" : "=&v"(acc[3][2]) : "v"(W0[3]), "v"(X0[2]));
    asm volatile("s_addk_i32 m0, 0x1000" ::: "scc", "memory");
    asm volatile("v_mfma_f32_16x16x32_bf16 %0, %1, %2, 0" : "=&v"(acc[3][3]) : "v"(W0[3]), "v"(X0[3]));
    asm volatile("buffer_load_dwordx4 %0, %1, %2 offen lds" :: "v"(wv[5]), "s"(rW), "s"(kw) : "memory");
    asm volatile("s_add_u32 %0, %0, 196608\n\ts_cmp_eq_u32 %0, 1966080\n\ts_cselect_b32 %0, 0, %0" : "+s"(kw) :: "scc");
    asm volatile("v_mfma_f32_16x16x32_bf16 %0, %1, %2, 0" : "=&v"(cs[3]) : "v"(ones), "v"(W0[3]));
    asm volatile("s_add_u32 m0, %0, 28672" :: "s"(lw) : "scc", "memory");
    asm volatile("v_mfma_f32_16x16x32_bf16 %0, %1, %2, 0" : "=&v"(acc[4][0]) : "v"(W0[4]), "v"(X0[0]));
    asm volatile("buffer_load_dwordx4 %0, %1, %2 offen lds" :: "v"(xv[0]), "s"(rX), "s"(kx) : "memory");
    asm volatile("v_mfma_f32_16x16x32_bf16 %0, %1, %2, 0" : "=&v"(acc[4][1]) : "v"(W0[4]), "v"(X0[1]));
    asm volatile("s_addk_i32 m0, 0x1000" ::: "scc", "memory");
    asm volatile("v_mfma_f32_16x16x32_bf16 %0, %1, %2, 0" : "=&v"(acc[4][2]) : "v"(W0[4]), "v"(X0[2]));
    asm volatile("buffer_load_dwordx4 %0, %1, %2 offen lds" :: "v"(xv[1]), "s"(rX), "s"(kx) : "memory");
    asm volatile("v_mfma_f32_16x16x32_bf16 %0, %1, %2, 0" : "=&v"(acc[4][3]) : "v"(W0[4]), "v"(X0[3]));
    asm volatile("s_addk_i32 m0, 0x1000" ::: "scc", "memory");
    asm volatile("v_mfma_f32_16x16x32_bf16 %0, %1, %2, 0" : "=&v"(cs[4]) : "v"(ones), "v"(W0[4]));
    asm volatile("buffer_load_dwordx4 %0, %1, %2 offen lds" :: "v"(xv[2]), "s"(rX), "s"(kx) : "memory");
    asm volatile("v_mfma_f32_16x16x32_bf16 %0, %1, %2, 0" : "=&v"(acc[5][0]) : "v"(W0[5]), "v"(X0[0]));
    asm volatile("s_addk_i32 m0, 0x1000" ::: "scc", "memory");
    asm volatile("v_mfma_f32_16x16x32_bf16 %0, %1, %2, 0" : "=&v"(acc[5][1]) : "v"(W0[5]), "v"(X0[1]));
    asm volatile("buffer_load_dwordx4 %0, %1, %2 offen lds" :: "v"(xv[3]), "s"(rX), "s"(kx) : "memory");
    asm volatile("s_add_u32 %0, %0, 128\n\ts_cmp_eq_u32 %0, 1280\n\ts_cselect_b32 %0, 0, %0" : "+s"(kx) :: "scc");
    asm volatile("v_mfma_f32_16x16x32_bf16 %0, %1, %2, 0" : "=&v"(acc[5][2]) : "v"(W0[5]), "v"(X0[2]));
    asm volatile("v_mfma_f32_16x16x32_bf16 %0, %1, %2, 0" : "=&v"(acc[5][3]) : "v"(W0[5]), "v"(X0[3]));
    asm volatile("v_mfma_f32_16x16x32_bf16 %0, %1, %2, 0" : "=&v"(cs[5]) : "v"(ones), "v"(W0[5]));
    __builtin_amdgcn_sched_barrier(0);
    asm volatile("s_waitcnt vmcnt(14)\n\ts_barrier" ::: "memory");
    asm volatile("" : "+v"(pfd));
    asm volatile("" :: "a"(wtd));
    { u2 cre[2], cim[2];
      const unsigned oc = lds0 + 143360 + (r16 >> 3)*2048 + (r16 & 7)*128 + 32*w + 8*g;
      asm volatile("ds_read_b64 %0, %1 offset:0" : "=v"(cre[0]) : "v"(oc) : "memory");
      asm volatile("ds_read_b64 %0, %1 offset:1024" : "=v"(cim[0]) : "v"(oc) : "memory");
      asm volatile("ds_read_b64 %0, %1 offset:4096" : "=v"(cre[1]) : "v"(oc) : "memory");
      asm volatile("ds_read_b64 %0, %1 offset:5120" : "=v"(cim[1]) : "v"(oc) : "memory");
      float a, bb, lr, li; lam_bar(ard(q_re), ard(q_im), ard(q_dt), a, bb, lr, li);
      const float nr = lr - 1.f, dn = 2.f*__builtin_amdgcn_rcpf(a*a + bb*bb), cr = (nr*a + li*bb)*dn, ci = (li*a - nr*bb)*dn;
      const float cr4[4] = {qb<0>(cr), qb<1>(cr), qb<2>(cr), qb<3>(cr)}, ci4[4] = {qb<0>(ci), qb<1>(ci), qb<2>(ci), qb<3>(ci)};
      asm volatile("s_waitcnt lgkmcnt(0)" : "+v"(cre[0]), "+v"(cre[1]), "+v"(cim[0]), "+v"(cim[1]) :: "memory");
      { const f2 r0 = unpk(cre[0].x), r1 = unpk(cre[0].y), i0 = unpk(cim[0].x), i1 = unpk(cim[0].y);
        LDS(u4, 151552 + w*2560 + vl16) = u4{cvtpk(cr4[0]*r0.x - ci4[0]*i0.x, -(cr4[0]*i0.x + ci4[0]*r0.x)), cvtpk(cr4[1]*r0.y - ci4[1]*i0.y, -(cr4[1]*i0.y + ci4[1]*r0.y)),
                   cvtpk(cr4[2]*r1.x - ci4[2]*i1.x, -(cr4[2]*i1.x + ci4[2]*r1.x)), cvtpk(cr4[3]*r1.y - ci4[3]*i1.y, -(cr4[3]*i1.y + ci4[3]*r1.y))}; }
      { const f2 r0 = unpk(cre[1].x), r1 = unpk(cre[1].y), i0 = unpk(cim[1].x), i1 = unpk(cim[1].y);
        LDS(u4, 152576 + w*2560 + vl16) = u4{cvtpk(cr4[0]*r0.x - ci4[0]*i0.x, -(cr4[0]*i0.x + ci4[0]*r0.x)), cvtpk(cr4[1]*r0.y - ci4[1]*i0.y, -(cr4[1]*i0.y + ci4[1]*r0.y)),
                   cvtpk(cr4[2]*r1.x - ci4[2]*i1.x, -(cr4[2]*i1.x + ci4[2]*r1.x)), cvtpk(cr4[3]*r1.y - ci4[3]*i1.y, -(cr4[3]*i1.y + ci4[3]*r1.y))}; }
    }
    { f2 v[12]; f2 s2 = {0.f, 0.f};
      _Pragma("unroll") for (int i = 0; i < 12; ++i) { v[i] = unpk(ard(sx[i >> 2][i & 3])); s2 += v[i]; }
      const float mean = red32(s2.x + s2.y)*(1.f/640.f), m3 = (lane & 16) ? 0.f : mean; f2 q2 = {0.f, 0.f};
      _Pragma("unroll") for (int i = 0; i < 12; ++i) { const f2 d = v[i] - (i < 8 ? mean : m3); q2 += d*d; }
      const float var = red32(q2.x + q2.y)*(1.f/640.f);
      LDS(f2, 153600 + w*2560 + lane*8) = f2{mean, var}; }
    __builtin_amdgcn_sched_barrier(0);
    asm volatile("s_waitcnt vmcnt(4) lgkmcnt(0)\n\ts_barrier" ::: "memory");
    asm volatile("v_mfma_f32_16x16x32_bf16 %0, %1, %2, %0" : "+v"(acc[0][0]) : "v"(W1[0]), "v"(X1[0]));
    asm volatile("ds_read_b128 %0, %1 offset:32768" : "=v"(X0[0]) : "v"(xa0) : "memory");
    asm volatile("s_add_u32 m0, %0, 86016" :: "s"(lw) : "scc", "memory");
    asm volatile("v_mfma_f32_16x16x32_bf16 %0, %1, %2, %0" : "+v"(acc[0][1]) : "v"(W1[0]), "v"(X1[1]));
    asm volatile("ds_read_b128 %0, %1 offset:34816" : "=v"(X0[1]) : "v"(xa0) : "memory");
    asm volatile("buffer_load_dwordx4 %0, %1, %2 offen lds" :: "v"(wv[0]), "s"(rW), "s"(kw) : "memory");
    asm volatile("v_mfma_f32_16x16x32_bf16 %0, %1, %2, %0" : "+v"(acc[0][2]) : "v"(W1[0]), "v"(X1[2]));
    asm volatile("ds_read_b128 %0, %1 offset:36864" : "=v"(X0[2]) : "v"(xa0) : "memory");
    asm volatile("v_mfma_f32_16x16x32_bf16 %0, %1, %2, %0" : "+v"(acc[0][3]) : "v"(W1[0]), "v"(X1[3]));
    asm volatile("ds_read_b128 %0, %1 offset:38912" : "=v"(X0[3]) : "v"(xa0) : "memory");
    asm volatile("s_addk_i32 m0, 0x1000" ::: "scc", "memory");
    asm volatile("v_mfma_f32_16x16x32_bf16 %0, %1, %2, %0" : "+v"(acc[1][0]) : "v"(W1[1]), "v"(X1[0]));
    asm volatile("ds_read_b64_tr_b16 %0, %1 offset:49152" : "=v"(wl[0]) : "v"(wa0[0]) : "memory");
    asm volatile("buffer_load_dwordx4 %0, %1, %2 offen lds" :: "v"(wv[1]), "s"(rW), "s"(kw) : "memory");
    asm volatile("v_mfma_f32_16x16x32_bf16 %0, %1, %2, %0" : "+v"(acc[1][1]) : "v"(W1[1]), "v"(X1[1]));
    asm volatile("ds_read_b64_tr_b16 %0, %1 offset:50688" : "=v"(wh[0]) : "v"(wa0[0]) : "memory");
    W0[0] = cat(wl[0], wh[0]);
    asm volatile("v_mfma_f32_16x16x32_bf16 %0, %1, %2, %0" : "+v"(acc[1][2]) : "v"(W1[1]), "v"(X1[2]));
    asm volatile("ds_read_b64_tr_b16 %0, %1 offset:49152" : "=v"(wl[1]) : "v"(wa0[1]) : "memory");
    asm volatile("s_addk_i32 m0, 0x1000" ::: "scc", "memory");
    asm volatile("v_mfma_f32_16x16x32_bf16 %0, %1, %2, %0" : "+v"(acc[1][3]) : "v"(W1[1]), "v"(X1[3]));
    asm volatile("ds_read_b64_tr_b16 %0, %1 offset:50688" : "=v"(wh[1]) : "v"(wa0[1]) : "memory");
    W0[1] = cat(wl[1], wh[1]);
    asm volatile("buffer_load_dwordx4 %0, %1, %2 offen lds" :: "v"(wv[2]), "s"(rW), "s"(kw) : "memory");
    asm volatile("v_mfma_f32_16x16x32_bf16 %0, %1, %2, %0" : "+v"(acc[2][0]) : "v"(W1[2]), "v"(X1[0]));
    asm volatile("ds_read_b64_tr_b16 %0, %1 offset:49152" : "=v"(wl[2]) : "v"(wa0[2]) : "memory");
    asm volatile("v_mfma_f32_16x16x32_bf16 %0, %1, %2, %0" : "+v"(acc[2][1]) : "v"(W1[2]), "v"(X1[1]));
    asm volatile("ds_read_b64_tr_b16 %0, %1 offset:50688" : "=v"(wh[2]) : "v"(wa0[2]) : "memory");
    W0[2] = cat(wl[2], wh[2]);
    asm volatile("s_addk_i32 m0, 0x1000" ::: "scc", "memory");
    asm volatile("v_mfma_f32_16x16x32_bf16 %0, %1, %2, %0" : "+v"(acc[2][2]) : "v"(W1[2]), "v"(X1[2]));
    asm volatile("ds_read_b64_tr_b16 %0, %1 offset:49152" : "=v"(wl[3]) : "v"(wa0[3]) : "memory");
    asm volatile("buffer_load_dwordx4 %0, %1, %2 offen lds" :: "v"(wv[3]), "s"(rW), "s"(kw) : "memory");
    asm volatile("v_mfma_f32_16x16x32_bf16 %0, %1, %2, %0" : "+v"(acc[2][3]) : "v"(W1[2]), "v"(X1[3]));
    asm volatile("ds_read_b64_tr_b16 %0, %1 offset:50688" : "=v"(wh[3]) : "v"(wa0[3]) : "memory");
    W0[3] = cat(wl[3], wh[3]);
    asm volatile("v_mfma_f32_16x16x32_bf16 %0, %1, %2, %0" : "+v"(acc[3][0]) : "v"(W1[3]), "v"(X1[0]));
    asm volatile("ds_read_b64_tr_b16 %0, %1 offset:49280" : "=v"(wl[4]) : "v"(wa0[0]) : "memory");
    asm volatile("s_addk_i32 m0, 0x1000" ::: "scc", "memory");
    asm volatile("v_mfma_f32_16x16x32_bf16 %0, %1, %2, %0" : "+v"(acc[3][1]) : "v"(W1[3]), "v"(X1[1]));
    asm volatile("ds_read_b64_tr_b16 %0, %1 offset:50816" : "=v"(wh[4]) : "v"(wa0[0]) : "memory");
    W0[4] = cat(wl[4], wh[4]);
    asm volatile("buffer_load_dwordx4 %0, %1, %2 offen lds" :: "v"(wv[4]), "s"(rW), "s"(kw) : "memory");
    asm volatile("v_mfma_f32_16x16x32_bf16 %0, %1, %2, %0" : "+v"(acc[3][2]) : "v"(W1[3]), "v"(X1[2]));
    asm volatile("ds_read_b64_tr_b16 %0, %1 offset:49280" : "=v"(wl[5]) : "v"(wa0[1]) : "memory");
    asm volatile("v_mfma_f32_16x16x32_bf16 %0, %1, %2, %0" : "+v"(acc[3][3]) : "v"(W1[3]), "v"(X1[3]));
    asm volatile("ds_read_b64_tr_b16 %0, %1 offset:50816" : "=v"(wh[5]) : "v"(wa0[1]) : "memory");
    W0[5] = cat(wl[5], wh[5]);
    asm volatile("s_addk_i32 m0, 0x1000" ::: "scc", "memory");
    asm volatile("v_mfma_f32_16x16x32_bf16 %0, %1, %2, %0" : "+v"(acc[4][0]) : "v"(W1[4]), "v"(X1[0]));
    asm volatile("buffer_load_dwordx4 %0, %1, %2 offen lds" :: "v"(wv[5]), "s"(rW), "s"(kw) : "memory");
    asm volatile("s_add_u32 %0, %0, 196608\n\ts_cmp_eq_u32 %0, 1966080\n\ts_cselect_b32 %0, 0, %0" : "+s"(kw) :: "scc");
    asm volatile("v_mfma_f32_16x16x32_bf16 %0, %1, %2, %0" : "+v"(acc[4][1]) : "v"(W1[4]), "v"(X1[1]));
    asm volatile("v_mfma_f32_16x16x32_bf16 %0, %1, %2, %0" : "+v"(acc[4][2]) : "v"(W1[4]), "v"(X1[2]));
    asm volatile("v_mfma_f32_16x16x32_bf16 %0, %1, %2, %0" : "+v"(acc[4][3]) : "v"(W1[4]), "v"(X1[3]));
    asm volatile("s_add_u32 m0, %0, 12288" :: "s"(lw) : "scc", "memory");
    asm volatile("v_mfma_f32_16x16x32_bf16 %0, %1, %2, %0" : "+v"(acc[5][0]) : "v"(W1[5]), "v"(X1[0]));
    asm volatile("buffer_load_dwordx4 %0, %1, %2 offen lds" :: "v"(xv[0]), "s"(rX), "s"(kx) : "memory");
    asm volatile("v_mfma_f32_16x16x32_bf16 %0, %1, %2, %0" : "+v"(acc[5][1]) : "v"(W1[5]), "v"(X1[1]));
    asm volatile("v_mfma_f32_16x16x32_bf16 %0, %1, %2, %0" : "+v"(acc[5][2]) : "v"(W1[5]), "v"(X1[2]));
    asm volatile("s_addk_i32 m0, 0x1000" ::: "scc", "memory");
    asm volatile("v_mfma_f32_16x16x32_bf16 %0, %1, %2, %0" : "+v"(acc[5][3]) : "v"(W1[5]), "v"(X1[3]));
    asm volatile("buffer_load_dwordx4 %0, %1, %2 offen lds" :: "v"(xv[1]), "s"(rX), "s"(kx) : "memory");
    asm volatile("s_waitcnt lgkmcnt(0)" ::: "memory");
    asm volatile("v_mfma_f32_16x16x32_bf16 %0, %1, %2, %0" : "+v"(acc[0][0]) : "v"(W0[0]), "v"(X0[0]));
    asm volatile("ds_read_b128 %0, %1 offset:32768" : "=v"(X1[0]) : "v"(xa1) : "memory");
    asm volatile("v_mfma_f32_16x16x32_bf16 %0, %1, %2, %0" : "+v"(acc[0][1]) : "v"(W0[0]), "v"(X0[1]));
    asm volatile("ds_read_b128 %0, %1 offset:34816" : "=v"(X1[1]) : "v"(xa1) : "memory");
    asm volatile("s_addk_i32 m0, 0x1000" ::: "scc", "memory");
    asm volatile("v_mfma_f32_16x16x32_bf16 %0, %1, %2, %0" : "+v"(acc[0][2]) : "v"(W0[0]), "v"(X0[2]));
    asm volatile("ds_read_b128 %0, %1 offset:36864" : "=v"(X1[2]) : "v"(xa1) : "memory");
    asm volatile("buffer_load_dwordx4 %0, %1, %2 offen lds" :: "v"(xv[2]), "s"(rX), "s"(kx) : "memory");
    asm volatile("v_mfma_f32_16x16x32_bf16 %0, %1, %2, %0" : "+v"(acc[0][3]) : "v"(W0[0]), "v"(X0[3]));
    asm volatile("ds_read_b128 %0, %1 offset:38912" : "=v"(X1[3]) : "v"(xa1) : "memory");
    asm volatile("v_mfma_f32_16x16x32_bf16 %0, %1, %2, %0" : "+v"(cs[0]) : "v"(ones), "v"(W0[0]));
    asm volatile("ds_read_b64_tr_b16 %0, %1 offset:49152" : "=v"(wl[0]) : "v"(wa1[0]) : "memory");
    asm volatile("s_addk_i32 m0, 0x1000" ::: "scc", "memory");
    asm volatile("v_mfma_f32_16x16x32_bf16 %0, %1, %2, %0" : "+v"(acc[1][0]) : "v"(W0[1]), "v"(X0[0]));
    asm volatile("ds_read_b64_tr_b16 %0, %1 offset:50688" : "=v"(wh[0]) : "v"(wa1[0]) : "memory");
    W1[0] = cat(wl[0], wh[0]);
    asm volatile("buffer_load_dwordx4 %0, %1, %2 offen lds" :: "v"(xv[3]), "s"(rX), "s"(kx) : "memory");
    asm volatile("s_add_u32 %0, %0, 128\n\ts_cmp_eq_u32 %0, 1280\n\ts_cselect_b32 %0, 0, %0" : "+s"(kx) :: "scc");
    asm volatile("v_mfma_f32_16x16x32_bf16 %0, %1, %2, %0" : "+v"(acc[1][1]) : "v"(W0[1]), "v"(X0[1]));
    asm volatile("ds_read_b64_tr_b16 %0, %1 offset:49152" : "=v"(wl[1]) : "v"(wa1[1]) : "memory");
    asm volatile("v_mfma_f32_16x16x32_bf16 %0, %1, %2, %0" : "+v"(acc[1][2]) : "v"(W0[1]), "v"(X0[2]));
    asm volatile("ds_read_b64_tr_b16 %0, %1 offset:50688" : "=v"(wh[1]) : "v"(wa1[1]) : "memory");
    W1[1] = cat(wl[1], wh[1]);
    asm volatile("s_add_u32 m0, %0, 61440" :: "s"(lw) : "scc", "memory");
    asm volatile("v_mfma_f32_16x16x32_bf16 %0, %1, %2, %0" : "+v"(acc[1][3]) : "v"(W0[1]), "v"(X0[3]));
    asm volatile("ds_read_b64_tr_b16 %0, %1 offset:49152" : "=v"(wl[2]) : "v"(wa1[2]) : "memory");
    asm volatile("buffer_load_dwordx4 %0, %1, %2 offen lds" :: "v"(wv[0]), "s"(rW), "s"(kw) : "memory");
    asm volatile("v_mfma_f32_16x16x32_bf16 %0, %1, %2, %0" : "+v"(cs[1]) : "v"(ones), "v"(W0[1]));
    asm volatile("ds_read_b64_tr_b16 %0, %1 offset:50688" : "=v"(wh[2]) : "v"(wa1[2]) : "memory");
    W1[2] = cat(wl[2], wh[2]);
    asm volatile("v_mfma_f32_16x16x32_bf16 %0, %1, %2, %0" : "+v"(acc[2][0]) : "v"(W0[2]), "v"(X0[0]));
    asm volatile("ds_read_b64_tr_b16 %0, %1 offset:49152" : "=v"(wl[3]) : "v"(wa1[3]) : "memory");
    asm volatile("v_mfma_f32_16x16x32_bf16 %0, %1, %2, %0" : "+v"(acc[2][1]) : "v"(W0[2]), "v"(X0[1]));
    asm volatile("ds_read_b64_tr_b16 %0, %1 offset:50688" : "=v"(wh[3]) : "v"(wa1[3]) : "memory");
    W1[3] = cat(wl[3], wh[3]);
    asm volatile("s_addk_i32 m0, 0x1000" ::: "scc", "memory");
    asm volatile("v_mfma_f32_16x16x32_bf16 %0, %1, %2, %0" : "+v"(acc[2][2]) : "v"(W0[2]), "v"(X0[2]));
    asm volatile("ds_read_b64_tr_b16 %0, %1 offset:49280" : "=v"(wl[4]) : "v"(wa1[0]) : "memory");
    asm volatile("buffer_load_dwordx4 %0, %1, %2 offen lds" :: "v"(wv[1]), "s"(rW), "s"(kw) : "memory");
    asm volatile("v_mfma_f32_16x16x32_bf16 %0, %1, %2, %0" : "+v"(acc[2][3]) : "v"(W0[2]), "v"(X0[3]));
    asm volatile("ds_read_b64_tr_b16 %0, %1 offset:50816" : "=v"(wh[4]) : "v"(wa1[0]) : "memory");
    W1[4] = cat(wl[4], wh[4]);
    asm volatile("v_mfma_f32_16x16x32_bf16 %0, %1, %2, %0" : "+v"(cs[2]) : "v"(ones), "v"(W0[2]));
    asm volatile("ds_read_b64_tr_b16 %0, %1 offset:49280" : "=v"(wl[5]) : "v"(wa1[1]) : "memory");
    asm volatile("s_addk_i32 m0, 0x1000" ::: "scc", "memory");
    asm volatile("v_mfma_f32_16x16x32_bf16 %0, %1, %2, %0" : "+v"(acc[3][0]) : "v"(W0[3]), "v"(X0[0]));
    asm volatile("ds_read_b64_tr_b16 %0, %1 offset:50816" : "=v"(wh[5]) : "v"(wa1[1]) : "memory");
    W1[5] = cat(wl[5], wh[5]);
    asm volatile("buffer_load_dwordx4 %0, %1, %2 offen lds" :: "v"(wv[2]), "s"(rW), "s"(kw) : "memory");
    asm volatile("v_mfma_f32_16x16x32_bf16 %0, %1, %2, %0" : "+v"(acc[3][1]) : "v"(W0[3]), "v"(X0[1]));
    asm volatile("v_mfma_f32_16x16x32_bf16 %0, %1, %2, %0" : "+v"(acc[3][2]) : "v"(W0[3]), "v"(X0[2]));
    asm volatile("s_addk_i32 m0, 0x1000" ::: "scc", "memory");
    asm volatile("v_mfma_f32_16x16x32_bf16 %0, %1, %2, %0" : "+v"(acc[3][3]) : "v"(W0[3]), "v"(X0[3]));
    asm volatile("buffer_load_dwordx4 %0, %1, %2 offen lds" :: "v"(wv[3]), "s"(rW), "s"(kw) : "memory");
    asm volatile("v_mfma_f32_16x16x32_bf16 %0, %1, %2, %0" : "+v"(cs[3]) : "v"(ones), "v"(W0[3]));
    asm volatile("v_mfma_f32_16x16x32_bf16 %0, %1, %2, %0" : "+v"(acc[4][0]) : "v"(W0[4]), "v"(X0[0]));
    asm volatile("s_addk_i32 m0, 0x1000" ::: "scc", "memory");
    asm volatile("v_mfma_f32_16x16x32_bf16 %0, %1, %2, %0" : "+v"(acc[4][1]) : "v"(W0[4]), "v"(X0[1]));
    asm volatile("buffer_load_dwordx4 %0, %1, %2 offen lds" :: "v"(wv[4]), "s"(rW), "s"(kw) : "memory");
    asm volatile("v_mfma_f32_16x16x32_bf16 %0, %1, %2, %0" : "+v"(acc[4][2]) : "v"(W0[4]), "v"(X0[2]));
    asm volatile("v_mfma_f32_16x16x32_bf16 %0, %1, %2, %0" : "+v"(acc[4][3]) : "v"(W0[4]), "v"(X0[3]));
    asm volatile("s_addk_i32 m0, 0x1000" ::: "scc", "memory");
    asm volatile("v_mfma_f32_16x16x32_bf16 %0, %1, %2, %0" : "+v"(cs[4]) : "v"(ones), "v"(W0[4]));
    asm volatile("buffer_load_dwordx4 %0, %1, %2 offen lds" :: "v"(wv[5]), "s"(rW), "s"(kw) : "memory");
    asm volatile("s_add_u32 %0, %0, 196608\n\ts_cmp_eq_u32 %0, 1966080\n\ts_cselect_b32 %0, 0, %0" : "+s"(kw) :: "scc");
    asm volatile("v_mfma_f32_16x16x32_bf16 %0, %1, %2, %0" : "+v"(acc[5][0]) : "v"(W0[5]), "v"(X0[0]));
    asm volatile("v_mfma_f32_16x16x32_bf16 %0, %1, %2, %0" : "+v"(acc[5][1]) : "v"(W0[5]), "v"(X0[1]));
    asm volatile("v_mfma_f32_16x16x32_bf16 %0, %1, %2, %0" : "+v"(acc[5][2]) : "v"(W0[5]), "v"(X0[2]));
    asm volatile("v_mfma_f32_16x16x32_bf16 %0, %1, %2, %0" : "+v"(acc[5][3]) : "v"(W0[5]), "v"(X0[3]));
    asm volatile("v_mfma_f32_16x16x32_bf16 %0, %1, %2, %0" : "+v"(cs[5]) : "v"(ones), "v"(W0[5]));
    asm volatile("s_waitcnt vmcnt(10) lgkmcnt(0)\n\ts_barrier" ::: "memory");
    asm volatile("v_mfma_f32_16x16x32_bf16 %0, %1, %2, %0" : "+v"(acc[0][0]) : "v"(W1[0]), "v"(X1[0]));
    asm volatile("ds_read_b128 %0, %1 offset:16384" : "=v"(X0[0]) : "v"(xa0) : "memory");
    asm volatile("s_add_u32 m0, %0, 45056" :: "s"(lw) : "scc", "memory");
    asm volatile("v_mfma_f32_16x16x32_bf16 %0, %1, %2, %0" : "+v"(acc[0][1]) : "v"(W1[0]), "v"(X1[1]));
    asm volatile("ds_read_b128 %0, %1 offset:18432" : "=v"(X0[1]) : "v"(xa0) : "memory");
    asm volatile("buffer_load_dwordx4 %0, %1, %2 offen lds" :: "v"(xv[0]), "s"(rX), "s"(kx) : "memory");
    asm volatile("v_mfma_f32_16x16x32_bf16 %0, %1, %2, %0" : "+v"(acc[0][2]) : "v"(W1[0]), "v"(X1[2]));
    asm volatile("ds_read_b128 %0, %1 offset:20480" : "=v"(X0[2]) : "v"(xa0) : "memory");
    asm volatile("v_mfma_f32_16x16x32_bf16 %0, %1, %2, %0" : "+v"(acc[0][3]) : "v"(W1[0]), "v"(X1[3]));
    asm volatile("ds_read_b128 %0, %1 offset:22528" : "=v"(X0[3]) : "v"(xa0) : "memory");
    asm volatile("v_mfma_f32_16x16x32_bf16 %0, %1, %2, %0" : "+v"(acc[1][0]) : "v"(W1[1]), "v"(X1[0]));
    asm volatile("ds_read_b64_tr_b16 %0, %1 offset:24576" : "=v"(wl[0]) : "v"(wa0[0]) : "memory");
    asm volatile("v_mfma_f32_16x16x32_bf16 %0, %1, %2, %0" : "+v"(acc[1][1]) : "v"(W1[1]), "v"(X1[1]));
    asm volatile("ds_read_b64_tr_b16 %0, %1 offset:26112" : "=v"(wh[0]) : "v"(wa0[0]) : "memory");
    W0[0] = cat(wl[0], wh[0]);
    asm volatile("s_addk_i32 m0, 0x1000" ::: "scc", "memory");
    asm volatile("v_mfma_f32_16x16x32_bf16 %0, %1, %2, %0" : "+v"(acc[1][2]) : "v"(W1[1]), "v"(X1[2]));
    asm volatile("ds_read_b64_tr_b16 %0, %1 offset:24576" : "=v"(wl[1]) : "v"(wa0[1]) : "memory");
    asm volatile("buffer_load_dwordx4 %0, %1, %2 offen lds" :: "v"(xv[1]), "s"(rX), "s"(kx) : "memory");
    asm volatile("v_mfma_f32_16x16x32_bf16 %0, %1, %2, %0" : "+v"(acc[1][3]) : "v"(W1[1]), "v"(X1[3]));
    asm volatile("ds_read_b64_tr_b16 %0, %1 offset:26112" : "=v"(wh[1]) : "v"(wa0[1]) : "memory");
    W0[1] = cat(wl[1], wh[1]);
    asm volatile("v_mfma_f32_16x16x32_bf16 %0, %1, %2, %0" : "+v"(acc[2][0]) : "v"(W1[2]), "v"(X1[0]));
    asm volatile("ds_read_b64_tr_b16 %0, %1 offset:24576" : "=v"(wl[2]) : "v"(wa0[2]) : "memory");
    asm volatile("v_mfma_f32_16x16x32_bf16 %0, %1, %2, %0" : "+v"(acc[2][1]) : "v"(W1[2]), "v"(X1[1]));
    asm volatile("ds_read_b64_tr_b16 %0, %1 offset:26112" : "=v"(wh[2]) : "v"(wa0[2]) : "memory");
    W0[2] = cat(wl[2], wh[2]);
    asm volatile("v_mfma_f32_16x16x32_bf16 %0, %1, %2, %0" : "+v"(acc[2][2]) : "v"(W1[2]), "v"(X1[2]));
    asm volatile("ds_read_b64_tr_b16 %0, %1 offset:24576" : "=v"(wl[3]) : "v"(wa0[3]) : "memory");
    asm volatile("s_addk_i32 m0, 0x1000" ::: "scc", "memory");
    asm volatile("v_mfma_f32_16x16x32_bf16 %0, %1, %2, %0" : "+v"(acc[2][3]) : "v"(W1[2]), "v"(X1[3]));
    asm volatile("ds_read_b64_tr_b16 %0, %1 offset:26112" : "=v"(wh[3]) : "v"(wa0[3]) : "memory");
    W0[3] = cat(wl[3], wh[3]);
    asm volatile("buffer_load_dwordx4 %0, %1, %2 offen lds" :: "v"(xv[2]), "s"(rX), "s"(kx) : "memory");
    asm volatile("v_mfma_f32_16x16x32_bf16 %0, %1, %2, %0" : "+v"(acc[3][0]) : "v"(W1[3]), "v"(X1[0]));
    asm volatile("ds_read_b64_tr_b16 %0, %1 offset:24704" : "=v"(wl[4]) : "v"(wa0[0]) : "memory");
    asm volatile("v_mfma_f32_16x16x32_bf16 %0, %1, %2, %0" : "+v"(acc[3][1]) : "v"(W1[3]), "v"(X1[1]));
    asm volatile("ds_read_b64_tr_b16 %0, %1 offset:26240" : "=v"(wh[4]) : "v"(wa0[0]) : "memory");
    W0[4] = cat(wl[4], wh[4]);
    asm volatile("v_mfma_f32_16x16x32_bf16 %0, %1, %2, %0" : "+v"(acc[3][2]) : "v"(W1[3]), "v"(X1[2]));
    asm volatile("ds_read_b64_tr_b16 %0, %1 offset:24704" : "=v"(wl[5]) : "v"(wa0[1]) : "memory");
    asm volatile("v_mfma_f32_16x16x32_bf16 %0, %1, %2, %0" : "+v"(acc[3][3]) : "v"(W1[3]), "v"(X1[3]));
    asm volatile("ds_read_b64_tr_b16 %0, %1 offset:26240" : "=v"(wh[5]) : "v"(wa0[1]) : "memory");
    W0[5] = cat(wl[5], wh[5]);
    asm volatile("s_addk_i32 m0, 0x1000" ::: "scc", "memory");
    asm volatile("v_mfma_f32_16x16x32_bf16 %0, %1, %2, %0" : "+v"(acc[4][0]) : "v"(W1[4]), "v"(X1[0]));
    asm volatile("buffer_load_dwordx4 %0, %1, %2 offen lds" :: "v"(xv[3]), "s"(rX), "s"(kx) : "memory");
    asm volatile("s_add_u32 %0, %0, 128\n\ts_cmp_eq_u32 %0, 1280\n\ts_cselect_b32 %0, 0, %0" : "+s"(kx) :: "scc");
    asm volatile("v_mfma_f32_16x16x32_bf16 %0, %1, %2, %0" : "+v"(acc[4][1]) : "v"(W1[4]), "v"(X1[1]));
    asm volatile("v_mfma_f32_16x16x32_bf16 %0, %1, %2, %0" : "+v"(acc[4][2]) : "v"(W1[4]), "v"(X1[2]));
    asm volatile("v_mfma_f32_16x16x32_bf16 %0, %1, %2, %0" : "+v"(acc[4][3]) : "v"(W1[4]), "v"(X1[3]));
    asm volatile("v_mfma_f32_16x16x32_bf16 %0, %1, %2, %0" : "+v"(acc[5][0]) : "v"(W1[5]), "v"(X1[0]));
    asm volatile("s_add_u32 m0, %0, 110592" :: "s"(lw) : "scc", "memory");
    asm volatile("v_mfma_f32_16x16x32_bf16 %0, %1, %2, %0" : "+v"(acc[5][1]) : "v"(W1[5]), "v"(X1[1]));
    asm volatile("buffer_load_dwordx4 %0, %1, %2 offen lds" :: "v"(wv[0]), "s"(rW), "s"(kw) : "memory");
    asm volatile("v_mfma_f32_16x16x32_bf16 %0, %1, %2, %0" : "+v"(acc[5][2]) : "v"(W1[5]), "v"(X1[2]));
    asm volatile("v_mfma_f32_16x16x32_bf16 %0, %1, %2, %0" : "+v"(acc[5][3]) : "v"(W1[5]), "v"(X1[3]));
    asm volatile("s_waitcnt lgkmcnt(0)" ::: "memory");
    asm volatile("v_mfma_f32_16x16x32_bf16 %0, %1, %2, %0" : "+v"(acc[0][0]) : "v"(W0[0]), "v"(X0[0]));
    asm volatile("ds_read_b128 %0, %1 offset:16384" : "=v"(X1[0]) : "v"(xa1) : "memory");
    asm volatile("v_mfma_f32_16x16x32_bf16 %0, %1, %2, %0" : "+v"(acc[0][1]) : "v"(W0[0]), "v"(X0[1]));
    asm volatile("ds_read_b128 %0, %1 offset:18432" : "=v"(X1[1]) : "v"(xa1) : "memory");
    asm volatile("s_addk_i32 m0, 0x1000" ::: "scc", "memory");
    asm volatile("v_mfma_f32_16x16x32_bf16 %0, %1, %2, %0" : "+v"(acc[0][2]) : "v"(W0[0]), "v"(X0[2]));
    asm volatile("ds_read_b128 %0, %1 offset:20480" : "=v"(X1[2]) : "v"(xa1) : "memory");
    asm volatile("buffer_load_dwordx4 %0, %1, %2 offen lds" :: "v"(wv[1]), "s"(rW), "s"(kw) : "memory");
    asm volatile("v_mfma_f32_16x16x32_bf16 %0, %1, %2, %0" : "+v"(acc[0][3]) : "v"(W0[0]), "v"(X0[3]));
    asm volatile("ds_read_b128 %0, %1 offset:22528" : "=v"(X1[3]) : "v"(xa1) : "memory");
    asm volatile("v_mfma_f32_16x16x32_bf16 %0, %1, %2, %0" : "+v"(cs[0]) : "v"(ones3), "v"(W0[0]));
    asm volatile("ds_read_b64_tr_b16 %0, %1 offset:24576" : "=v"(wl[0]) : "v"(wa1[0]) : "memory");
    asm volatile("v_mfma_f32_16x16x32_bf16 %0, %1, %2, %0" : "+v"(acc[1][0]) : "v"(W0[1]), "v"(X0[0]));
    asm volatile("ds_read_b64_tr_b16 %0, %1 offset:26112" : "=v"(wh[0]) : "v"(wa1[0]) : "memory");
    W1[0] = cat(wl[0], wh[0]);
    asm volatile("v_mfma_f32_16x16x32_bf16 %0, %1, %2, %0" : "+v"(acc[1][1]) : "v"(W0[1]), "v"(X0[1]));
    asm volatile("ds_read_b64_tr_b16 %0, %1 offset:24576" : "=v"(wl[1]) : "v"(wa1[1]) : "memory");
    asm volatile("s_addk_i32 m0, 0x1000" ::: "scc", "memory");
    asm volatile("v_mfma_f32_16x16x32_bf16 %0, %1, %2, %0" : "+v"(acc[1][2]) : "v"(W0[1]), "v"(X0[2]));
    asm volatile("ds_read_b64_tr_b16 %0, %1 offset:26112" : "=v"(wh[1]) : "v"(wa1[1]) : "memory");
    W1[1] = cat(wl[1], wh[1]);
    asm volatile("buffer_load_dwordx4 %0, %1, %2 offen lds" :: "v"(wv[2]), "s"(rW), "s"(kw) : "memory");
    asm volatile("v_mfma_f32_16x16x32_bf16 %0, %1, %2, %0" : "+v"(acc[1][3]) : "v"(W0[1]), "v"(X0[3]));
    asm volatile("ds_read_b64_tr_b16 %0, %1 offset:24576" : "=v"(wl[2]) : "v"(wa1[2]) : "memory");
    asm volatile("v_mfma_f32_16x16x32_bf16 %0, %1, %2, %0" : "+v"(cs[1]) : "v"(ones3), "v"(W0[1]));
    asm volatile("ds_read_b64_tr_b16 %0, %1 offset:26112" : "=v"(wh[2]) : "v"(wa1[2]) : "memory");
    W1[2] = cat(wl[2], wh[2]);
    asm volatile("v_mfma_f32_16x16x32_bf16 %0, %1, %2, %0" : "+v"(acc[2][0]) : "v"(W0[2]), "v"(X0[0]));
    asm volatile("ds_read_b64_tr_b16 %0, %1 offset:24576" : "=v"(wl[3]) : "v"(wa1[3]) : "memory");
    asm volatile("v_mfma_f32_16x16x32_bf16 %0, %1, %2, %0" : "+v"(acc[2][1]) : "v"(W0[2]), "v"(X0[1]));
    asm volatile("ds_read_b64_tr_b16 %0, %1 offset:26112" : "=v"(wh[3]) : "v"(wa1[3]) : "memory");
    W1[3] = cat(wl[3], wh[3]);
    asm volatile("s_addk_i32 m0, 0x1000" ::: "scc", "memory");
    asm volatile("v_mfma_f32_16x16x32_bf16 %0, %1, %2, %0" : "+v"(acc[2][2]) : "v"(W0[2]), "v"(X0[2]));
    asm volatile("ds_read_b64_tr_b16 %0, %1 offset:24704" : "=v"(wl[4]) : "v"(wa1[0]) : "memory");
    asm volatile("buffer_load_dwordx4 %0, %1, %2 offen lds" :: "v"(wv[3]), "s"(rW), "s"(kw) : "memory");
    asm volatile("v_mfma_f32_16x16x32_bf16 %0, %1, %2, %0" : "+v"(acc[2][3]) : "v"(W0[2]), "v"(X0[3]));
    asm volatile("ds_read_b64_tr_b16 %0, %1 offset:26240" : "=v"(wh[4]) : "v"(wa1[0]) : "memory");
    W1[4] = cat(wl[4], wh[4]);
    asm volatile("v_mfma_f32_16x16x32_bf16 %0, %1, %2, %0" : "+v"(cs[2]) : "v"(ones3), "v"(W0[2]));
    asm volatile("ds_read_b64_tr_b16 %0, %1 offset:24704" : "=v"(wl[5]) : "v"(wa1[1]) : "memory");
    asm volatile("v_mfma_f32_16x16x32_bf16 %0, %1, %2, %0" : "+v"(acc[3][0]) : "v"(W0[3]), "v"(X0[0]));
    asm volatile("ds_read_b64_tr_b16 %0, %1 offset:26240" : "=v"(wh[5]) : "v"(wa1[1]) : "memory");
    W1[5] = cat(wl[5], wh[5]);
    asm volatile("v_mfma_f32_16x16x32_bf16 %0, %1, %2, %0" : "+v"(acc[3][1]) : "v"(W0[3]), "v"(X0[1]));
    asm volatile("s_addk_i32 m0, 0x1000" ::: "scc", "memory");
    asm volatile("v_mfma_f32_16x16x32_bf16 %0, %1, %2, %0" : "+v"(acc[3][2]) : "v"(W0[3]), "v"(X0[2]));
    asm volatile("buffer_load_dwordx4 %0, %1, %2 offen lds" :: "v"(wv[4]), "s"(rW), "s"(kw) : "memory");
    asm volatile("v_mfma_f32_16x16x32_bf16 %0, %1, %2, %0" : "+v"(acc[3][3]) : "v"(W0[3]), "v"(X0[3]));
    asm volatile("v_mfma_f32_16x16x32_bf16 %0, %1, %2, %0" : "+v"(cs[3]) : "v"(ones3), "v"(W0[3]));
    asm volatile("v_mfma_f32_16x16x32_bf16 %0, %1, %2, %0" : "+v"(acc[4][0]) : "v"(W0[4]), "v"(X0[0]));
    asm volatile("v_mfma_f32_16x16x32_bf16 %0, %1, %2, %0" : "+v"(acc[4][1]) : "v"(W0[4]), "v"(X0[1]));
    asm volatile("s_addk_i32 m0, 0x1000" ::: "scc", "memory");
    asm volatile("v_mfma_f32_16x16x32_bf16 %0, %1, %2, %0" : "+v"(acc[4][2]) : "v"(W0[4]), "v"(X0[2]));
    asm volatile("buffer_load_dwordx4 %0, %1, %2 offen lds" :: "v"(wv[5]), "s"(rW), "s"(kw) : "memory");
    asm volatile("s_add_u32 %0, %0, 196608\n\ts_cmp_eq_u32 %0, 1966080\n\ts_cselect_b32 %0, 0, %0" : "+s"(kw) :: "scc");
    asm volatile("v_mfma_f32_16x16x32_bf16 %0, %1, %2, %0" : "+v"(acc[4][3]) : "v"(W0[4]), "v"(X0[3]));
    asm volatile("v_mfma_f32_16x16x32_bf16 %0, %1, %2, %0" : "+v"(cs[4]) : "v"(ones3), "v"(W0[4]));
    asm volatile("v_mfma_f32_16x16x32_bf16 %0, %1, %2, %0" : "+v"(acc[5][0]) : "v"(W0[5]), "v"(X0[0]));
    asm volatile("v_mfma_f32_16x16x32_bf16 %0, %1, %2, %0" : "+v"(acc[5][1]) : "v"(W0[5]), "v"(X0[1]));
    asm volatile("v_mfma_f32_16x16x32_bf16 %0, %1, %2, %0" : "+v"(acc[5][2]) : "v"(W0[5]), "v"(X0[2]));
    asm volatile("v_mfma_f32_16x16x32_bf16 %0, %1, %2, %0" : "+v"(acc[5][3]) : "v"(W0[5]), "v"(X0[3]));
    asm volatile("v_mfma_f32_16x16x32_bf16 %0, %1, %2, %0" : "+v"(cs[5]) : "v"(ones3), "v"(W0[5]));
    asm volatile("s_waitcnt vmcnt(10) lgkmcnt(0)\n\ts_barrier" ::: "memory");
    asm volatile("v_mfma_f32_16x16x32_bf16 %0, %1, %2, %0" : "+v"(acc[0][0]) : "v"(W1[0]), "v"(X1[0]));
    asm volatile("ds_read_b128 %0, %1 offset:0" : "=v"(X0[0]) : "v"(xa0) : "memory");
    asm volatile("s_add_u32 m0, %0, 28672" :: "s"(lw) : "scc", "memory");
    asm volatile("v_mfma_f32_16x16x32_bf16 %0, %1, %2, %0" : "+v"(acc[0][1]) : "v"(W1[0]), "v"(X1[1]));
    asm volatile("ds_read_b128 %0, %1 offset:2048" : "=v"(X0[1]) : "v"(xa0) : "memory");
    asm volatile("buffer_load_dwordx4 %0, %1, %2 offen lds" :: "v"(xv[0]), "s"(rX), "s"(kx) : "memory");
    asm volatile("v_mfma_f32_16x16x32_bf16 %0, %1, %2, %0" : "+v"(acc[0][2]) : "v"(W1[0]), "v"(X1[2]));
    asm volatile("ds_read_b128 %0, %1 offset:4096" : "=v"(X0[2]) : "v"(xa0) : "memory");
    asm volatile("v_mfma_f32_16x16x32_bf16 %0, %1, %2, %0" : "+v"(acc[0][3]) : "v"(W1[0]), "v"(X1[3]));
    asm volatile("ds_read_b128 %0, %1 offset:6144" : "=v"(X0[3]) : "v"(xa0) : "memory");
    asm volatile("v_mfma_f32_16x16x32_bf16 %0, %1, %2, %0" : "+v"(acc[1][0]) : "v"(W1[1]), "v"(X1[0]));
    asm volatile("ds_read_b64_tr_b16 %0, %1 offset:0" : "=v"(wl[0]) : "v"(wa0[0]) : "memory");
    asm volatile("s_addk_i32 m0, 0x1000" ::: "scc", "memory");
    asm volatile("" : "+v"(cs[0]), "+v"(cs[1]), "+v"(cs[2]), "+v"(cs[3]), "+v"(cs[4]), "+v"(cs[5]));
    const unsigned cso = (idx & 6) == 0 ? 4096000 + (8 + j)*6144 + (96*wn + lane)*32 + (4*((idx ^ (idx >> 3)) & 1) + 2*(xc & 1) + wm)*4 : OOB;
    asm volatile("buffer_store_dword %0, %1, %2, 0 offen offset:0 sc1" :: "v"(g == 0 ? cs[0].x : g == 1 ? cs[1].x : g == 2 ? cs[2].x : cs[3].x), "v"(cso), "s"(rS) : "memory");
    asm volatile("v_mfma_f32_16x16x32_bf16 %0, %1, %2, %0" : "+v"(acc[1][1]) : "v"(W1[1]), "v"(X1[1]));
    asm volatile("ds_read_b64_tr_b16 %0, %1 offset:1536" : "=v"(wh[0]) : "v"(wa0[0]) : "memory");
    W0[0] = cat(wl[0], wh[0]);
    asm volatile("buffer_load_dwordx4 %0, %1, %2 offen lds" :: "v"(xv[1]), "s"(rX), "s"(kx) : "memory");
    asm volatile("v_mfma_f32_16x16x32_bf16 %0, %1, %2, %0" : "+v"(acc[1][2]) : "v"(W1[1]), "v"(X1[2]));
    asm volatile("ds_read_b64_tr_b16 %0, %1 offset:0" : "=v"(wl[1]) : "v"(wa0[1]) : "memory");
    asm volatile("v_mfma_f32_16x16x32_bf16 %0, %1, %2, %0" : "+v"(acc[1][3]) : "v"(W1[1]), "v"(X1[3]));
    asm volatile("ds_read_b64_tr_b16 %0, %1 offset:1536" : "=v"(wh[1]) : "v"(wa0[1]) : "memory");
    W0[1] = cat(wl[1], wh[1]);
    asm volatile("v_mfma_f32_16x16x32_bf16 %0, %1, %2, %0" : "+v"(acc[2][0]) : "v"(W1[2]), "v"(X1[0]));
    asm volatile("ds_read_b64_tr_b16 %0, %1 offset:0" : "=v"(wl[2]) : "v"(wa0[2]) : "memory");
    asm volatile("buffer_store_dword %0, %1, %2, 0 offen offset:2048 sc1" :: "v"(g == 0 ? cs[4].x : cs[5].x), "v"(g < 2 ? cso : OOB), "s"(rS) : "memory");
    asm volatile("v_mfma_f32_16x16x32_bf16 %0, %1, %2, %0" : "+v"(acc[2][1]) : "v"(W1[2]), "v"(X1[1]));
    asm volatile("ds_read_b64_tr_b16 %0, %1 offset:1536" : "=v"(wh[2]) : "v"(wa0[2]) : "memory");
    W0[2] = cat(wl[2], wh[2]);
    asm volatile("s_addk_i32 m0, 0x1000" ::: "scc", "memory");
    asm volatile("v_mfma_f32_16x16x32_bf16 %0, %1, %2, %0" : "+v"(acc[2][2]) : "v"(W1[2]), "v"(X1[2]));
    asm volatile("ds_read_b64_tr_b16 %0, %1 offset:0" : "=v"(wl[3]) : "v"(wa0[3]) : "memory");
    asm volatile("buffer_load_dwordx4 %0, %1, %2 offen lds" :: "v"(xv[2]), "s"(rX), "s"(kx) : "memory");
    asm volatile("v_mfma_f32_16x16x32_bf16 %0, %1, %2, %0" : "+v"(acc[2][3]) : "v"(W1[2]), "v"(X1[3]));
    asm volatile("ds_read_b64_tr_b16 %0, %1 offset:1536" : "=v"(wh[3]) : "v"(wa0[3]) : "memory");
    W0[3] = cat(wl[3], wh[3]);
    asm volatile("v_mfma_f32_16x16x32_bf16 %0, %1, %2, %0" : "+v"(acc[3][0]) : "v"(W1[3]), "v"(X1[0]));
    asm volatile("ds_read_b64_tr_b16 %0, %1 offset:128" : "=v"(wl[4]) : "v"(wa0[0]) : "memory");
    asm volatile("v_mfma_f32_16x16x32_bf16 %0, %1, %2, %0" : "+v"(acc[3][1]) : "v"(W1[3]), "v"(X1[1]));
    asm volatile("ds_read_b64_tr_b16 %0, %1 offset:1664" : "=v"(wh[4]) : "v"(wa0[0]) : "memory");
    W0[4] = cat(wl[4], wh[4]);
    asm volatile("s_addk_i32 m0, 0x1000" ::: "scc", "memory");
    asm volatile("v_mfma_f32_16x16x32_bf16 %0, %1, %2, %0" : "+v"(acc[3][2]) : "v"(W1[3]), "v"(X1[2]));
    asm volatile("ds_read_b64_tr_b16 %0, %1 offset:128" : "=v"(wl[5]) : "v"(wa0[1]) : "memory");
    asm volatile("buffer_load_dwordx4 %0, %1, %2 offen lds" :: "v"(xv[3]), "s"(rX), "s"(kx) : "memory");
    asm volatile("s_add_u32 %0, %0, 128\n\ts_cmp_eq_u32 %0, 1280\n\ts_cselect_b32 %0, 0, %0" : "+s"(kx) :: "scc");
    asm volatile("v_mfma_f32_16x16x32_bf16 %0, %1, %2, %0" : "+v"(acc[3][3]) : "v"(W1[3]), "v"(X1[3]));
    asm volatile("ds_read_b64_tr_b16 %0, %1 offset:1664" : "=v"(wh[5]) : "v"(wa0[1]) : "memory");
    W0[5] = cat(wl[5], wh[5]);
    u2 pa0, pa1, pb0, pb1, sst; u4 so2, so3; const unsigned po = lds0 + 135168 + (4*g + (r16 >> 2))*128 + 32*w + 8*(r16 & 3), pk = lds0 + 151552 + w*2560 + vl16;
    asm volatile("v_mfma_f32_16x16x32_bf16 %0, %1, %2, %0" : "+v"(acc[4][0]) : "v"(W1[4]), "v"(X1[0]));
    asm volatile("ds_read_b64_tr_b16 %0, %1 offset:0" : "=v"(pa0) : "v"(po) : "memory");
    asm volatile("v_mfma_f32_16x16x32_bf16 %0, %1, %2, %0" : "+v"(acc[4][1]) : "v"(W1[4]), "v"(X1[1]));
    asm volatile("ds_read_b64_tr_b16 %0, %1 offset:2048" : "=v"(pa1) : "v"(po) : "memory");
    asm volatile("v_mfma_f32_16x16x32_bf16 %0, %1, %2, %0" : "+v"(acc[4][2]) : "v"(W1[4]), "v"(X1[2]));
    asm volatile("s_add_u32 m0, %0, 86016" :: "s"(lw) : "scc", "memory");
    asm volatile("ds_read_b64_tr_b16 %0, %1 offset:4096" : "=v"(pb0) : "v"(po) : "memory");
    asm volatile("v_mfma_f32_16x16x32_bf16 %0, %1, %2, %0" : "+v"(acc[4][3]) : "v"(W1[4]), "v"(X1[3]));
    asm volatile("buffer_load_dwordx4 %0, %1, %2 offen lds" :: "v"(wv[0]), "s"(rW), "s"(kw) : "memory");
    asm volatile("ds_read_b64_tr_b16 %0, %1 offset:6144" : "=v"(pb1) : "v"(po) : "memory");
    asm volatile("v_mfma_f32_16x16x32_bf16 %0, %1, %2, %0" : "+v"(acc[5][0]) : "v"(W1[5]), "v"(X1[0]));
    asm volatile("ds_read_b128 %0, %1" : "=a"(so2) : "v"(pk) : "memory");
    asm volatile("v_mfma_f32_16x16x32_bf16 %0, %1, %2, %0" : "+v"(acc[5][1]) : "v"(W1[5]), "v"(X1[1]));
    asm volatile("ds_read_b128 %0, %1 offset:1024" : "=a"(so3) : "v"(pk) : "memory");
    asm volatile("v_mfma_f32_16x16x32_bf16 %0, %1, %2, %0" : "+v"(acc[5][2]) : "v"(W1[5]), "v"(X1[2]));
    asm volatile("s_addk_i32 m0, 0x1000" ::: "scc", "memory");
    asm volatile("ds_read_b64 %0, %1 offset:2048" : "=a"(sst) : "v"(pk - lane*8) : "memory");
    asm volatile("v_mfma_f32_16x16x32_bf16 %0, %1, %2, %0" : "+v"(acc[5][3]) : "v"(W1[5]), "v"(X1[3]));
    asm volatile("buffer_load_dwordx4 %0, %1, %2 offen lds" :: "v"(wv[1]), "s"(rW), "s"(kw) : "memory");
    asm volatile("s_waitcnt lgkmcnt(0)" ::: "memory");
    asm volatile("v_mfma_f32_16x16x32_bf16 %0, %1, %2, %0" : "+v"(acc[0][0]) : "v"(W0[0]), "v"(X0[0]));
    asm volatile("ds_read_b128 %0, %1 offset:0" : "=v"(X1[0]) : "v"(xa1) : "memory");
    asm volatile("v_mfma_f32_16x16x32_bf16 %0, %1, %2, %0" : "+v"(acc[0][1]) : "v"(W0[0]), "v"(X0[1]));
    asm volatile("ds_read_b128 %0, %1 offset:2048" : "=v"(X1[1]) : "v"(xa1) : "memory");
    asm volatile("v_mfma_f32_16x16x32_bf16 %0, %1, %2, %0" : "+v"(acc[0][2]) : "v"(W0[0]), "v"(X0[2]));
    asm volatile("ds_read_b128 %0, %1 offset:4096" : "=v"(X1[2]) : "v"(xa1) : "memory");
    asm volatile("v_mfma_f32_16x16x32_bf16 %0, %1, %2, %0" : "+v"(acc[0][3]) : "v"(W0[0]), "v"(X0[3]));
    asm volatile("ds_read_b128 %0, %1 offset:6144" : "=v"(X1[3]) : "v"(xa1) : "memory");
    asm volatile("s_addk_i32 m0, 0x1000" ::: "scc", "memory");
    asm volatile("buffer_store_dwordx4 %0, %1, %2, %3 offen sc1\n\ts_nop 1" :: "v"(cat(pa0, pa1)), "v"(vl16), "s"(rS), "s"(262144 + ((40 + k0)*40 + d0 + w)*1024) : "memory");
    asm volatile("v_mfma_f32_16x16x32_bf16 %0, %1, %2, %0" : "+v"(acc[1][0]) : "v"(W0[1]), "v"(X0[0]));
    asm volatile("ds_read_b64_tr_b16 %0, %1 offset:0" : "=v"(wl[0]) : "v"(wa1[0]) : "memory");
    asm volatile("buffer_load_dwordx4 %0, %1, %2 offen lds" :: "v"(wv[2]), "s"(rW), "s"(kw) : "memory");
    asm volatile("v_mfma_f32_16x16x32_bf16 %0, %1, %2, %0" : "+v"(acc[1][1]) : "v"(W0[1]), "v"(X0[1]));
    asm volatile("ds_read_b64_tr_b16 %0, %1 offset:1536" : "=v"(wh[0]) : "v"(wa1[0]) : "memory");
    W1[0] = cat(wl[0], wh[0]);
    asm volatile("v_mfma_f32_16x16x32_bf16 %0, %1, %2, %0" : "+v"(acc[1][2]) : "v"(W0[1]), "v"(X0[2]));
    asm volatile("ds_read_b64_tr_b16 %0, %1 offset:0" : "=v"(wl[1]) : "v"(wa1[1]) : "memory");
    asm volatile("v_mfma_f32_16x16x32_bf16 %0, %1, %2, %0" : "+v"(acc[1][3]) : "v"(W0[1]), "v"(X0[3]));
    asm volatile("ds_read_b64_tr_b16 %0, %1 offset:1536" : "=v"(wh[1]) : "v"(wa1[1]) : "memory");
    W1[1] = cat(wl[1], wh[1]);
    asm volatile("s_addk_i32 m0, 0x1000" ::: "scc", "memory");
    asm volatile("buffer_store_dwordx4 %0, %1, %2, %3 offen sc1\n\ts_nop 1" :: "v"(cat(pb0, pb1)), "v"(vl16), "s"(rS), "s"(262144 + ((40 + k1)*40 + d1 + w)*1024) : "memory");
    asm volatile("v_mfma_f32_16x16x32_bf16 %0, %1, %2, %0" : "+v"(acc[2][0]) : "v"(W0[2]), "v"(X0[0]));
    asm volatile("ds_read_b64_tr_b16 %0, %1 offset:0" : "=v"(wl[2]) : "v"(wa1[2]) : "memory");
    asm volatile("buffer_load_dwordx4 %0, %1, %2 offen lds" :: "v"(wv[3]), "s"(rW), "s"(kw) : "memory");
    asm volatile("v_mfma_f32_16x16x32_bf16 %0, %1, %2, %0" : "+v"(acc[2][1]) : "v"(W0[2]), "v"(X0[1]));
    asm volatile("ds_read_b64_tr_b16 %0, %1 offset:1536" : "=v"(wh[2]) : "v"(wa1[2]) : "memory");
    W1[2] = cat(wl[2], wh[2]);
    asm volatile("v_mfma_f32_16x16x32_bf16 %0, %1, %2, %0" : "+v"(acc[2][2]) : "v"(W0[2]), "v"(X0[2]));
    asm volatile("ds_read_b64_tr_b16 %0, %1 offset:0" : "=v"(wl[3]) : "v"(wa1[3]) : "memory");
    asm volatile("v_mfma_f32_16x16x32_bf16 %0, %1, %2, %0" : "+v"(acc[2][3]) : "v"(W0[2]), "v"(X0[3]));
    asm volatile("ds_read_b64_tr_b16 %0, %1 offset:1536" : "=v"(wh[3]) : "v"(wa1[3]) : "memory");
    W1[3] = cat(wl[3], wh[3]);
    asm volatile("buffer_store_dwordx4 %0, %1, %2, %3 offen sc1\n\ts_nop 1" :: "a"(so2), "v"(vl16), "s"(rS), "s"(262144 + ((4*cpb + w)*40 + cdt)*1024) : "memory");
    asm volatile("v_mfma_f32_16x16x32_bf16 %0, %1, %2, %0" : "+v"(acc[3][0]) : "v"(W0[3]), "v"(X0[0]));
    asm volatile("ds_read_b64_tr_b16 %0, %1 offset:128" : "=v"(wl[4]) : "v"(wa1[0]) : "memory");
    asm volatile("s_addk_i32 m0, 0x1000" ::: "scc", "memory");
    asm volatile("v_mfma_f32_16x16x32_bf16 %0, %1, %2, %0" : "+v"(acc[3][1]) : "v"(W0[3]), "v"(X0[1]));
    asm volatile("ds_read_b64_tr_b16 %0, %1 offset:1664" : "=v"(wh[4]) : "v"(wa1[0]) : "memory");
    W1[4] = cat(wl[4], wh[4]);
    asm volatile("buffer_load_dwordx4 %0, %1, %2 offen lds" :: "v"(wv[4]), "s"(rW), "s"(kw) : "memory");
    asm volatile("v_mfma_f32_16x16x32_bf16 %0, %1, %2, %0" : "+v"(acc[3][2]) : "v"(W0[3]), "v"(X0[2]));
    asm volatile("ds_read_b64_tr_b16 %0, %1 offset:128" : "=v"(wl[5]) : "v"(wa1[1]) : "memory");
    asm volatile("v_mfma_f32_16x16x32_bf16 %0, %1, %2, %0" : "+v"(acc[3][3]) : "v"(W0[3]), "v"(X0[3]));
    asm volatile("ds_read_b64_tr_b16 %0, %1 offset:1664" : "=v"(wh[5]) : "v"(wa1[1]) : "memory");
    W1[5] = cat(wl[5], wh[5]);
    asm volatile("buffer_store_dwordx4 %0, %1, %2, %3 offen sc1\n\ts_nop 1" :: "a"(so3), "v"(vl16), "s"(rS), "s"(262144 + ((4*cpb + w)*40 + cdt + 1)*1024) : "memory");
    asm volatile("v_mfma_f32_16x16x32_bf16 %0, %1, %2, %0" : "+v"(acc[4][0]) : "v"(W0[4]), "v"(X0[0]));
    asm volatile("s_addk_i32 m0, 0x1000" ::: "scc", "memory");
    asm volatile("v_mfma_f32_16x16x32_bf16 %0, %1, %2, %0" : "+v"(acc[4][1]) : "v"(W0[4]), "v"(X0[1]));
    asm volatile("buffer_load_dwordx4 %0, %1, %2 offen lds" :: "v"(wv[5]), "s"(rW), "s"(kw) : "memory");
    asm volatile("s_add_u32 %0, %0, 196608\n\ts_cmp_eq_u32 %0, 1966080\n\ts_cselect_b32 %0, 0, %0" : "+s"(kw) :: "scc");
    asm volatile("v_mfma_f32_16x16x32_bf16 %0, %1, %2, %0" : "+v"(acc[4][2]) : "v"(W0[4]), "v"(X0[2]));
    asm volatile("v_mfma_f32_16x16x32_bf16 %0, %1, %2, %0" : "+v"(acc[4][3]) : "v"(W0[4]), "v"(X0[3]));
    asm volatile("buffer_store_dwordx2 %0, %1, %2, 0 offen sc1" :: "a"(sst), "v"((lane & 31) == 0 ? 40960 + (srow + (lane >> 5))*8 : OOB), "s"(rS) : "memory");
    asm volatile("v_mfma_f32_16x16x32_bf16 %0, %1, %2, %0" : "+v"(acc[5][0]) : "v"(W0[5]), "v"(X0[0]));
    asm volatile("v_mfma_f32_16x16x32_bf16 %0, %1, %2, %0" : "+v"(acc[5][1]) : "v"(W0[5]), "v"(X0[1]));
    asm volatile("v_mfma_f32_16x16x32_bf16 %0, %1, %2, %0" : "+v"(acc[5][2]) : "v"(W0[5]), "v"(X0[2]));
    asm volatile("v_mfma_f32_16x16x32_bf16 %0, %1, %2, %0" : "+v"(acc[5][3]) : "v"(W0[5]), "v"(X0[3]));
    asm volatile("s_waitcnt vmcnt(17) lgkmcnt(0)\n\ts_barrier" ::: "memory");
    asm volatile("v_mfma_f32_16x16x32_bf16 %0, %1, %2, %0" : "+v"(acc[0][0]) : "v"(W1[0]), "v"(X1[0]));
    asm volatile("ds_read_b128 %0, %1 offset:32768" : "=v"(X0[0]) : "v"(xa0) : "memory");
    asm volatile("s_add_u32 m0, %0, 12288" :: "s"(lw) : "scc", "memory");
    asm volatile("v_mfma_f32_16x16x32_bf16 %0, %1, %2, %0" : "+v"(acc[0][1]) : "v"(W1[0]), "v"(X1[1]));
    asm volatile("ds_read_b128 %0, %1 offset:34816" : "=v"(X0[1]) : "v"(xa0) : "memory");
    asm volatile("buffer_load_dwordx4 %0, %1, %2 offen lds" :: "v"(xv[0]), "s"(rX), "s"(kx) : "memory");
    asm volatile("v_mfma_f32_16x16x32_bf16 %0, %1, %2, %0" : "+v"(acc[0][2]) : "v"(W1[0]), "v"(X1[2]));
    asm volatile("ds_read_b128 %0, %1 offset:36864" : "=v"(X0[2]) : "v"(xa0) : "memory");
    asm volatile("v_mfma_f32_16x16x32_bf16 %0, %1, %2, %0" : "+v"(acc[0][3]) : "v"(W1[0]), "v"(X1[3]));
    asm volatile("ds_read_b128 %0, %1 offset:38912" : "=v"(X0[3]) : "v"(xa0) : "memory");
    asm volatile("v_mfma_f32_16x16x32_bf16 %0, %1, %2, %0" : "+v"(acc[1][0]) : "v"(W1[1]), "v"(X1[0]));
    asm volatile("ds_read_b64_tr_b16 %0, %1 offset:49152" : "=v"(wl[0]) : "v"(wa0[0]) : "memory");
    asm volatile("s_addk_i32 m0, 0x1000" ::: "scc", "memory");
    asm volatile("v_mfma_f32_16x16x32_bf16 %0, %1, %2, %0" : "+v"(acc[1][1]) : "v"(W1[1]), "v"(X1[1]));
    asm volatile("ds_read_b64_tr_b16 %0, %1 offset:50688" : "=v"(wh[0]) : "v"(wa0[0]) : "memory");
    W0[0] = cat(wl[0], wh[0]);
    asm volatile("buffer_load_dwordx4 %0, %1, %2 offen lds" :: "v"(xv[1]), "s"(rX), "s"(kx) : "memory");
    asm volatile("v_mfma_f32_16x16x32_bf16 %0, %1, %2, %0" : "+v"(acc[1][2]) : "v"(W1[1]), "v"(X1[2]));
    asm volatile("ds_read_b64_tr_b16 %0, %1 offset:49152" : "=v"(wl[1]) : "v"(wa0[1]) : "memory");
    asm volatile("v_mfma_f32_16x16x32_bf16 %0, %1, %2, %0" : "+v"(acc[1][3]) : "v"(W1[1]), "v"(X1[3]));
    asm volatile("ds_read_b64_tr_b16 %0, %1 offset:50688" : "=v"(wh[1]) : "v"(wa0[1]) : "memory");
    W0[1] = cat(wl[1], wh[1]);
    asm volatile("v_mfma_f32_16x16x32_bf16 %0, %1, %2, %0" : "+v"(acc[2][0]) : "v"(W1[2]), "v"(X1[0]));
    asm volatile("ds_read_b64_tr_b16 %0, %1 offset:49152" : "=v"(wl[2]) : "v"(wa0[2]) : "memory");
    asm volatile("v_mfma_f32_16x16x32_bf16 %0, %1, %2, %0" : "+v"(acc[2][1]) : "v"(W1[2]), "v"(X1[1]));
    asm volatile("ds_read_b64_tr_b16 %0, %1 offset:50688" : "=v"(wh[2]) : "v"(wa0[2]) : "memory");
    W0[2] = cat(wl[2], wh[2]);
    asm volatile("s_addk_i32 m0, 0x1000" ::: "scc", "memory");
    asm volatile("v_mfma_f32_16x16x32_bf16 %0, %1, %2, %0" : "+v"(acc[2][2]) : "v"(W1[2]), "v"(X1[2]));
    asm volatile("ds_read_b64_tr_b16 %0, %1 offset:49152" : "=v"(wl[3]) : "v"(wa0[3]) : "memory");
    asm volatile("buffer_load_dwordx4 %0, %1, %2 offen lds" :: "v"(xv[2]), "s"(rX), "s"(kx) : "memory");
    asm volatile("v_mfma_f32_16x16x32_bf16 %0, %1, %2, %0" : "+v"(acc[2][3]) : "v"(W1[2]), "v"(X1[3]));
    asm volatile("ds_read_b64_tr_b16 %0, %1 offset:50688" : "=v"(wh[3]) : "v"(wa0[3]) : "memory");
    W0[3] = cat(wl[3], wh[3]);
    asm volatile("v_mfma_f32_16x16x32_bf16 %0, %1, %2, %0" : "+v"(acc[3][0]) : "v"(W1[3]), "v"(X1[0]));
    asm volatile("ds_read_b64_tr_b16 %0, %1 offset:49280" : "=v"(wl[4]) : "v"(wa0[0]) : "memory");
    asm volatile("v_mfma_f32_16x16x32_bf16 %0, %1, %2, %0" : "+v"(acc[3][1]) : "v"(W1[3]), "v"(X1[1]));
    asm volatile("ds_read_b64_tr_b16 %0, %1 offset:50816" : "=v"(wh[4]) : "v"(wa0[0]) : "memory");
    W0[4] = cat(wl[4], wh[4]);
    asm volatile("s_addk_i32 m0, 0x1000" ::: "scc", "memory");
    asm volatile("v_mfma_f32_16x16x32_bf16 %0, %1, %2, %0" : "+v"(acc[3][2]) : "v"(W1[3]), "v"(X1[2]));
    asm volatile("ds_read_b64_tr_b16 %0, %1 offset:49280" : "=v"(wl[5]) : "v"(wa0[1]) : "memory");
    asm volatile("buffer_load_dwordx4 %0, %1, %2 offen lds" :: "v"(xv[3]), "s"(rX), "s"(kx) : "memory");
    asm volatile("s_add_u32 %0, %0, 128\n\ts_cmp_eq_u32 %0, 1280\n\ts_cselect_b32 %0, 0, %0" : "+s"(kx) :: "scc");
    asm volatile("v_mfma_f32_16x16x32_bf16 %0, %1, %2, %0" : "+v"(acc[3][3]) : "v"(W1[3]), "v"(X1[3]));
    asm volatile("ds_read_b64_tr_b16 %0, %1 offset:50816" : "=v"(wh[5]) : "v"(wa0[1]) : "memory");
    W0[5] = cat(wl[5], wh[5]);
    asm volatile("v_mfma_f32_16x16x32_bf16 %0, %1, %2, %0" : "+v"(acc[4][0]) : "v"(W1[4]), "v"(X1[0]));
    asm volatile("v_mfma_f32_16x16x32_bf16 %0, %1, %2, %0" : "+v"(acc[4][1]) : "v"(W1[4]), "v"(X1[1]));
    asm volatile("v_mfma_f32_16x16x32_bf16 %0, %1, %2, %0" : "+v"(acc[4][2]) : "v"(W1[4]), "v"(X1[2]));
    asm volatile("s_add_u32 m0, %0, 61440" :: "s"(lw) : "scc", "memory");
    asm volatile("v_mfma_f32_16x16x32_bf16 %0, %1, %2, %0" : "+v"(acc[4][3]) : "v"(W1[4]), "v"(X1[3]));
    asm volatile("buffer_load_dwordx4 %0, %1, %2 offen lds" :: "v"(wv[0]), "s"(rW), "s"(kw) : "memory");
    asm volatile("v_mfma_f32_16x16x32_bf16 %0, %1, %2, %0" : "+v"(acc[5][0]) : "v"(W1[5]), "v"(X1[0]));
    asm volatile("v_mfma_f32_16x16x32_bf16 %0, %1, %2, %0" : "+v"(acc[5][1]) : "v"(W1[5]), "v"(X1[1]));
    asm volatile("v_mfma_f32_16x16x32_bf16 %0, %1, %2, %0" : "+v"(acc[5][2]) : "v"(W1[5]), "v"(X1[2]));
    asm volatile("s_addk_i32 m0, 0x1000" ::: "scc", "memory");
    asm volatile("v_mfma_f32_16x16x32_bf16 %0, %1, %2, %0" : "+v"(acc[5][3]) : "v"(W1[5]), "v"(X1[3]));
    asm volatile("buffer_load_dwordx4 %0, %1, %2 offen lds" :: "v"(wv[1]), "s"(rW), "s"(kw) : "memory");
    asm volatile("s_waitcnt lgkmcnt(0)" ::: "memory");
    asm volatile("v_mfma_f32_16x16x32_bf16 %0, %1, %2, %0" : "+v"(acc[0][0]) : "v"(W0[0]), "v"(X0[0]));
    asm volatile("ds_read_b128 %0, %1 offset:32768" : "=v"(X1[0]) : "v"(xa1) : "memory");
    asm volatile("v_mfma_f32_16x16x32_bf16 %0, %1, %2, %0" : "+v"(acc[0][1]) : "v"(W0[0]), "v"(X0[1]));
    asm volatile("ds_read_b128 %0, %1 offset:34816" : "=v"(X1[1]) : "v"(xa1) : "memory");
    asm volatile("v_mfma_f32_16x16x32_bf16 %0, %1, %2, %0" : "+v"(acc[0][2]) : "v"(W0[0]), "v"(X0[2]));
    asm volatile("ds_read_b128 %0, %1 offset:36864" : "=v"(X1[2]) : "v"(xa1) : "memory");
    asm volatile("v_mfma_f32_16x16x32_bf16 %0, %1, %2, %0" : "+v"(acc[0][3]) : "v"(W0[0]), "v"(X0[3]));
    asm volatile("ds_read_b128 %0, %1 offset:38912" : "=v"(X1[3]) : "v"(xa1) : "memory");
    asm volatile("s_addk_i32 m0, 0x1000" ::: "scc", "memory");
    asm volatile("v_mfma_f32_16x16x32_bf16 %0, %1, %2, %0" : "+v"(acc[1][0]) : "v"(W0[1]), "v"(X0[0]));
    asm volatile("ds_read_b64_tr_b16 %0, %1 offset:49152" : "=v"(wl[0]) : "v"(wa1[0]) : "memory");
    asm volatile("buffer_load_dwordx4 %0, %1, %2 offen lds" :: "v"(wv[2]), "s"(rW), "s"(kw) : "memory");
    asm volatile("v_mfma_f32_16x16x32_bf16 %0, %1, %2, %0" : "+v"(acc[1][1]) : "v"(W0[1]), "v"(X0[1]));
    asm volatile("ds_read_b64_tr_b16 %0, %1 offset:50688" : "=v"(wh[0]) : "v"(wa1[0]) : "memory");
    W1[0] = cat(wl[0], wh[0]);
    asm volatile("v_mfma_f32_16x16x32_bf16 %0, %1, %2, %0" : "+v"(acc[1][2]) : "v"(W0[1]), "v"(X0[2]));
    asm volatile("ds_read_b64_tr_b16 %0, %1 offset:49152" : "=v"(wl[1]) : "v"(wa1[1]) : "memory");
    asm volatile("v_mfma_f32_16x16x32_bf16 %0, %1, %2, %0" : "+v"(acc[1][3]) : "v"(W0[1]), "v"(X0[3]));
    asm volatile("ds_read_b64_tr_b16 %0, %1 offset:50688" : "=v"(wh[1]) : "v"(wa1[1]) : "memory");
    W1[1] = cat(wl[1], wh[1]);
    asm volatile("s_addk_i32 m0, 0x1000" ::: "scc", "memory");
    asm volatile("v_mfma_f32_16x16x32_bf16 %0, %1, %2, %0" : "+v"(acc[2][0]) : "v"(W0[2]), "v"(X0[0]));
    asm volatile("ds_read_b64_tr_b16 %0, %1 offset:49152" : "=v"(wl[2]) : "v"(wa1[2]) : "memory");
    asm volatile("buffer_load_dwordx4 %0, %1, %2 offen lds" :: "v"(wv[3]), "s"(rW), "s"(kw) : "memory");
    asm volatile("v_mfma_f32_16x16x32_bf16 %0, %1, %2, %0" : "+v"(acc[2][1]) : "v"(W0[2]), "v"(X0[1]));
    asm volatile("ds_read_b64_tr_b16 %0, %1 offset:50688" : "=v"(wh[2]) : "v"(wa1[2]) : "memory");
    W1[2] = cat(wl[2], wh[2]);
    asm volatile("v_mfma_f32_16x16x32_bf16 %0, %1, %2, %0" : "+v"(acc[2][2]) : "v"(W0[2]), "v"(X0[2]));
    asm volatile("ds_read_b64_tr_b16 %0, %1 offset:49152" : "=v"(wl[3]) : "v"(wa1[3]) : "memory");
    asm volatile("v_mfma_f32_16x16x32_bf16 %0, %1, %2, %0" : "+v"(acc[2][3]) : "v"(W0[2]), "v"(X0[3]));
    asm volatile("ds_read_b64_tr_b16 %0, %1 offset:50688" : "=v"(wh[3]) : "v"(wa1[3]) : "memory");
    W1[3] = cat(wl[3], wh[3]);
    asm volatile("v_mfma_f32_16x16x32_bf16 %0, %1, %2, %0" : "+v"(acc[3][0]) : "v"(W0[3]), "v"(X0[0]));
    asm volatile("ds_read_b64_tr_b16 %0, %1 offset:49280" : "=v"(wl[4]) : "v"(wa1[0]) : "memory");
    asm volatile("s_addk_i32 m0, 0x1000" ::: "scc", "memory");
    asm volatile("v_mfma_f32_16x16x32_bf16 %0, %1, %2, %0" : "+v"(acc[3][1]) : "v"(W0[3]), "v"(X0[1]));
    asm volatile("ds_read_b64_tr_b16 %0, %1 offset:50816" : "=v"(wh[4]) : "v"(wa1[0]) : "memory");
    W1[4] = cat(wl[4], wh[4]);
    asm volatile("buffer_load_dwordx4 %0, %1, %2 offen lds" :: "v"(wv[4]), "s"(rW), "s"(kw) : "memory");
    asm volatile("v_mfma_f32_16x16x32_bf16 %0, %1, %2, %0" : "+v"(acc[3][2]) : "v"(W0[3]), "v"(X0[2]));
    asm volatile("ds_read_b64_tr_b16 %0, %1 offset:49280" : "=v"(wl[5]) : "v"(wa1[1]) : "memory");
    asm volatile("v_mfma_f32_16x16x32_bf16 %0, %1, %2, %0" : "+v"(acc[3][3]) : "v"(W0[3]), "v"(X0[3]));
    asm volatile("ds_read_b64_tr_b16 %0, %1 offset:50816" : "=v"(wh[5]) : "v"(wa1[1]) : "memory");
    W1[5] = cat(wl[5], wh[5]);
    asm volatile("v_mfma_f32_16x16x32_bf16 %0, %1, %2, %0" : "+v"(acc[4][0]) : "v"(W0[4]), "v"(X0[0]));
    asm volatile("s_addk_i32 m0, 0x1000" ::: "scc", "memory");
    asm volatile("v_mfma_f32_16x16x32_bf16 %0, %1, %2, %0" : "+v"(acc[4][1]) : "v"(W0[4]), "v"(X0[1]));
    asm volatile("buffer_load_dwordx4 %0, %1, %2 offen lds" :: "v"(wv[5]), "s"(rW), "s"(kw) : "memory");
    asm volatile("s_add_u32 %0, %0, 196608\n\ts_cmp_eq_u32 %0, 1966080\n\ts_cselect_b32 %0, 0, %0" : "+s"(kw) :: "scc");
    asm volatile("v_mfma_f32_16x16x32_bf16 %0, %1, %2, %0" : "+v"(acc[4][2]) : "v"(W0[4]), "v"(X0[2]));
    asm volatile("v_mfma_f32_16x16x32_bf16 %0, %1, %2, %0" : "+v"(acc[4][3]) : "v"(W0[4]), "v"(X0[3]));
    asm volatile("v_mfma_f32_16x16x32_bf16 %0, %1, %2, %0" : "+v"(acc[5][0]) : "v"(W0[5]), "v"(X0[0]));
    asm volatile("v_mfma_f32_16x16x32_bf16 %0, %1, %2, %0" : "+v"(acc[5][1]) : "v"(W0[5]), "v"(X0[1]));
    asm volatile("v_mfma_f32_16x16x32_bf16 %0, %1, %2, %0" : "+v"(acc[5][2]) : "v"(W0[5]), "v"(X0[2]));
    asm volatile("v_mfma_f32_16x16x32_bf16 %0, %1, %2, %0" : "+v"(acc[5][3]) : "v"(W0[5]), "v"(X0[3]));
    asm volatile("s_waitcnt vmcnt(10) lgkmcnt(0)\n\ts_barrier" ::: "memory");
    __builtin_amdgcn_raw_buffer_store_b32(tag, rS, tid == 0 ? 3932160 + b*64 : OOB, 0, SC1);
    asm volatile("v_mfma_f32_16x16x32_bf16 %0, %1, %2, %0" : "+v"(acc[0][0]) : "v"(W1[0]), "v"(X1[0]));
    asm volatile("ds_read_b128 %0, %1 offset:16384" : "=v"(X0[0]) : "v"(xa0) : "memory");
    asm volatile("s_add_u32 m0, %0, 45056" :: "s"(lw) : "scc", "memory");
    asm volatile("v_mfma_f32_16x16x32_bf16 %0, %1, %2, %0" : "+v"(acc[0][1]) : "v"(W1[0]), "v"(X1[1]));
    asm volatile("ds_read_b128 %0, %1 offset:18432" : "=v"(X0[1]) : "v"(xa0) : "memory");
    asm volatile("buffer_load_dwordx4 %0, %1, %2 offen lds" :: "v"(xv[0]), "s"(rX), "s"(kx) : "memory");
    asm volatile("v_mfma_f32_16x16x32_bf16 %0, %1, %2, %0" : "+v"(acc[0][2]) : "v"(W1[0]), "v"(X1[2]));
    asm volatile("ds_read_b128 %0, %1 offset:20480" : "=v"(X0[2]) : "v"(xa0) : "memory");
    asm volatile("v_mfma_f32_16x16x32_bf16 %0, %1, %2, %0" : "+v"(acc[0][3]) : "v"(W1[0]), "v"(X1[3]));
    asm volatile("ds_read_b128 %0, %1 offset:22528" : "=v"(X0[3]) : "v"(xa0) : "memory");
    asm volatile("v_mfma_f32_16x16x32_bf16 %0, %1, %2, %0" : "+v"(acc[1][0]) : "v"(W1[1]), "v"(X1[0]));
    asm volatile("ds_read_b64_tr_b16 %0, %1 offset:24576" : "=v"(wl[0]) : "v"(wa0[0]) : "memory");
    asm volatile("s_addk_i32 m0, 0x1000" ::: "scc", "memory");
    asm volatile("v_mfma_f32_16x16x32_bf16 %0, %1, %2, %0" : "+v"(acc[1][1]) : "v"(W1[1]), "v"(X1[1]));
    asm volatile("ds_read_b64_tr_b16 %0, %1 offset:26112" : "=v"(wh[0]) : "v"(wa0[0]) : "memory");
    W0[0] = cat(wl[0], wh[0]);
    asm volatile("buffer_load_dwordx4 %0, %1, %2 offen lds" :: "v"(xv[1]), "s"(rX), "s"(kx) : "memory");
    asm volatile("v_mfma_f32_16x16x32_bf16 %0, %1, %2, %0" : "+v"(acc[1][2]) : "v"(W1[1]), "v"(X1[2]));
    asm volatile("ds_read_b64_tr_b16 %0, %1 offset:24576" : "=v"(wl[1]) : "v"(wa0[1]) : "memory");
    asm volatile("v_mfma_f32_16x16x32_bf16 %0, %1, %2, %0" : "+v"(acc[1][3]) : "v"(W1[1]), "v"(X1[3]));
    asm volatile("ds_read_b64_tr_b16 %0, %1 offset:26112" : "=v"(wh[1]) : "v"(wa0[1]) : "memory");
    W0[1] = cat(wl[1], wh[1]);
    asm volatile("v_mfma_f32_16x16x32_bf16 %0, %1, %2, %0" : "+v"(acc[2][0]) : "v"(W1[2]), "v"(X1[0]));
    asm volatile("ds_read_b64_tr_b16 %0, %1 offset:24576" : "=v"(wl[2]) : "v"(wa0[2]) : "memory");
    asm volatile("v_mfma_f32_16x16x32_bf16 %0, %1, %2, %0" : "+v"(acc[2][1]) : "v"(W1[2]), "v"(X1[1]));
    asm volatile("ds_read_b64_tr_b16 %0, %1 offset:26112" : "=v"(wh[2]) : "v"(wa0[2]) : "memory");
    W0[2] = cat(wl[2], wh[2]);
    asm volatile("s_addk_i32 m0, 0x1000" ::: "scc", "memory");
    asm volatile("v_mfma_f32_16x16x32_bf16 %0, %1, %2, %0" : "+v"(acc[2][2]) : "v"(W1[2]), "v"(X1[2]));
    asm volatile("ds_read_b64_tr_b16 %0, %1 offset:24576" : "=v"(wl[3]) : "v"(wa0[3]) : "memory");
    asm volatile("buffer_load_dwordx4 %0, %1, %2 offen lds" :: "v"(xv[2]), "s"(rX), "s"(kx) : "memory");
    asm volatile("v_mfma_f32_16x16x32_bf16 %0, %1, %2, %0" : "+v"(acc[2][3]) : "v"(W1[2]), "v"(X1[3]));
    asm volatile("ds_read_b64_tr_b16 %0, %1 offset:26112" : "=v"(wh[3]) : "v"(wa0[3]) : "memory");
    W0[3] = cat(wl[3], wh[3]);
    asm volatile("v_mfma_f32_16x16x32_bf16 %0, %1, %2, %0" : "+v"(acc[3][0]) : "v"(W1[3]), "v"(X1[0]));
    asm volatile("ds_read_b64_tr_b16 %0, %1 offset:24704" : "=v"(wl[4]) : "v"(wa0[0]) : "memory");
    asm volatile("v_mfma_f32_16x16x32_bf16 %0, %1, %2, %0" : "+v"(acc[3][1]) : "v"(W1[3]), "v"(X1[1]));
    asm volatile("ds_read_b64_tr_b16 %0, %1 offset:26240" : "=v"(wh[4]) : "v"(wa0[0]) : "memory");
    W0[4] = cat(wl[4], wh[4]);
    asm volatile("s_addk_i32 m0, 0x1000" ::: "scc", "memory");
    asm volatile("v_mfma_f32_16x16x32_bf16 %0, %1, %2, %0" : "+v"(acc[3][2]) : "v"(W1[3]), "v"(X1[2]));
    asm volatile("ds_read_b64_tr_b16 %0, %1 offset:24704" : "=v"(wl[5]) : "v"(wa0[1]) : "memory");
    asm volatile("buffer_load_dwordx4 %0, %1, %2 offen lds" :: "v"(xv[3]), "s"(rX), "s"(kx) : "memory");
    asm volatile("s_add_u32 %0, %0, 128\n\ts_cmp_eq_u32 %0, 1280\n\ts_cselect_b32 %0, 0, %0" : "+s"(kx) :: "scc");
    asm volatile("v_mfma_f32_16x16x32_bf16 %0, %1, %2, %0" : "+v"(acc[3][3]) : "v"(W1[3]), "v"(X1[3]));
    asm volatile("ds_read_b64_tr_b16 %0, %1 offset:26240" : "=v"(wh[5]) : "v"(wa0[1]) : "memory");
    W0[5] = cat(wl[5], wh[5]);
    asm volatile("v_mfma_f32_16x16x32_bf16 %0, %1, %2, %0" : "+v"(acc[4][0]) : "v"(W1[4]), "v"(X1[0]));
    asm volatile("v_mfma_f32_16x16x32_bf16 %0, %1, %2, %0" : "+v"(acc[4][1]) : "v"(W1[4]), "v"(X1[1]));
    asm volatile("v_mfma_f32_16x16x32_bf16 %0, %1, %2, %0" : "+v"(acc[4][2]) : "v"(W1[4]), "v"(X1[2]));
    asm volatile("s_add_u32 m0, %0, 110592" :: "s"(lw) : "scc", "memory");
    asm volatile("v_mfma_f32_16x16x32_bf16 %0, %1, %2, %0" : "+v"(acc[4][3]) : "v"(W1[4]), "v"(X1[3]));
    asm volatile("buffer_load_dwordx4 %0, %1, %2 offen lds" :: "v"(wv[0]), "s"(rW), "s"(kw) : "memory");
    asm volatile("v_mfma_f32_16x16x32_bf16 %0, %1, %2, %0" : "+v"(acc[5][0]) : "v"(W1[5]), "v"(X1[0]));
    asm volatile("v_mfma_f32_16x16x32_bf16 %0, %1, %2, %0" : "+v"(acc[5][1]) : "v"(W1[5]), "v"(X1[1]));
    asm volatile("v_mfma_f32_16x16x32_bf16 %0, %1, %2, %0" : "+v"(acc[5][2]) : "v"(W1[5]), "v"(X1[2]));
    asm volatile("s_addk_i32 m0, 0x1000" ::: "scc", "memory");
    asm volatile("v_mfma_f32_16x16x32_bf16 %0, %1, %2, %0" : "+v"(acc[5][3]) : "v"(W1[5]), "v"(X1[3]));
    asm volatile("buffer_load_dwordx4 %0, %1, %2 offen lds" :: "v"(wv[1]), "s"(rW), "s"(kw) : "memory");
    asm volatile("s_waitcnt lgkmcnt(0)" ::: "memory");
    asm volatile("v_mfma_f32_16x16x32_bf16 %0, %1, %2, %0" : "+v"(acc[0][0]) : "v"(W0[0]), "v"(X0[0]));
    asm volatile("ds_read_b128 %0, %1 offset:16384" : "=v"(X1[0]) : "v"(xa1) : "memory");
    asm volatile("v_mfma_f32_16x16x32_bf16 %0, %1, %2, %0" : "+v"(acc[0][1]) : "v"(W0[0]), "v"(X0[1]));
    asm volatile("ds_read_b128 %0, %1 offset:18432" : "=v"(X1[1]) : "v"(xa1) : "memory");
    asm volatile("v_mfma_f32_16x16x32_bf16 %0, %1, %2, %0" : "+v"(acc[0][2]) : "v"(W0[0]), "v"(X0[2]));
    asm volatile("ds_read_b128 %0, %1 offset:20480" : "=v"(X1[2]) : "v"(xa1) : "memory");
    asm volatile("v_mfma_f32_16x16x32_bf16 %0, %1, %2, %0" : "+v"(acc[0][3]) : "v"(W0[0]), "v"(X0[3]));
    asm volatile("ds_read_b128 %0, %1 offset:22528" : "=v"(X1[3]) : "v"(xa1) : "memory");
    asm volatile("s_addk_i32 m0, 0x1000" ::: "scc", "memory");
    asm volatile("v_mfma_f32_16x16x32_bf16 %0, %1, %2, %0" : "+v"(acc[1][0]) : "v"(W0[1]), "v"(X0[0]));
    asm volatile("ds_read_b64_tr_b16 %0, %1 offset:24576" : "=v"(wl[0]) : "v"(wa1[0]) : "memory");
    asm volatile("buffer_load_dwordx4 %0, %1, %2 offen lds" :: "v"(wv[2]), "s"(rW), "s"(kw) : "memory");
    asm volatile("v_mfma_f32_16x16x32_bf16 %0, %1, %2, %0" : "+v"(acc[1][1]) : "v"(W0[1]), "v"(X0[1]));
    asm volatile("ds_read_b64_tr_b16 %0, %1 offset:26112" : "=v"(wh[0]) : "v"(wa1[0]) : "memory");
    W1[0] = cat(wl[0], wh[0]);
    asm volatile("v_mfma_f32_16x16x32_bf16 %0, %1, %2, %0" : "+v"(acc[1][2]) : "v"(W0[1]), "v"(X0[2]));
    asm volatile("ds_read_b64_tr_b16 %0, %1 offset:24576" : "=v"(wl[1]) : "v"(wa1[1]) : "memory");
    asm volatile("v_mfma_f32_16x16x32_bf16 %0, %1, %2, %0" : "+v"(acc[1][3]) : "v"(W0[1]), "v"(X0[3]));
    asm volatile("ds_read_b64_tr_b16 %0, %1 offset:26112" : "=v"(wh[1]) : "v"(wa1[1]) : "memory");
    W1[1] = cat(wl[1], wh[1]);
    asm volatile("s_addk_i32 m0, 0x1000" ::: "scc", "memory");
    asm volatile("v_mfma_f32_16x16x32_bf16 %0, %1, %2, %0" : "+v"(acc[2][0]) : "v"(W0[2]), "v"(X0[0]));
    asm volatile("ds_read_b64_tr_b16 %0, %1 offset:24576" : "=v"(wl[2]) : "v"(wa1[2]) : "memory");
    asm volatile("buffer_load_dwordx4 %0, %1, %2 offen lds" :: "v"(wv[3]), "s"(rW), "s"(kw) : "memory");
    asm volatile("v_mfma_f32_16x16x32_bf16 %0, %1, %2, %0" : "+v"(acc[2][1]) : "v"(W0[2]), "v"(X0[1]));
    asm volatile("ds_read_b64_tr_b16 %0, %1 offset:26112" : "=v"(wh[2]) : "v"(wa1[2]) : "memory");
    W1[2] = cat(wl[2], wh[2]);
    asm volatile("v_mfma_f32_16x16x32_bf16 %0, %1, %2, %0" : "+v"(acc[2][2]) : "v"(W0[2]), "v"(X0[2]));
    asm volatile("ds_read_b64_tr_b16 %0, %1 offset:24576" : "=v"(wl[3]) : "v"(wa1[3]) : "memory");
    asm volatile("v_mfma_f32_16x16x32_bf16 %0, %1, %2, %0" : "+v"(acc[2][3]) : "v"(W0[2]), "v"(X0[3]));
    asm volatile("ds_read_b64_tr_b16 %0, %1 offset:26112" : "=v"(wh[3]) : "v"(wa1[3]) : "memory");
    W1[3] = cat(wl[3], wh[3]);
    asm volatile("v_mfma_f32_16x16x32_bf16 %0, %1, %2, %0" : "+v"(acc[3][0]) : "v"(W0[3]), "v"(X0[0]));
    asm volatile("ds_read_b64_tr_b16 %0, %1 offset:24704" : "=v"(wl[4]) : "v"(wa1[0]) : "memory");
    asm volatile("s_addk_i32 m0, 0x1000" ::: "scc", "memory");
    asm volatile("v_mfma_f32_16x16x32_bf16 %0, %1, %2, %0" : "+v"(acc[3][1]) : "v"(W0[3]), "v"(X0[1]));
    asm volatile("ds_read_b64_tr_b16 %0, %1 offset:26240" : "=v"(wh[4]) : "v"(wa1[0]) : "memory");
    W1[4] = cat(wl[4], wh[4]);
    asm volatile("buffer_load_dwordx4 %0, %1, %2 offen lds" :: "v"(wv[4]), "s"(rW), "s"(kw) : "memory");
    asm volatile("v_mfma_f32_16x16x32_bf16 %0, %1, %2, %0" : "+v"(acc[3][2]) : "v"(W0[3]), "v"(X0[2]));
    asm volatile("ds_read_b64_tr_b16 %0, %1 offset:24704" : "=v"(wl[5]) : "v"(wa1[1]) : "memory");
    asm volatile("v_mfma_f32_16x16x32_bf16 %0, %1, %2, %0" : "+v"(acc[3][3]) : "v"(W0[3]), "v"(X0[3]));
    asm volatile("ds_read_b64_tr_b16 %0, %1 offset:26240" : "=v"(wh[5]) : "v"(wa1[1]) : "memory");
    W1[5] = cat(wl[5], wh[5]);
    asm volatile("v_mfma_f32_16x16x32_bf16 %0, %1, %2, %0" : "+v"(acc[4][0]) : "v"(W0[4]), "v"(X0[0]));
    asm volatile("s_addk_i32 m0, 0x1000" ::: "scc", "memory");
    asm volatile("v_mfma_f32_16x16x32_bf16 %0, %1, %2, %0" : "+v"(acc[4][1]) : "v"(W0[4]), "v"(X0[1]));
    asm volatile("buffer_load_dwordx4 %0, %1, %2 offen lds" :: "v"(wv[5]), "s"(rW), "s"(kw) : "memory");
    asm volatile("s_add_u32 %0, %0, 196608\n\ts_cmp_eq_u32 %0, 1966080\n\ts_cselect_b32 %0, 0, %0" : "+s"(kw) :: "scc");
    asm volatile("v_mfma_f32_16x16x32_bf16 %0, %1, %2, %0" : "+v"(acc[4][2]) : "v"(W0[4]), "v"(X0[2]));
    asm volatile("v_mfma_f32_16x16x32_bf16 %0, %1, %2, %0" : "+v"(acc[4][3]) : "v"(W0[4]), "v"(X0[3]));
    asm volatile("v_mfma_f32_16x16x32_bf16 %0, %1, %2, %0" : "+v"(acc[5][0]) : "v"(W0[5]), "v"(X0[0]));
    asm volatile("v_mfma_f32_16x16x32_bf16 %0, %1, %2, %0" : "+v"(acc[5][1]) : "v"(W0[5]), "v"(X0[1]));
    asm volatile("v_mfma_f32_16x16x32_bf16 %0, %1, %2, %0" : "+v"(acc[5][2]) : "v"(W0[5]), "v"(X0[2]));
    asm volatile("v_mfma_f32_16x16x32_bf16 %0, %1, %2, %0" : "+v"(acc[5][3]) : "v"(W0[5]), "v"(X0[3]));
    asm volatile("s_waitcnt vmcnt(11) lgkmcnt(0)\n\ts_barrier" ::: "memory");
    asm volatile("v_mfma_f32_16x16x32_bf16 %0, %1, %2, %0" : "+v"(acc[0][0]) : "v"(W1[0]), "v"(X1[0]));
    asm volatile("ds_read_b128 %0, %1 offset:0" : "=v"(X0[0]) : "v"(xa0) : "memory");
    asm volatile("s_add_u32 m0, %0, 28672" :: "s"(lw) : "scc", "memory");
    asm volatile("v_mfma_f32_16x16x32_bf16 %0, %1, %2, %0" : "+v"(acc[0][1]) : "v"(W1[0]), "v"(X1[1]));
    asm volatile("ds_read_b128 %0, %1 offset:2048" : "=v"(X0[1]) : "v"(xa0) : "memory");
    asm volatile("buffer_load_dwordx4 %0, %1, %2 offen lds" :: "v"(xv[0]), "s"(rX), "s"(kx) : "memory");
    asm volatile("v_mfma_f32_16x16x32_bf16 %0, %1, %2, %0" : "+v"(acc[0][2]) : "v"(W1[0]), "v"(X1[2]));
    asm volatile("ds_read_b128 %0, %1 offset:4096" : "=v"(X0[2]) : "v"(xa0) : "memory");
    asm volatile("v_mfma_f32_16x16x32_bf16 %0, %1, %2, %0" : "+v"(acc[0][3]) : "v"(W1[0]), "v"(X1[3]));
    asm volatile("ds_read_b128 %0, %1 offset:6144" : "=v"(X0[3]) : "v"(xa0) : "memory");
    asm volatile("v_mfma_f32_16x16x32_bf16 %0, %1, %2, %0" : "+v"(acc[1][0]) : "v"(W1[1]), "v"(X1[0]));
    asm volatile("ds_read_b64_tr_b16 %0, %1 offset:0" : "=v"(wl[0]) : "v"(wa0[0]) : "memory");
    asm volatile("s_addk_i32 m0, 0x1000" ::: "scc", "memory");
    asm volatile("v_mfma_f32_16x16x32_bf16 %0, %1, %2, %0" : "+v"(acc[1][1]) : "v"(W1[1]), "v"(X1[1]));
    asm volatile("ds_read_b64_tr_b16 %0, %1 offset:1536" : "=v"(wh[0]) : "v"(wa0[0]) : "memory");
    W0[0] = cat(wl[0], wh[0]);
    asm volatile("buffer_load_dwordx4 %0, %1, %2 offen lds" :: "v"(xv[1]), "s"(rX), "s"(kx) : "memory");
    asm volatile("v_mfma_f32_16x16x32_bf16 %0, %1, %2, %0" : "+v"(acc[1][2]) : "v"(W1[1]), "v"(X1[2]));
    asm volatile("ds_read_b64_tr_b16 %0, %1 offset:0" : "=v"(wl[1]) : "v"(wa0[1]) : "memory");
    asm volatile("v_mfma_f32_16x16x32_bf16 %0, %1, %2, %0" : "+v"(acc[1][3]) : "v"(W1[1]), "v"(X1[3]));
    asm volatile("ds_read_b64_tr_b16 %0, %1 offset:1536" : "=v"(wh[1]) : "v"(wa0[1]) : "memory");
    W0[1] = cat(wl[1], wh[1]);
    asm volatile("v_mfma_f32_16x16x32_bf16 %0, %1, %2, %0" : "+v"(acc[2][0]) : "v"(W1[2]), "v"(X1[0]));
    asm volatile("ds_read_b64_tr_b16 %0, %1 offset:0" : "=v"(wl[2]) : "v"(wa0[2]) : "memory");
    asm volatile("v_mfma_f32_16x16x32_bf16 %0, %1, %2, %0" : "+v"(acc[2][1]) : "v"(W1[2]), "v"(X1[1]));
    asm volatile("ds_read_b64_tr_b16 %0, %1 offset:1536" : "=v"(wh[2]) : "v"(wa0[2]) : "memory");
    W0[2] = cat(wl[2], wh[2]);
    asm volatile("s_addk_i32 m0, 0x1000" ::: "scc", "memory");
    asm volatile("v_mfma_f32_16x16x32_bf16 %0, %1, %2, %0" : "+v"(acc[2][2]) : "v"(W1[2]), "v"(X1[2]));
    asm volatile("ds_read_b64_tr_b16 %0, %1 offset:0" : "=v"(wl[3]) : "v"(wa0[3]) : "memory");
    asm volatile("buffer_load_dwordx4 %0, %1, %2 offen lds" :: "v"(xv[2]), "s"(rX), "s"(kx) : "memory");
    asm volatile("v_mfma_f32_16x16x32_bf16 %0, %1, %2, %0" : "+v"(acc[2][3]) : "v"(W1[2]), "v"(X1[3]));
    asm volatile("ds_read_b64_tr_b16 %0, %1 offset:1536" : "=v"(wh[3]) : "v"(wa0[3]) : "memory");
    W0[3] = cat(wl[3], wh[3]);
    asm volatile("v_mfma_f32_16x16x32_bf16 %0, %1, %2, %0" : "+v"(acc[3][0]) : "v"(W1[3]), "v"(X1[0]));
    asm volatile("ds_read_b64_tr_b16 %0, %1 offset:128" : "=v"(wl[4]) : "v"(wa0[0]) : "memory");
    asm volatile("v_mfma_f32_16x16x32_bf16 %0, %1, %2, %0" : "+v"(acc[3][1]) : "v"(W1[3]), "v"(X1[1]));
    asm volatile("ds_read_b64_tr_b16 %0, %1 offset:1664" : "=v"(wh[4]) : "v"(wa0[0]) : "memory");
    W0[4] = cat(wl[4], wh[4]);
    asm volatile("s_addk_i32 m0, 0x1000" ::: "scc", "memory");
    asm volatile("v_mfma_f32_16x16x32_bf16 %0, %1, %2, %0" : "+v"(acc[3][2]) : "v"(W1[3]), "v"(X1[2]));
    asm volatile("ds_read_b64_tr_b16 %0, %1 offset:128" : "=v"(wl[5]) : "v"(wa0[1]) : "memory");
    asm volatile("buffer_load_dwordx4 %0, %1, %2 offen lds" :: "v"(xv[3]), "s"(rX), "s"(kx) : "memory");
    asm volatile("s_add_u32 %0, %0, 128\n\ts_cmp_eq_u32 %0, 1280\n\ts_cselect_b32 %0, 0, %0" : "+s"(kx) :: "scc");
    asm volatile("v_mfma_f32_16x16x32_bf16 %0, %1, %2, %0" : "+v"(acc[3][3]) : "v"(W1[3]), "v"(X1[3]));
    asm volatile("ds_read_b64_tr_b16 %0, %1 offset:1664" : "=v"(wh[5]) : "v"(wa0[1]) : "memory");
    W0[5] = cat(wl[5], wh[5]);
    asm volatile("v_mfma_f32_16x16x32_bf16 %0, %1, %2, %0" : "+v"(acc[4][0]) : "v"(W1[4]), "v"(X1[0]));
    asm volatile("v_mfma_f32_16x16x32_bf16 %0, %1, %2, %0" : "+v"(acc[4][1]) : "v"(W1[4]), "v"(X1[1]));
    asm volatile("v_mfma_f32_16x16x32_bf16 %0, %1, %2, %0" : "+v"(acc[4][2]) : "v"(W1[4]), "v"(X1[2]));
    asm volatile("s_add_u32 m0, %0, 86016" :: "s"(lw) : "scc", "memory");
    asm volatile("v_mfma_f32_16x16x32_bf16 %0, %1, %2, %0" : "+v"(acc[4][3]) : "v"(W1[4]), "v"(X1[3]));
    asm volatile("buffer_load_dwordx4 %0, %1, %2 offen lds" :: "v"(wv[0]), "s"(rW), "s"(kw) : "memory");
    asm volatile("v_mfma_f32_16x16x32_bf16 %0, %1, %2, %0" : "+v"(acc[5][0]) : "v"(W1[5]), "v"(X1[0]));
    asm volatile("v_mfma_f32_16x16x32_bf16 %0, %1, %2, %0" : "+v"(acc[5][1]) : "v"(W1[5]), "v"(X1[1]));
    asm volatile("v_mfma_f32_16x16x32_bf16 %0, %1, %2, %0" : "+v"(acc[5][2]) : "v"(W1[5]), "v"(X1[2]));
    asm volatile("s_addk_i32 m0, 0x1000" ::: "scc", "memory");
    asm volatile("v_mfma_f32_16x16x32_bf16 %0, %1, %2, %0" : "+v"(acc[5][3]) : "v"(W1[5]), "v"(X1[3]));
    asm volatile("buffer_load_dwordx4 %0, %1, %2 offen lds" :: "v"(wv[1]), "s"(rW), "s"(kw) : "memory");
    asm volatile("s_waitcnt lgkmcnt(0)" ::: "memory");
    asm volatile("v_mfma_f32_16x16x32_bf16 %0, %1, %2, %0" : "+v"(acc[0][0]) : "v"(W0[0]), "v"(X0[0]));
    asm volatile("ds_read_b128 %0, %1 offset:0" : "=v"(X1[0]) : "v"(xa1) : "memory");
    asm volatile("v_mfma_f32_16x16x32_bf16 %0, %1, %2, %0" : "+v"(acc[0][1]) : "v"(W0[0]), "v"(X0[1]));
    asm volatile("ds_read_b128 %0, %1 offset:2048" : "=v"(X1[1]) : "v"(xa1) : "memory");
    asm volatile("v_mfma_f32_16x16x32_bf16 %0, %1, %2, %0" : "+v"(acc[0][2]) : "v"(W0[0]), "v"(X0[2]));
    asm volatile("ds_read_b128 %0, %1 offset:4096" : "=v"(X1[2]) : "v"(xa1) : "memory");
    asm volatile("v_mfma_f32_16x16x32_bf16 %0, %1, %2, %0" : "+v"(acc[0][3]) : "v"(W0[0]), "v"(X0[3]));
    asm volatile("ds_read_b128 %0, %1 offset:6144" : "=v"(X1[3]) : "v"(xa1) : "memory");
    asm volatile("s_addk_i32 m0, 0x1000" ::: "scc", "memory");
    asm volatile("v_mfma_f32_16x16x32_bf16 %0, %1, %2, %0" : "+v"(acc[1][0]) : "v"(W0[1]), "v"(X0[0]));
    asm volatile("ds_read_b64_tr_b16 %0, %1 offset:0" : "=v"(wl[0]) : "v"(wa1[0]) : "memory");
    asm volatile("buffer_load_dwordx4 %0, %1, %2 offen lds" :: "v"(wv[2]), "s"(rW), "s"(kw) : "memory");
    asm volatile("v_mfma_f32_16x16x32_bf16 %0, %1, %2, %0" : "+v"(acc[1][1]) : "v"(W0[1]), "v"(X0[1]));
    asm volatile("ds_read_b64_tr_b16 %0, %1 offset:1536" : "=v"(wh[0]) : "v"(wa1[0]) : "memory");
    W1[0] = cat(wl[0], wh[0]);
    asm volatile("v_mfma_f32_16x16x32_bf16 %0, %1, %2, %0" : "+v"(acc[1][2]) : "v"(W0[1]), "v"(X0[2]));
    asm volatile("ds_read_b64_tr_b16 %0, %1 offset:0" : "=v"(wl[1]) : "v"(wa1[1]) : "memory");
    asm volatile("v_mfma_f32_16x16x32_bf16 %0, %1, %2, %0" : "+v"(acc[1][3]) : "v"(W0[1]), "v"(X0[3]));
    asm volatile("ds_read_b64_tr_b16 %0, %1 offset:1536" : "=v"(wh[1]) : "v"(wa1[1]) : "memory");
    W1[1] = cat(wl[1], wh[1]);
    asm volatile("s_addk_i32 m0, 0x1000" ::: "scc", "memory");
    asm volatile("v_mfma_f32_16x16x32_bf16 %0, %1, %2, %0" : "+v"(acc[2][0]) : "v"(W0[2]), "v"(X0[0]));
    asm volatile("ds_read_b64_tr_b16 %0, %1 offset:0" : "=v"(wl[2]) : "v"(wa1[2]) : "memory");
    asm volatile("buffer_load_dwordx4 %0, %1, %2 offen lds" :: "v"(wv[3]), "s"(rW), "s"(kw) : "memory");
    asm volatile("v_mfma_f32_16x16x32_bf16 %0, %1, %2, %0" : "+v"(acc[2][1]) : "v"(W0[2]), "v"(X0[1]));
    asm volatile("ds_read_b64_tr_b16 %0, %1 offset:1536" : "=v"(wh[2]) : "v"(wa1[2]) : "memory");
    W1[2] = cat(wl[2], wh[2]);
    asm volatile("v_mfma_f32_16x16x32_bf16 %0, %1, %2, %0" : "+v"(acc[2][2]) : "v"(W0[2]), "v"(X0[2]));
    asm volatile("ds_read_b64_tr_b16 %0, %1 offset:0" : "=v"(wl[3]) : "v"(wa1[3]) : "memory");
    asm volatile("v_mfma_f32_16x16x32_bf16 %0, %1, %2, %0" : "+v"(acc[2][3]) : "v"(W0[2]), "v"(X0[3]));
    asm volatile("ds_read_b64_tr_b16 %0, %1 offset:1536" : "=v"(wh[3]) : "v"(wa1[3]) : "memory");
    W1[3] = cat(wl[3], wh[3]);
    asm volatile("v_mfma_f32_16x16x32_bf16 %0, %1, %2, %0" : "+v"(acc[3][0]) : "v"(W0[3]), "v"(X0[0]));
    asm volatile("ds_read_b64_tr_b16 %0, %1 offset:128" : "=v"(wl[4]) : "v"(wa1[0]) : "memory");
    asm volatile("s_addk_i32 m0, 0x1000" ::: "scc", "memory");
    asm volatile("v_mfma_f32_16x16x32_bf16 %0, %1, %2, %0" : "+v"(acc[3][1]) : "v"(W0[3]), "v"(X0[1]));
    asm volatile("ds_read_b64_tr_b16 %0, %1 offset:1664" : "=v"(wh[4]) : "v"(wa1[0]) : "memory");
    W1[4] = cat(wl[4], wh[4]);
    asm volatile("buffer_load_dwordx4 %0, %1, %2 offen lds" :: "v"(wv[4]), "s"(rW), "s"(kw) : "memory");
    asm volatile("v_mfma_f32_16x16x32_bf16 %0, %1, %2, %0" : "+v"(acc[3][2]) : "v"(W0[3]), "v"(X0[2]));
    asm volatile("ds_read_b64_tr_b16 %0, %1 offset:128" : "=v"(wl[5]) : "v"(wa1[1]) : "memory");
    asm volatile("v_mfma_f32_16x16x32_bf16 %0, %1, %2, %0" : "+v"(acc[3][3]) : "v"(W0[3]), "v"(X0[3]));
    asm volatile("ds_read_b64_tr_b16 %0, %1 offset:1664" : "=v"(wh[5]) : "v"(wa1[1]) : "memory");
    W1[5] = cat(wl[5], wh[5]);
    asm volatile("v_mfma_f32_16x16x32_bf16 %0, %1, %2, %0" : "+v"(acc[4][0]) : "v"(W0[4]), "v"(X0[0]));
    asm volatile("s_addk_i32 m0, 0x1000" ::: "scc", "memory");
    asm volatile("v_mfma_f32_16x16x32_bf16 %0, %1, %2, %0" : "+v"(acc[4][1]) : "v"(W0[4]), "v"(X0[1]));
    asm volatile("buffer_load_dwordx4 %0, %1, %2 offen lds" :: "v"(wv[5]), "s"(rW), "s"(kw) : "memory");
    asm volatile("s_add_u32 %0, %0, 196608\n\ts_cmp_eq_u32 %0, 1966080\n\ts_cselect_b32 %0, 0, %0" : "+s"(kw) :: "scc");
    asm volatile("v_mfma_f32_16x16x32_bf16 %0, %1, %2, %0" : "+v"(acc[4][2]) : "v"(W0[4]), "v"(X0[2]));
    asm volatile("v_mfma_f32_16x16x32_bf16 %0, %1, %2, %0" : "+v"(acc[4][3]) : "v"(W0[4]), "v"(X0[3]));
    asm volatile("v_mfma_f32_16x16x32_bf16 %0, %1, %2, %0" : "+v"(acc[5][0]) : "v"(W0[5]), "v"(X0[0]));
    asm volatile("v_mfma_f32_16x16x32_bf16 %0, %1, %2, %0" : "+v"(acc[5][1]) : "v"(W0[5]), "v"(X0[1]));
    asm volatile("v_mfma_f32_16x16x32_bf16 %0, %1, %2, %0" : "+v"(acc[5][2]) : "v"(W0[5]), "v"(X0[2]));
    asm volatile("v_mfma_f32_16x16x32_bf16 %0, %1, %2, %0" : "+v"(acc[5][3]) : "v"(W0[5]), "v"(X0[3]));
    asm volatile("s_waitcnt vmcnt(10) lgkmcnt(0)\n\ts_barrier" ::: "memory");
    asm volatile("v_mfma_f32_16x16x32_bf16 %0, %1, %2, %0" : "+v"(acc[0][0]) : "v"(W1[0]), "v"(X1[0]));
    asm volatile("ds_read_b128 %0, %1 offset:32768" : "=v"(X0[0]) : "v"(xa0) : "memory");
    asm volatile("s_add_u32 m0, %0, 12288" :: "s"(lw) : "scc", "memory");
    asm volatile("v_mfma_f32_16x16x32_bf16 %0, %1, %2, %0" : "+v"(acc[0][1]) : "v"(W1[0]), "v"(X1[1]));
    asm volatile("ds_read_b128 %0, %1 offset:34816" : "=v"(X0[1]) : "v"(xa0) : "memory");
    asm volatile("buffer_load_dwordx4 %0, %1, %2 offen lds" :: "v"(xv[0]), "s"(rX), "s"(kx) : "memory");
    asm volatile("v_mfma_f32_16x16x32_bf16 %0, %1, %2, %0" : "+v"(acc[0][2]) : "v"(W1[0]), "v"(X1[2]));
    asm volatile("ds_read_b128 %0, %1 offset:36864" : "=v"(X0[2]) : "v"(xa0) : "memory");
    asm volatile("v_mfma_f32_16x16x32_bf16 %0, %1, %2, %0" : "+v"(acc[0][3]) : "v"(W1[0]), "v"(X1[3]));
    asm volatile("ds_read_b128 %0, %1 offset:38912" : "=v"(X0[3]) : "v"(xa0) : "memory");
    asm volatile("v_mfma_f32_16x16x32_bf16 %0, %1, %2, %0" : "+v"(acc[1][0]) : "v"(W1[1]), "v"(X1[0]));
    asm volatile("ds_read_b64_tr_b16 %0, %1 offset:49152" : "=v"(wl[0]) : "v"(wa0[0]) : "memory");
    asm volatile("s_addk_i32 m0, 0x1000" ::: "scc", "memory");
    asm volatile("v_mfma_f32_16x16x32_bf16 %0, %1, %2, %0" : "+v"(acc[1][1]) : "v"(W1[1]), "v"(X1[1]));
    asm volatile("ds_read_b64_tr_b16 %0, %1 offset:50688" : "=v"(wh[0]) : "v"(wa0[0]) : "memory");
    W0[0] = cat(wl[0], wh[0]);
    asm volatile("buffer_load_dwordx4 %0, %1, %2 offen lds" :: "v"(xv[1]), "s"(rX), "s"(kx) : "memory");
    asm volatile("v_mfma_f32_16x16x32_bf16 %0, %1, %2, %0" : "+v"(acc[1][2]) : "v"(W1[1]), "v"(X1[2]));
    asm volatile("ds_read_b64_tr_b16 %0, %1 offset:49152" : "=v"(wl[1]) : "v"(wa0[1]) : "memory");
    asm volatile("v_mfma_f32_16x16x32_bf16 %0, %1, %2, %0" : "+v"(acc[1][3]) : "v"(W1[1]), "v"(X1[3]));
    asm volatile("ds_read_b64_tr_b16 %0, %1 offset:50688" : "=v"(wh[1]) : "v"(wa0[1]) : "memory");
    W0[1] = cat(wl[1], wh[1]);
    asm volatile("v_mfma_f32_16x16x32_bf16 %0, %1, %2, %0" : "+v"(acc[2][0]) : "v"(W1[2]), "v"(X1[0]));
    asm volatile("ds_read_b64_tr_b16 %0, %1 offset:49152" : "=v"(wl[2]) : "v"(wa0[2]) : "memory");
    asm volatile("v_mfma_f32_16x16x32_bf16 %0, %1, %2, %0" : "+v"(acc[2][1]) : "v"(W1[2]), "v"(X1[1]));
    asm volatile("ds_read_b64_tr_b16 %0, %1 offset:50688" : "=v"(wh[2]) : "v"(wa0[2]) : "memory");
    W0[2] = cat(wl[2], wh[2]);
    asm volatile("s_addk_i32 m0, 0x1000" ::: "scc", "memory");
    asm volatile("v_mfma_f32_16x16x32_bf16 %0, %1, %2, %0" : "+v"(acc[2][2]) : "v"(W1[2]), "v"(X1[2]));
    asm volatile("ds_read_b64_tr_b16 %0, %1 offset:49152" : "=v"(wl[3]) : "v"(wa0[3]) : "memory");
    asm volatile("buffer_load_dwordx4 %0, %1, %2 offen lds" :: "v"(xv[2]), "s"(rX), "s"(kx) : "memory");
    asm volatile("v_mfma_f32_16x16x32_bf16 %0, %1, %2, %0" : "+v"(acc[2][3]) : "v"(W1[2]), "v"(X1[3]));
    asm volatile("ds_read_b64_tr_b16 %0, %1 offset:50688" : "=v"(wh[3]) : "v"(wa0[3]) : "memory");
    W0[3] = cat(wl[3], wh[3]);
    asm volatile("v_mfma_f32_16x16x32_bf16 %0, %1, %2, %0" : "+v"(acc[3][0]) : "v"(W1[3]), "v"(X1[0]));
    asm volatile("ds_read_b64_tr_b16 %0, %1 offset:49280" : "=v"(wl[4]) : "v"(wa0[0]) : "memory");
    asm volatile("v_mfma_f32_16x16x32_bf16 %0, %1, %2, %0" : "+v"(acc[3][1]) : "v"(W1[3]), "v"(X1[1]));
    asm volatile("ds_read_b64_tr_b16 %0, %1 offset:50816" : "=v"(wh[4]) : "v"(wa0[0]) : "memory");
    W0[4] = cat(wl[4], wh[4]);
    asm volatile("s_addk_i32 m0, 0x1000" ::: "scc", "memory");
    asm volatile("v_mfma_f32_16x16x32_bf16 %0, %1, %2, %0" : "+v"(acc[3][2]) : "v"(W1[3]), "v"(X1[2]));
    asm volatile("ds_read_b64_tr_b16 %0, %1 offset:49280" : "=v"(wl[5]) : "v"(wa0[1]) : "memory");
    asm volatile("buffer_load_dwordx4 %0, %1, %2 offen lds" :: "v"(xv[3]), "s"(rX), "s"(kx) : "memory");
    asm volatile("s_add_u32 %0, %0, 128\n\ts_cmp_eq_u32 %0, 1280\n\ts_cselect_b32 %0, 0, %0" : "+s"(kx) :: "scc");
    asm volatile("v_mfma_f32_16x16x32_bf16 %0, %1, %2, %0" : "+v"(acc[3][3]) : "v"(W1[3]), "v"(X1[3]));
    asm volatile("ds_read_b64_tr_b16 %0, %1 offset:50816" : "=v"(wh[5]) : "v"(wa0[1]) : "memory");
    W0[5] = cat(wl[5], wh[5]);
    asm volatile("v_mfma_f32_16x16x32_bf16 %0, %1, %2, %0" : "+v"(acc[4][0]) : "v"(W1[4]), "v"(X1[0]));
    asm volatile("v_mfma_f32_16x16x32_bf16 %0, %1, %2, %0" : "+v"(acc[4][1]) : "v"(W1[4]), "v"(X1[1]));
    asm volatile("v_mfma_f32_16x16x32_bf16 %0, %1, %2, %0" : "+v"(acc[4][2]) : "v"(W1[4]), "v"(X1[2]));
    asm volatile("s_add_u32 m0, %0, 61440" :: "s"(lw) : "scc", "memory");
    asm volatile("v_mfma_f32_16x16x32_bf16 %0, %1, %2, %0" : "+v"(acc[4][3]) : "v"(W1[4]), "v"(X1[3]));
    asm volatile("buffer_load_dwordx4 %0, %1, %2 offen lds" :: "v"(wv[0]), "s"(rW), "s"(kw) : "memory");
    asm volatile("v_mfma_f32_16x16x32_bf16 %0, %1, %2, %0" : "+v"(acc[5][0]) : "v"(W1[5]), "v"(X1[0]));
    asm volatile("v_mfma_f32_16x16x32_bf16 %0, %1, %2, %0" : "+v"(acc[5][1]) : "v"(W1[5]), "v"(X1[1]));
    asm volatile("v_mfma_f32_16x16x32_bf16 %0, %1, %2, %0" : "+v"(acc[5][2]) : "v"(W1[5]), "v"(X1[2]));
    asm volatile("s_addk_i32 m0, 0x1000" ::: "scc", "memory");
    asm volatile("v_mfma_f32_16x16x32_bf16 %0, %1, %2, %0" : "+v"(acc[5][3]) : "v"(W1[5]), "v"(X1[3]));
    asm volatile("buffer_load_dwordx4 %0, %1, %2 offen lds" :: "v"(wv[1]), "s"(rW), "s"(kw) : "memory");
    asm volatile("s_waitcnt lgkmcnt(0)" ::: "memory");
    asm volatile("v_mfma_f32_16x16x32_bf16 %0, %1, %2, %0" : "+v"(acc[0][0]) : "v"(W0[0]), "v"(X0[0]));
    asm volatile("ds_read_b128 %0, %1 offset:32768" : "=v"(X1[0]) : "v"(xa1) : "memory");
    asm volatile("v_mfma_f32_16x16x32_bf16 %0, %1, %2, %0" : "+v"(acc[0][1]) : "v"(W0[0]), "v"(X0[1]));
    asm volatile("ds_read_b128 %0, %1 offset:34816" : "=v"(X1[1]) : "v"(xa1) : "memory");
    asm volatile("v_mfma_f32_16x16x32_bf16 %0, %1, %2, %0" : "+v"(acc[0][2]) : "v"(W0[0]), "v"(X0[2]));
    asm volatile("ds_read_b128 %0, %1 offset:36864" : "=v"(X1[2]) : "v"(xa1) : "memory");
    asm volatile("v_mfma_f32_16x16x32_bf16 %0, %1, %2, %0" : "+v"(acc[0][3]) : "v"(W0[0]), "v"(X0[3]));
    asm volatile("ds_read_b128 %0, %1 offset:38912" : "=v"(X1[3]) : "v"(xa1) : "memory");
    asm volatile("s_addk_i32 m0, 0x1000" ::: "scc", "memory");
    asm volatile("v_mfma_f32_16x16x32_bf16 %0, %1, %2, %0" : "+v"(acc[1][0]) : "v"(W0[1]), "v"(X0[0]));
    asm volatile("ds_read_b64_tr_b16 %0, %1 offset:49152" : "=v"(wl[0]) : "v"(wa1[0]) : "memory");
    asm volatile("buffer_load_dwordx4 %0, %1, %2 offen lds" :: "v"(wv[2]), "s"(rW), "s"(kw) : "memory");
    asm volatile("v_mfma_f32_16x16x32_bf16 %0, %1, %2, %0" : "+v"(acc[1][1]) : "v"(W0[1]), "v"(X0[1]));
    asm volatile("ds_read_b64_tr_b16 %0, %1 offset:50688" : "=v"(wh[0]) : "v"(wa1[0]) : "memory");
    W1[0] = cat(wl[0], wh[0]);
    asm volatile("v_mfma_f32_16x16x32_bf16 %0, %1, %2, %0" : "+v"(acc[1][2]) : "v"(W0[1]), "v"(X0[2]));
    asm volatile("ds_read_b64_tr_b16 %0, %1 offset:49152" : "=v"(wl[1]) : "v"(wa1[1]) : "memory");
    asm volatile("v_mfma_f32_16x16x32_bf16 %0, %1, %2, %0" : "+v"(acc[1][3]) : "v"(W0[1]), "v"(X0[3]));
    asm volatile("ds_read_b64_tr_b16 %0, %1 offset:50688" : "=v"(wh[1]) : "v"(wa1[1]) : "memory");
    W1[1] = cat(wl[1], wh[1]);
    asm volatile("s_addk_i32 m0, 0x1000" ::: "scc", "memory");
    asm volatile("v_mfma_f32_16x16x32_bf16 %0, %1, %2, %0" : "+v"(acc[2][0]) : "v"(W0[2]), "v"(X0[0]));
    asm volatile("ds_read_b64_tr_b16 %0, %1 offset:49152" : "=v"(wl[2]) : "v"(wa1[2]) : "memory");
    asm volatile("buffer_load_dwordx4 %0, %1, %2 offen lds" :: "v"(wv[3]), "s"(rW), "s"(kw) : "memory");
    asm volatile("v_mfma_f32_16x16x32_bf16 %0, %1, %2, %0" : "+v"(acc[2][1]) : "v"(W0[2]), "v"(X0[1]));
    asm volatile("ds_read_b64_tr_b16 %0, %1 offset:50688" : "=v"(wh[2]) : "v"(wa1[2]) : "memory");
    W1[2] = cat(wl[2], wh[2]);
    asm volatile("v_mfma_f32_16x16x32_bf16 %0, %1, %2, %0" : "+v"(acc[2][2]) : "v"(W0[2]), "v"(X0[2]));
    asm volatile("ds_read_b64_tr_b16 %0, %1 offset:49152" : "=v"(wl[3]) : "v"(wa1[3]) : "memory");
    asm volatile("v_mfma_f32_16x16x32_bf16 %0, %1, %2, %0" : "+v"(acc[2][3]) : "v"(W0[2]), "v"(X0[3]));
    asm volatile("ds_read_b64_tr_b16 %0, %1 offset:50688" : "=v"(wh[3]) : "v"(wa1[3]) : "memory");
    W1[3] = cat(wl[3], wh[3]);
    asm volatile("v_mfma_f32_16x16x32_bf16 %0, %1, %2, %0" : "+v"(acc[3][0]) : "v"(W0[3]), "v"(X0[0]));
    asm volatile("ds_read_b64_tr_b16 %0, %1 offset:49280" : "=v"(wl[4]) : "v"(wa1[0]) : "memory");
    asm volatile("s_addk_i32 m0, 0x1000" ::: "scc", "memory");
    asm volatile("v_mfma_f32_16x16x32_bf16 %0, %1, %2, %0" : "+v"(acc[3][1]) : "v"(W0[3]), "v"(X0[1]));
    asm volatile("ds_read_b64_tr_b16 %0, %1 offset:50816" : "=v"(wh[4]) : "v"(wa1[0]) : "memory");
    W1[4] = cat(wl[4], wh[4]);
    asm volatile("buffer_load_dwordx4 %0, %1, %2 offen lds" :: "v"(wv[4]), "s"(rW), "s"(kw) : "memory");
    asm volatile("v_mfma_f32_16x16x32_bf16 %0, %1, %2, %0" : "+v"(acc[3][2]) : "v"(W0[3]), "v"(X0[2]));
    asm volatile("ds_read_b64_tr_b16 %0, %1 offset:49280" : "=v"(wl[5]) : "v"(wa1[1]) : "memory");
    asm volatile("v_mfma_f32_16x16x32_bf16 %0, %1, %2, %0" : "+v"(acc[3][3]) : "v"(W0[3]), "v"(X0[3]));
    asm volatile("ds_read_b64_tr_b16 %0, %1 offset:50816" : "=v"(wh[5]) : "v"(wa1[1]) : "memory");
    W1[5] = cat(wl[5], wh[5]);
    asm volatile("v_mfma_f32_16x16x32_bf16 %0, %1, %2, %0" : "+v"(acc[4][0]) : "v"(W0[4]), "v"(X0[0]));
    asm volatile("s_addk_i32 m0, 0x1000" ::: "scc", "memory");
    asm volatile("v_mfma_f32_16x16x32_bf16 %0, %1, %2, %0" : "+v"(acc[4][1]) : "v"(W0[4]), "v"(X0[1]));
    asm volatile("buffer_load_dwordx4 %0, %1, %2 offen lds" :: "v"(wv[5]), "s"(rW), "s"(kw) : "memory");
    asm volatile("s_add_u32 %0, %0, 196608\n\ts_cmp_eq_u32 %0, 1966080\n\ts_cselect_b32 %0, 0, %0" : "+s"(kw) :: "scc");
    asm volatile("v_mfma_f32_16x16x32_bf16 %0, %1, %2, %0" : "+v"(acc[4][2]) : "v"(W0[4]), "v"(X0[2]));
    asm volatile("v_mfma_f32_16x16x32_bf16 %0, %1, %2, %0" : "+v"(acc[4][3]) : "v"(W0[4]), "v"(X0[3]));
    asm volatile("v_mfma_f32_16x16x32_bf16 %0, %1, %2, %0" : "+v"(acc[5][0]) : "v"(W0[5]), "v"(X0[0]));
    asm volatile("v_mfma_f32_16x16x32_bf16 %0, %1, %2, %0" : "+v"(acc[5][1]) : "v"(W0[5]), "v"(X0[1]));
    asm volatile("v_mfma_f32_16x16x32_bf16 %0, %1, %2, %0" : "+v"(acc[5][2]) : "v"(W0[5]), "v"(X0[2]));
    asm volatile("v_mfma_f32_16x16x32_bf16 %0, %1, %2, %0" : "+v"(acc[5][3]) : "v"(W0[5]), "v"(X0[3]));
    asm volatile("s_waitcnt vmcnt(10) lgkmcnt(0)\n\ts_barrier" ::: "memory");
    asm volatile("v_mfma_f32_16x16x32_bf16 %0, %1, %2, %0" : "+v"(acc[0][0]) : "v"(W1[0]), "v"(X1[0]));
    asm volatile("ds_read_b128 %0, %1 offset:16384" : "=v"(X0[0]) : "v"(xa0) : "memory");
    asm volatile("v_mfma_f32_16x16x32_bf16 %0, %1, %2, %0" : "+v"(acc[0][1]) : "v"(W1[0]), "v"(X1[1]));
    asm volatile("ds_read_b128 %0, %1 offset:18432" : "=v"(X0[1]) : "v"(xa0) : "memory");
    asm volatile("v_mfma_f32_16x16x32_bf16 %0, %1, %2, %0" : "+v"(acc[0][2]) : "v"(W1[0]), "v"(X1[2]));
    asm volatile("ds_read_b128 %0, %1 offset:20480" : "=v"(X0[2]) : "v"(xa0) : "memory");
    asm volatile("v_mfma_f32_16x16x32_bf16 %0, %1, %2, %0" : "+v"(acc[0][3]) : "v"(W1[0]), "v"(X1[3]));
    asm volatile("ds_read_b128 %0, %1 offset:22528" : "=v"(X0[3]) : "v"(xa0) : "memory");
    asm volatile("v_mfma_f32_16x16x32_bf16 %0, %1, %2, %0" : "+v"(acc[1][0]) : "v"(W1[1]), "v"(X1[0]));
    asm volatile("ds_read_b64_tr_b16 %0, %1 offset:24576" : "=v"(wl[0]) : "v"(wa0[0]) : "memory");
    asm volatile("v_mfma_f32_16x16x32_bf16 %0, %1, %2, %0" : "+v"(acc[1][1]) : "v"(W1[1]), "v"(X1[1]));
    asm volatile("ds_read_b64_tr_b16 %0, %1 offset:26112" : "=v"(wh[0]) : "v"(wa0[0]) : "memory");
    W0[0] = cat(wl[0], wh[0]);
    asm volatile("v_mfma_f32_16x16x32_bf16 %0, %1, %2, %0" : "+v"(acc[1][2]) : "v"(W1[1]), "v"(X1[2]));
    asm volatile("ds_read_b64_tr_b16 %0, %1 offset:24576" : "=v"(wl[1]) : "v"(wa0[1]) : "memory");
    asm volatile("v_mfma_f32_16x16x32_bf16 %0, %1, %2, %0" : "+v"(acc[1][3]) : "v"(W1[1]), "v"(X1[3]));
    asm volatile("ds_read_b64_tr_b16 %0, %1 offset:26112" : "=v"(wh[1]) : "v"(wa0[1]) : "memory");
    W0[1] = cat(wl[1], wh[1]);
    asm volatile("v_mfma_f32_16x16x32_bf16 %0, %1, %2, %0" : "+v"(acc[2][0]) : "v"(W1[2]), "v"(X1[0]));
    asm volatile("ds_read_b64_tr_b16 %0, %1 offset:24576" : "=v"(wl[2]) : "v"(wa0[2]) : "memory");
    asm volatile("v_mfma_f32_16x16x32_bf16 %0, %1, %2, %0" : "+v"(acc[2][1]) : "v"(W1[2]), "v"(X1[1]));
    asm volatile("ds_read_b64_tr_b16 %0, %1 offset:26112" : "=v"(wh[2]) : "v"(wa0[2]) : "memory");
    W0[2] = cat(wl[2], wh[2]);
    asm volatile("v_mfma_f32_16x16x32_bf16 %0, %1, %2, %0" : "+v"(acc[2][2]) : "v"(W1[2]), "v"(X1[2]));
    asm volatile("ds_read_b64_tr_b16 %0, %1 offset:24576" : "=v"(wl[3]) : "v"(wa0[3]) : "memory");
    asm volatile("buffer_load_dword %0, %1, %2, 0 offen sc1" : "=v"(fl) : "v"(3932160 + tid*64), "s"(rS) : "memory");
    asm volatile("v_mfma_f32_16x16x32_bf16 %0, %1, %2, %0" : "+v"(acc[2][3]) : "v"(W1[2]), "v"(X1[3]));
    asm volatile("ds_read_b64_tr_b16 %0, %1 offset:26112" : "=v"(wh[3]) : "v"(wa0[3]) : "memory");
    W0[3] = cat(wl[3], wh[3]);
    asm volatile("v_mfma_f32_16x16x32_bf16 %0, %1, %2, %0" : "+v"(acc[3][0]) : "v"(W1[3]), "v"(X1[0]));
    asm volatile("ds_read_b64_tr_b16 %0, %1 offset:24704" : "=v"(wl[4]) : "v"(wa0[0]) : "memory");
    asm volatile("v_mfma_f32_16x16x32_bf16 %0, %1, %2, %0" : "+v"(acc[3][1]) : "v"(W1[3]), "v"(X1[1]));
    asm volatile("ds_read_b64_tr_b16 %0, %1 offset:26240" : "=v"(wh[4]) : "v"(wa0[0]) : "memory");
    W0[4] = cat(wl[4], wh[4]);
    asm volatile("v_mfma_f32_16x16x32_bf16 %0, %1, %2, %0" : "+v"(acc[3][2]) : "v"(W1[3]), "v"(X1[2]));
    asm volatile("ds_read_b64_tr_b16 %0, %1 offset:24704" : "=v"(wl[5]) : "v"(wa0[1]) : "memory");
    asm volatile("v_mfma_f32_16x16x32_bf16 %0, %1, %2, %0" : "+v"(acc[3][3]) : "v"(W1[3]), "v"(X1[3]));
    asm volatile("ds_read_b64_tr_b16 %0, %1 offset:26240" : "=v"(wh[5]) : "v"(wa0[1]) : "memory");
    W0[5] = cat(wl[5], wh[5]);
    asm volatile("v_mfma_f32_16x16x32_bf16 %0, %1, %2, %0" : "+v"(acc[4][0]) : "v"(W1[4]), "v"(X1[0]));
    asm volatile("v_mfma_f32_16x16x32_bf16 %0, %1, %2, %0" : "+v"(acc[4][1]) : "v"(W1[4]), "v"(X1[1]));
    asm volatile("v_mfma_f32_16x16x32_bf16 %0, %1, %2, %0" : "+v"(acc[4][2]) : "v"(W1[4]), "v"(X1[2]));
    asm volatile("v_mfma_f32_16x16x32_bf16 %0, %1, %2, %0" : "+v"(acc[4][3]) : "v"(W1[4]), "v"(X1[3]));
    asm volatile("v_mfma_f32_16x16x32_bf16 %0, %1, %2, %0" : "+v"(acc[5][0]) : "v"(W1[5]), "v"(X1[0]));
    asm volatile("v_mfma_f32_16x16x32_bf16 %0, %1, %2, %0" : "+v"(acc[5][1]) : "v"(W1[5]), "v"(X1[1]));
    asm volatile("v_mfma_f32_16x16x32_bf16 %0, %1, %2, %0" : "+v"(acc[5][2]) : "v"(W1[5]), "v"(X1[2]));
    asm volatile("v_mfma_f32_16x16x32_bf16 %0, %1, %2, %0" : "+v"(acc[5][3]) : "v"(W1[5]), "v"(X1[3]));
    asm volatile("s_waitcnt lgkmcnt(0)" ::: "memory");
    asm volatile("v_mfma_f32_16x16x32_bf16 %0, %1, %2, %0" : "+v"(acc[0][0]) : "v"(W0[0]), "v"(X0[0]));
    asm volatile("ds_read_b128 %0, %1 offset:16384" : "=v"(X1[0]) : "v"(xa1) : "memory");
    asm volatile("v_mfma_f32_16x16x32_bf16 %0, %1, %2, %0" : "+v"(acc[0][1]) : "v"(W0[0]), "v"(X0[1]));
    asm volatile("ds_read_b128 %0, %1 offset:18432" : "=v"(X1[1]) : "v"(xa1) : "memory");
    asm volatile("v_mfma_f32_16x16x32_bf16 %0, %1, %2, %0" : "+v"(acc[0][2]) : "v"(W0[0]), "v"(X0[2]));
    asm volatile("ds_read_b128 %0, %1 offset:20480" : "=v"(X1[2]) : "v"(xa1) : "memory");
    asm volatile("v_mfma_f32_16x16x32_bf16 %0, %1, %2, %0" : "+v"(acc[0][3]) : "v"(W0[0]), "v"(X0[3]));
    asm volatile("ds_read_b128 %0, %1 offset:22528" : "=v"(X1[3]) : "v"(xa1) : "memory");
    asm volatile("v_mfma_f32_16x16x32_bf16 %0, %1, %2, %0" : "+v"(acc[1][0]) : "v"(W0[1]), "v"(X0[0]));
    asm volatile("ds_read_b64_tr_b16 %0, %1 offset:24576" : "=v"(wl[0]) : "v"(wa1[0]) : "memory");
    asm volatile("v_mfma_f32_16x16x32_bf16 %0, %1, %2, %0" : "+v"(acc[1][1]) : "v"(W0[1]), "v"(X0[1]));
    asm volatile("ds_read_b64_tr_b16 %0, %1 offset:26112" : "=v"(wh[0]) : "v"(wa1[0]) : "memory");
    W1[0] = cat(wl[0], wh[0]);
    asm volatile("v_mfma_f32_16x16x32_bf16 %0, %1, %2, %0" : "+v"(acc[1][2]) : "v"(W0[1]), "v"(X0[2]));
    asm volatile("ds_read_b64_tr_b16 %0, %1 offset:24576" : "=v"(wl[1]) : "v"(wa1[1]) : "memory");
    asm volatile("v_mfma_f32_16x16x32_bf16 %0, %1, %2, %0" : "+v"(acc[1][3]) : "v"(W0[1]), "v"(X0[3]));
    asm volatile("ds_read_b64_tr_b16 %0, %1 offset:26112" : "=v"(wh[1]) : "v"(wa1[1]) : "memory");
    W1[1] = cat(wl[1], wh[1]);
    asm volatile("v_mfma_f32_16x16x32_bf16 %0, %1, %2, %0" : "+v"(acc[2][0]) : "v"(W0[2]), "v"(X0[0]));
    asm volatile("ds_read_b64_tr_b16 %0, %1 offset:24576" : "=v"(wl[2]) : "v"(wa1[2]) : "memory");
    asm volatile("v_mfma_f32_16x16x32_bf16 %0, %1, %2, %0" : "+v"(acc[2][1]) : "v"(W0[2]), "v"(X0[1]));
    asm volatile("ds_read_b64_tr_b16 %0, %1 offset:26112" : "=v"(wh[2]) : "v"(wa1[2]) : "memory");
    W1[2] = cat(wl[2], wh[2]);
    asm volatile("v_mfma_f32_16x16x32_bf16 %0, %1, %2, %0" : "+v"(acc[2][2]) : "v"(W0[2]), "v"(X0[2]));
    asm volatile("ds_read_b64_tr_b16 %0, %1 offset:24576" : "=v"(wl[3]) : "v"(wa1[3]) : "memory");
    asm volatile("v_mfma_f32_16x16x32_bf16 %0, %1, %2, %0" : "+v"(acc[2][3]) : "v"(W0[2]), "v"(X0[3]));
    asm volatile("ds_read_b64_tr_b16 %0, %1 offset:26112" : "=v"(wh[3]) : "v"(wa1[3]) : "memory");
    W1[3] = cat(wl[3], wh[3]);
    asm volatile("v_mfma_f32_16x16x32_bf16 %0, %1, %2, %0" : "+v"(acc[3][0]) : "v"(W0[3]), "v"(X0[0]));
    asm volatile("ds_read_b64_tr_b16 %0, %1 offset:24704" : "=v"(wl[4]) : "v"(wa1[0]) : "memory");
    asm volatile("v_mfma_f32_16x16x32_bf16 %0, %1, %2, %0" : "+v"(acc[3][1]) : "v"(W0[3]), "v"(X0[1]));
    asm volatile("ds_read_b64_tr_b16 %0, %1 offset:26240" : "=v"(wh[4]) : "v"(wa1[0]) : "memory");
    W1[4] = cat(wl[4], wh[4]);
    asm volatile("v_mfma_f32_16x16x32_bf16 %0, %1, %2, %0" : "+v"(acc[3][2]) : "v"(W0[3]), "v"(X0[2]));
    asm volatile("ds_read_b64_tr_b16 %0, %1 offset:24704" : "=v"(wl[5]) : "v"(wa1[1]) : "memory");
    asm volatile("v_mfma_f32_16x16x32_bf16 %0, %1, %2, %0" : "+v"(acc[3][3]) : "v"(W0[3]), "v"(X0[3]));
    asm volatile("ds_read_b64_tr_b16 %0, %1 offset:26240" : "=v"(wh[5]) : "v"(wa1[1]) : "memory");
    W1[5] = cat(wl[5], wh[5]);
    asm volatile("v_mfma_f32_16x16x32_bf16 %0, %1, %2, %0" : "+v"(acc[4][0]) : "v"(W0[4]), "v"(X0[0]));
    asm volatile("v_mfma_f32_16x16x32_bf16 %0, %1, %2, %0" : "+v"(acc[4][1]) : "v"(W0[4]), "v"(X0[1]));
    asm volatile("v_mfma_f32_16x16x32_bf16 %0, %1, %2, %0" : "+v"(acc[4][2]) : "v"(W0[4]), "v"(X0[2]));
    asm volatile("v_mfma_f32_16x16x32_bf16 %0, %1, %2, %0" : "+v"(acc[4][3]) : "v"(W0[4]), "v"(X0[3]));
    asm volatile("v_mfma_f32_16x16x32_bf16 %0, %1, %2, %0" : "+v"(acc[5][0]) : "v"(W0[5]), "v"(X0[0]));
    asm volatile("v_mfma_f32_16x16x32_bf16 %0, %1, %2, %0" : "+v"(acc[5][1]) : "v"(W0[5]), "v"(X0[1]));
    asm volatile("v_mfma_f32_16x16x32_bf16 %0, %1, %2, %0" : "+v"(acc[5][2]) : "v"(W0[5]), "v"(X0[2]));
    asm volatile("v_mfma_f32_16x16x32_bf16 %0, %1, %2, %0" : "+v"(acc[5][3]) : "v"(W0[5]), "v"(X0[3]));
    asm volatile("s_waitcnt vmcnt(0)" ::: "memory");
    asm volatile("" : "+v"(fl));
    if (!__all(fl == tag)) spin1(rS, 3932160 + tid*64, false, tag);
    asm volatile("s_waitcnt lgkmcnt(0)\n\ts_barrier" ::: "memory");
    asm volatile("buffer_load_dwordx2 %0, %1, %2, 0 offen sc1" : "=v"(st) : "v"(40960 + (128*c + (tid & 127))*8), "s"(rS) : "memory");
    asm volatile("buffer_load_dwordx4 %0, %1, %2, 0 offen offset:0 sc1" : "=v"(c4[0]) : "v"(tid < 192 ? 4096000 + (8 + j)*6144 + tid*32 : OOB), "s"(rS) : "memory");
    asm volatile("buffer_load_dwordx4 %0, %1, %2, 0 offen offset:16 sc1" : "=v"(c4[1]) : "v"(tid < 192 ? 4096000 + (8 + j)*6144 + tid*32 : OOB), "s"(rS) : "memory");
    __builtin_amdgcn_sched_barrier(0);
    asm volatile("v_mfma_f32_16x16x32_bf16 %0, %1, %2, %0" : "+v"(acc[0][0]) : "v"(W1[0]), "v"(X1[0]));
    asm volatile("ds_read_b128 %0, %1 offset:0" : "=v"(X0[0]) : "v"(xa0) : "memory");
    sw = swb + (kso + 0 + wm*(1u))*40960;
    asm volatile("" : "+s"(sw));
    asm volatile("s_add_u32 m0, %0, 153600" :: "s"(lw2) : "scc", "memory");
    asm volatile("v_mfma_f32_16x16x32_bf16 %0, %1, %2, %0" : "+v"(acc[0][1]) : "v"(W1[0]), "v"(X1[1]));
    asm volatile("ds_read_b128 %0, %1 offset:2048" : "=v"(X0[1]) : "v"(xa0) : "memory");
    asm volatile("buffer_load_dwordx4 %0, %1, %2 offen offset:0 sc1 lds" :: "v"(vl16), "s"(rS), "s"(sw) : "memory");
    asm volatile("v_mfma_f32_16x16x32_bf16 %0, %1, %2, %0" : "+v"(acc[0][2]) : "v"(W1[0]), "v"(X1[2]));
    asm volatile("ds_read_b128 %0, %1 offset:4096" : "=v"(X0[2]) : "v"(xa0) : "memory");
    asm volatile("v_mfma_f32_16x16x32_bf16 %0, %1, %2, %0" : "+v"(acc[0][3]) : "v"(W1[0]), "v"(X1[3]));
    asm volatile("ds_read_b128 %0, %1 offset:6144" : "=v"(X0[3]) : "v"(xa0) : "memory");
    asm volatile("v_mfma_f32_16x16x32_bf16 %0, %1, %2, %0" : "+v"(acc[1][0]) : "v"(W1[1]), "v"(X1[0]));
    asm volatile("ds_read_b64_tr_b16 %0, %1 offset:0" : "=v"(wl[0]) : "v"(wa0[0]) : "memory");
    asm volatile("buffer_load_dwordx4 %0, %1, %2 offen offset:1024 sc1 lds" :: "v"(vl16), "s"(rS), "s"(sw) : "memory");
    asm volatile("v_mfma_f32_16x16x32_bf16 %0, %1, %2, %0" : "+v"(acc[1][1]) : "v"(W1[1]), "v"(X1[1]));
    asm volatile("ds_read_b64_tr_b16 %0, %1 offset:1536" : "=v"(wh[0]) : "v"(wa0[0]) : "memory");
    W0[0] = cat(wl[0], wh[0]);
    asm volatile("v_mfma_f32_16x16x32_bf16 %0, %1, %2, %0" : "+v"(acc[1][2]) : "v"(W1[1]), "v"(X1[2]));
    asm volatile("ds_read_b64_tr_b16 %0, %1 offset:0" : "=v"(wl[1]) : "v"(wa0[1]) : "memory");
    asm volatile("v_mfma_f32_16x16x32_bf16 %0, %1, %2, %0" : "+v"(acc[1][3]) : "v"(W1[1]), "v"(X1[3]));
    asm volatile("ds_read_b64_tr_b16 %0, %1 offset:1536" : "=v"(wh[1]) : "v"(wa0[1]) : "memory");
    W0[1] = cat(wl[1], wh[1]);
    asm volatile("buffer_load_dwordx4 %0, %1, %2 offen offset:2048 sc1 lds" :: "v"(vl16), "s"(rS), "s"(sw) : "memory");
    asm volatile("v_mfma_f32_16x16x32_bf16 %0, %1, %2, %0" : "+v"(acc[2][0]) : "v"(W1[2]), "v"(X1[0]));
    asm volatile("ds_read_b64_tr_b16 %0, %1 offset:0" : "=v"(wl[2]) : "v"(wa0[2]) : "memory");
    asm volatile("v_mfma_f32_16x16x32_bf16 %0, %1, %2, %0" : "+v"(acc[2][1]) : "v"(W1[2]), "v"(X1[1]));
    asm volatile("ds_read_b64_tr_b16 %0, %1 offset:1536" : "=v"(wh[2]) : "v"(wa0[2]) : "memory");
    W0[2] = cat(wl[2], wh[2]);
    asm volatile("v_mfma_f32_16x16x32_bf16 %0, %1, %2, %0" : "+v"(acc[2][2]) : "v"(W1[2]), "v"(X1[2]));
    asm volatile("ds_read_b64_tr_b16 %0, %1 offset:0" : "=v"(wl[3]) : "v"(wa0[3]) : "memory");
    asm volatile("v_mfma_f32_16x16x32_bf16 %0, %1, %2, %0" : "+v"(acc[2][3]) : "v"(W1[2]), "v"(X1[3]));
    asm volatile("ds_read_b64_tr_b16 %0, %1 offset:1536" : "=v"(wh[3]) : "v"(wa0[3]) : "memory");
    W0[3] = cat(wl[3], wh[3]);
    asm volatile("buffer_load_dwordx4 %0, %1, %2 offen offset:3072 sc1 lds" :: "v"(vl16), "s"(rS), "s"(sw) : "memory");
    asm volatile("v_mfma_f32_16x16x32_bf16 %0, %1, %2, %0" : "+v"(acc[3][0]) : "v"(W1[3]), "v"(X1[0]));
    asm volatile("ds_read_b64_tr_b16 %0, %1 offset:128" : "=v"(wl[4]) : "v"(wa0[0]) : "memory");
    asm volatile("v_mfma_f32_16x16x32_bf16 %0, %1, %2, %0" : "+v"(acc[3][1]) : "v"(W1[3]), "v"(X1[1]));
    asm volatile("ds_read_b64_tr_b16 %0, %1 offset:1664" : "=v"(wh[4]) : "v"(wa0[0]) : "memory");
    W0[4] = cat(wl[4], wh[4]);
    asm volatile("s_addk_i32 m0, 0x1000" ::: "scc", "memory");
    asm volatile("v_mfma_f32_16x16x32_bf16 %0, %1, %2, %0" : "+v"(acc[3][2]) : "v"(W1[3]), "v"(X1[2]));
    asm volatile("ds_read_b64_tr_b16 %0, %1 offset:128" : "=v"(wl[5]) : "v"(wa0[1]) : "memory");
    asm volatile("buffer_load_dwordx4 %0, %1, %2 offen offset:0 sc1 lds" :: "v"(vl16h), "s"(rS), "s"(sw) : "memory");
    asm volatile("v_mfma_f32_16x16x32_bf16 %0, %1, %2, %0" : "+v"(acc[3][3]) : "v"(W1[3]), "v"(X1[3]));
    asm volatile("ds_read_b64_tr_b16 %0, %1 offset:1664" : "=v"(wh[5]) : "v"(wa0[1]) : "memory");
    W0[5] = cat(wl[5], wh[5]);
    asm volatile("v_mfma_f32_16x16x32_bf16 %0, %1, %2, %0" : "+v"(acc[4][0]) : "v"(W1[4]), "v"(X1[0]));
    asm volatile("v_mfma_f32_16x16x32_bf16 %0, %1, %2, %0" : "+v"(acc[4][1]) : "v"(W1[4]), "v"(X1[1]));
    sw = swb + (kso + 2 + wm*(1u))*40960;
    asm volatile("" : "+s"(sw));
    asm volatile("s_add_u32 m0, %0, 133120" :: "s"(lw2) : "scc", "memory");
    asm volatile("v_mfma_f32_16x16x32_bf16 %0, %1, %2, %0" : "+v"(acc[4][2]) : "v"(W1[4]), "v"(X1[2]));
    asm volatile("buffer_load_dwordx4 %0, %1, %2 offen offset:0 sc1 lds" :: "v"(vl16), "s"(rS), "s"(sw) : "memory");
    asm volatile("v_mfma_f32_16x16x32_bf16 %0, %1, %2, %0" : "+v"(acc[4][3]) : "v"(W1[4]), "v"(X1[3]));
    asm volatile("v_mfma_f32_16x16x32_bf16 %0, %1, %2, %0" : "+v"(acc[5][0]) : "v"(W1[5]), "v"(X1[0]));
    asm volatile("v_mfma_f32_16x16x32_bf16 %0, %1, %2, %0" : "+v"(acc[5][1]) : "v"(W1[5]), "v"(X1[1]));
    asm volatile("buffer_load_dwordx4 %0, %1, %2 offen offset:1024 sc1 lds" :: "v"(vl16), "s"(rS), "s"(sw) : "memory");
    asm volatile("v_mfma_f32_16x16x32_bf16 %0, %1, %2, %0" : "+v"(acc[5][2]) : "v"(W1[5]), "v"(X1[2]));
    asm volatile("v_mfma_f32_16x16x32_bf16 %0, %1, %2, %0" : "+v"(acc[5][3]) : "v"(W1[5]), "v"(X1[3]));
    asm volatile("s_waitcnt lgkmcnt(0)" ::: "memory");
    asm volatile("v_mfma_f32_16x16x32_bf16 %0, %1, %2, %0" : "+v"(acc[0][0]) : "v"(W0[0]), "v"(X0[0]));
    asm volatile("ds_read_b128 %0, %1 offset:0" : "=v"(X1[0]) : "v"(xa1) : "memory");
    asm volatile("v_mfma_f32_16x16x32_bf16 %0, %1, %2, %0" : "+v"(acc[0][1]) : "v"(W0[0]), "v"(X0[1]));
    asm volatile("ds_read_b128 %0, %1 offset:2048" : "=v"(X1[1]) : "v"(xa1) : "memory");
    asm volatile("buffer_load_dwordx4 %0, %1, %2 offen offset:2048 sc1 lds" :: "v"(vl16), "s"(rS), "s"(sw) : "memory");
    asm volatile("v_mfma_f32_16x16x32_bf16 %0, %1, %2, %0" : "+v"(acc[0][2]) : "v"(W0[0]), "v"(X0[2]));
    asm volatile("ds_read_b128 %0, %1 offset:4096" : "=v"(X1[2]) : "v"(xa1) : "memory");
    asm volatile("v_mfma_f32_16x16x32_bf16 %0, %1, %2, %0" : "+v"(acc[0][3]) : "v"(W0[0]), "v"(X0[3]));
    asm volatile("ds_read_b128 %0, %1 offset:6144" : "=v"(X1[3]) : "v"(xa1) : "memory");
    asm volatile("v_mfma_f32_16x16x32_bf16 %0, %1, %2, %0" : "+v"(acc[1][0]) : "v"(W0[1]), "v"(X0[0]));
    asm volatile("ds_read_b64_tr_b16 %0, %1 offset:0" : "=v"(wl[0]) : "v"(wa1[0]) : "memory");
    asm volatile("buffer_load_dwordx4 %0, %1, %2 offen offset:3072 sc1 lds" :: "v"(vl16), "s"(rS), "s"(sw) : "memory");
    asm volatile("v_mfma_f32_16x16x32_bf16 %0, %1, %2, %0" : "+v"(acc[1][1]) : "v"(W0[1]), "v"(X0[1]));
    asm volatile("ds_read_b64_tr_b16 %0, %1 offset:1536" : "=v"(wh[0]) : "v"(wa1[0]) : "memory");
    W1[0] = cat(wl[0], wh[0]);
    asm volatile("v_mfma_f32_16x16x32_bf16 %0, %1, %2, %0" : "+v"(acc[1][2]) : "v"(W0[1]), "v"(X0[2]));
    asm volatile("ds_read_b64_tr_b16 %0, %1 offset:0" : "=v"(wl[1]) : "v"(wa1[1]) : "memory");
    asm volatile("v_mfma_f32_16x16x32_bf16 %0, %1, %2, %0" : "+v"(acc[1][3]) : "v"(W0[1]), "v"(X0[3]));
    asm volatile("ds_read_b64_tr_b16 %0, %1 offset:1536" : "=v"(wh[1]) : "v"(wa1[1]) : "memory");
    W1[1] = cat(wl[1], wh[1]);
    asm volatile("s_addk_i32 m0, 0x1000" ::: "scc", "memory");
    asm volatile("v_mfma_f32_16x16x32_bf16 %0, %1, %2, %0" : "+v"(acc[2][0]) : "v"(W0[2]), "v"(X0[0]));
    asm volatile("ds_read_b64_tr_b16 %0, %1 offset:0" : "=v"(wl[2]) : "v"(wa1[2]) : "memory");
    asm volatile("buffer_load_dwordx4 %0, %1, %2 offen offset:0 sc1 lds" :: "v"(vl16h), "s"(rS), "s"(sw) : "memory");
    asm volatile("v_mfma_f32_16x16x32_bf16 %0, %1, %2, %0" : "+v"(acc[2][1]) : "v"(W0[2]), "v"(X0[1]));
    asm volatile("ds_read_b64_tr_b16 %0, %1 offset:1536" : "=v"(wh[2]) : "v"(wa1[2]) : "memory");
    W1[2] = cat(wl[2], wh[2]);
    asm volatile("v_mfma_f32_16x16x32_bf16 %0, %1, %2, %0" : "+v"(acc[2][2]) : "v"(W0[2]), "v"(X0[2]));
    asm volatile("ds_read_b64_tr_b16 %0, %1 offset:0" : "=v"(wl[3]) : "v"(wa1[3]) : "memory");
    sw = swb + (kso + 4 + wm*(1u))*40960;
    asm volatile("" : "+s"(sw));
    asm volatile("s_add_u32 m0, %0, 112640" :: "s"(lw2) : "scc", "memory");
    asm volatile("v_mfma_f32_16x16x32_bf16 %0, %1, %2, %0" : "+v"(acc[2][3]) : "v"(W0[2]), "v"(X0[3]));
    asm volatile("ds_read_b64_tr_b16 %0, %1 offset:1536" : "=v"(wh[3]) : "v"(wa1[3]) : "memory");
    W1[3] = cat(wl[3], wh[3]);
    asm volatile("buffer_load_dwordx4 %0, %1, %2 offen offset:0 sc1 lds" :: "v"(vl16), "s"(rS), "s"(sw) : "memory");
    asm volatile("v_mfma_f32_16x16x32_bf16 %0, %1, %2, %0" : "+v"(acc[3][0]) : "v"(W0[3]), "v"(X0[0]));
    asm volatile("ds_read_b64_tr_b16 %0, %1 offset:128" : "=v"(wl[4]) : "v"(wa1[0]) : "memory");
    asm volatile("v_mfma_f32_16x16x32_bf16 %0, %1, %2, %0" : "+v"(acc[3][1]) : "v"(W0[3]), "v"(X0[1]));
    asm volatile("ds_read_b64_tr_b16 %0, %1 offset:1664" : "=v"(wh[4]) : "v"(wa1[0]) : "memory");
    W1[4] = cat(wl[4], wh[4]);
    asm volatile("s_waitcnt vmcnt(11)" ::: "memory");
    float la_, lb_, lc, ld, le, lm; const float k_eps = 1e-5f; const unsigned lra = lds0 + (tid & 127)*4;
    asm volatile("v_cvt_pk_bf16_f32 %0, %1, %1" : "=v"(la_) : "v"(st.y));
    asm volatile("v_mfma_f32_16x16x32_bf16 %0, %1, %2, %0" : "+v"(acc[3][2]) : "v"(W0[3]), "v"(X0[2]));
    asm volatile("ds_read_b64_tr_b16 %0, %1 offset:128" : "=v"(wl[5]) : "v"(wa1[1]) : "memory");
    asm volatile("v_lshlrev_b32 %0, 16, %0" : "+v"(la_) : );
    asm volatile("v_add_f32 %0, %1, %0" : "+v"(la_) : "s"(k_eps));
    asm volatile("v_mfma_f32_16x16x32_bf16 %0, %1, %2, %0" : "+v"(acc[3][3]) : "v"(W0[3]), "v"(X0[3]));
    asm volatile("ds_read_b64_tr_b16 %0, %1 offset:1664" : "=v"(wh[5]) : "v"(wa1[1]) : "memory");
    W1[5] = cat(wl[5], wh[5]);
    asm volatile("buffer_load_dwordx4 %0, %1, %2 offen offset:1024 sc1 lds" :: "v"(vl16), "s"(rS), "s"(sw) : "memory");
    asm volatile("v_cvt_pk_bf16_f32 %0, %1, %1" : "=v"(la_) : "v"(la_));
    asm volatile("v_lshlrev_b32 %0, 16, %0" : "+v"(la_) : );
    asm volatile("v_mfma_f32_16x16x32_bf16 %0, %1, %2, %0" : "+v"(acc[4][0]) : "v"(W0[4]), "v"(X0[0]));
    asm volatile("v_sqrt_f32 %0, %0" : "+v"(la_) : );
    asm volatile("v_add_f32 %0, %1, %2" : "=v"(lc) : "v"(c4[0].x), "v"(c4[0].y));
    asm volatile("v_mfma_f32_16x16x32_bf16 %0, %1, %2, %0" : "+v"(acc[4][1]) : "v"(W0[4]), "v"(X0[1]));
    asm volatile("v_cvt_pk_bf16_f32 %0, %1, %1" : "=v"(la_) : "v"(la_));
    asm volatile("v_lshlrev_b32 %0, 16, %0" : "+v"(la_) : );
    asm volatile("v_mfma_f32_16x16x32_bf16 %0, %1, %2, %0" : "+v"(acc[4][2]) : "v"(W0[4]), "v"(X0[2]));
    asm volatile("buffer_load_dwordx4 %0, %1, %2 offen offset:2048 sc1 lds" :: "v"(vl16), "s"(rS), "s"(sw) : "memory");
    asm volatile("v_rcp_f32 %0, %0" : "+v"(la_) : );
    asm volatile("v_add_f32 %0, %1, %2" : "=v"(ld) : "v"(c4[0].z), "v"(c4[0].w));
    asm volatile("v_mfma_f32_16x16x32_bf16 %0, %1, %2, %0" : "+v"(acc[4][3]) : "v"(W0[4]), "v"(X0[3]));
    asm volatile("v_mul_f32 %0, %1, %2" : "=v"(lb_) : "v"(st.y), "v"(la_));
    asm volatile("v_mul_f32 %0, %0, %1" : "+v"(lb_) : "v"(la_));
    asm volatile("v_mfma_f32_16x16x32_bf16 %0, %1, %2, %0" : "+v"(acc[5][0]) : "v"(W0[5]), "v"(X0[0]));
    asm volatile("v_cvt_pk_bf16_f32 %0, %1, %1" : "=v"(lb_) : "v"(lb_));
    asm volatile("v_lshlrev_b32 %0, 16, %0" : "+v"(lb_) : );
    asm volatile("v_mfma_f32_16x16x32_bf16 %0, %1, %2, %0" : "+v"(acc[5][1]) : "v"(W0[5]), "v"(X0[1]));
    asm volatile("v_add_f32 %0, %1, %0" : "+v"(lb_) : "s"(k_eps));
    asm volatile("v_cvt_pk_bf16_f32 %0, %1, %1" : "=v"(lb_) : "v"(lb_));
    asm volatile("v_mfma_f32_16x16x32_bf16 %0, %1, %2, %0" : "+v"(acc[5][2]) : "v"(W0[5]), "v"(X0[2]));
    asm volatile("v_lshlrev_b32 %0, 16, %0" : "+v"(lb_) : );
    asm volatile("v_sqrt_f32 %0, %0" : "+v"(lb_) : );
    asm volatile("v_mfma_f32_16x16x32_bf16 %0, %1, %2, %0" : "+v"(acc[5][3]) : "v"(W0[5]), "v"(X0[3]));
    asm volatile("s_waitcnt lgkmcnt(0)" ::: "memory");
    asm volatile("v_mfma_f32_16x16x32_bf16 %0, %1, %2, %0" : "+v"(acc[0][0]) : "v"(W1[0]), "v"(X1[0]));
    asm volatile("v_add_f32 %0, %1, %2" : "=v"(lc) : "v"(lc), "v"(ld));
    asm volatile("v_cvt_pk_bf16_f32 %0, %1, %1" : "=v"(lb_) : "v"(lb_));
    asm volatile("v_mfma_f32_16x16x32_bf16 %0, %1, %2, %0" : "+v"(acc[0][1]) : "v"(W1[0]), "v"(X1[1]));
    asm volatile("v_lshlrev_b32 %0, 16, %0" : "+v"(lb_) : );
    asm volatile("v_rcp_f32 %0, %0" : "+v"(lb_) : );
    asm volatile("v_mfma_f32_16x16x32_bf16 %0, %1, %2, %0" : "+v"(acc[0][2]) : "v"(W1[0]), "v"(X1[2]));
    asm volatile("v_add_f32 %0, %1, %2" : "=v"(ld) : "v"(c4[1].x), "v"(c4[1].y));
    asm volatile("v_mul_f32 %0, %0, %1" : "+v"(la_) : "v"(lb_));
    asm volatile("v_mfma_f32_16x16x32_bf16 %0, %1, %2, %0" : "+v"(acc[0][3]) : "v"(W1[0]), "v"(X1[3]));
    asm volatile("v_mul_f32 %0, %1, %2" : "=v"(lm) : "v"(st.x), "v"(la_));
    asm volatile("v_add_f32 %0, %1, %2" : "=v"(le) : "v"(c4[1].z), "v"(c4[1].w));
    asm volatile("v_mfma_f32_16x16x32_bf16 %0, %1, %2, %0" : "+v"(acc[1][0]) : "v"(W1[1]), "v"(X1[0]));
    asm volatile("v_add_f32 %0, %1, %2" : "=v"(ld) : "v"(ld), "v"(le));
    asm volatile("v_add_f32 %0, %1, %2" : "=v"(lc) : "v"(lc), "v"(ld));
    asm volatile("v_mfma_f32_16x16x32_bf16 %0, %1, %2, %0" : "+v"(acc[1][1]) : "v"(W1[1]), "v"(X1[1]));
    asm volatile("ds_write2st64_b32 %0, %1, %2 offset0:0 offset1:2" :: "v"(lra), "v"(la_), "v"(lm) : "memory");
    asm volatile("ds_write_b32 %0, %1 offset:1024" :: "v"(lds0 + tid*4), "v"(lc) : "memory");
    asm volatile("v_mfma_f32_16x16x32_bf16 %0, %1, %2, %0" : "+v"(acc[1][2]) : "v"(W1[1]), "v"(X1[2]));
    asm volatile("v_mfma_f32_16x16x32_bf16 %0, %1, %2, %0" : "+v"(acc[1][3]) : "v"(W1[1]), "v"(X1[3]));
    asm volatile("v_mfma_f32_16x16x32_bf16 %0, %1, %2, %0" : "+v"(acc[2][0]) : "v"(W1[2]), "v"(X1[0]));
    asm volatile("s_waitcnt lgkmcnt(0)\n\ts_barrier" ::: "memory");
    f4 sp_[6]; u2 bq[6]; float rs[4], mr[4];
    const unsigned ea = lds0 + (64*wm + r16)*4, eb = lds0 + 1024 + (96*wn + 4*g)*4, ec = lds0 + 9728 + (96*wn + 4*g)*2;
    asm volatile("v_mfma_f32_16x16x32_bf16 %0, %1, %2, %0" : "+v"(acc[2][1]) : "v"(W1[2]), "v"(X1[1]));
    asm volatile("ds_read_b128 %0, %1 offset:0" : "=v"(sp_[0]) : "v"(eb) : "memory");
    asm volatile("ds_read_b128 %0, %1 offset:64" : "=v"(sp_[1]) : "v"(eb) : "memory");
    asm volatile("v_mfma_f32_16x16x32_bf16 %0, %1, %2, %0" : "+v"(acc[2][2]) : "v"(W1[2]), "v"(X1[2]));
    asm volatile("ds_read_b128 %0, %1 offset:128" : "=v"(sp_[2]) : "v"(eb) : "memory");
    asm volatile("ds_read_b128 %0, %1 offset:192" : "=v"(sp_[3]) : "v"(eb) : "memory");
    asm volatile("v_mfma_f32_16x16x32_bf16 %0, %1, %2, %0" : "+v"(acc[2][3]) : "v"(W1[2]), "v"(X1[3]));
    asm volatile("ds_read_b128 %0, %1 offset:256" : "=v"(sp_[4]) : "v"(eb) : "memory");
    asm volatile("v_mfma_f32_16x16x32_bf16 %0, %1, %2, %0" : "+v"(acc[3][0]) : "v"(W1[3]), "v"(X1[0]));
    asm volatile("ds_read_b128 %0, %1 offset:320" : "=v"(sp_[5]) : "v"(eb) : "memory");
    asm volatile("ds_read_b64 %0, %1 offset:0" : "=v"(bq[0]) : "v"(ec) : "memory");
    asm volatile("v_mfma_f32_16x16x32_bf16 %0, %1, %2, %0" : "+v"(acc[3][1]) : "v"(W1[3]), "v"(X1[1]));
    asm volatile("ds_read_b64 %0, %1 offset:32" : "=v"(bq[1]) : "v"(ec) : "memory");
    asm volatile("ds_read_b64 %0, %1 offset:64" : "=v"(bq[2]) : "v"(ec) : "memory");
    asm volatile("v_mfma_f32_16x16x32_bf16 %0, %1, %2, %0" : "+v"(acc[3][2]) : "v"(W1[3]), "v"(X1[2]));
    asm volatile("ds_read_b64 %0, %1 offset:96" : "=v"(bq[3]) : "v"(ec) : "memory");
    asm volatile("v_mfma_f32_16x16x32_bf16 %0, %1, %2, %0" : "+v"(acc[3][3]) : "v"(W1[3]), "v"(X1[3]));
    asm volatile("ds_read_b64 %0, %1 offset:128" : "=v"(bq[4]) : "v"(ec) : "memory");
    asm volatile("ds_read_b64 %0, %1 offset:160" : "=v"(bq[5]) : "v"(ec) : "memory");
    asm volatile("v_mfma_f32_16x16x32_bf16 %0, %1, %2, %0" : "+v"(acc[4][0]) : "v"(W1[4]), "v"(X1[0]));
    asm volatile("ds_read_b32 %0, %1 offset:0" : "=v"(rs[0]) : "v"(ea) : "memory");
    asm volatile("ds_read_b32 %0, %1 offset:512" : "=v"(mr[0]) : "v"(ea) : "memory");
    asm volatile("v_mfma_f32_16x16x32_bf16 %0, %1, %2, %0" : "+v"(acc[4][1]) : "v"(W1[4]), "v"(X1[1]));
    asm volatile("ds_read_b32 %0, %1 offset:64" : "=v"(rs[1]) : "v"(ea) : "memory");
    asm volatile("v_mfma_f32_16x16x32_bf16 %0, %1, %2, %0" : "+v"(acc[4][2]) : "v"(W1[4]), "v"(X1[2]));
    asm volatile("ds_read_b32 %0, %1 offset:576" : "=v"(mr[1]) : "v"(ea) : "memory");
    asm volatile("ds_read_b32 %0, %1 offset:128" : "=v"(rs[2]) : "v"(ea) : "memory");
    asm volatile("v_mfma_f32_16x16x32_bf16 %0, %1, %2, %0" : "+v"(acc[4][3]) : "v"(W1[4]), "v"(X1[3]));
    asm volatile("ds_read_b32 %0, %1 offset:640" : "=v"(mr[2]) : "v"(ea) : "memory");
    asm volatile("ds_read_b32 %0, %1 offset:192" : "=v"(rs[3]) : "v"(ea) : "memory");
    asm volatile("v_mfma_f32_16x16x32_bf16 %0, %1, %2, %0" : "+v"(acc[5][0]) : "v"(W1[5]), "v"(X1[0]));
    asm volatile("ds_read_b32 %0, %1 offset:704" : "=v"(mr[3]) : "v"(ea) : "memory");
    asm volatile("v_mfma_f32_16x16x32_bf16 %0, %1, %2, %0" : "+v"(acc[5][1]) : "v"(W1[5]), "v"(X1[1]));
    asm volatile("v_mfma_f32_16x16x32_bf16 %0, %1, %2, %0" : "+v"(acc[5][2]) : "v"(W1[5]), "v"(X1[2]));
    asm volatile("v_mfma_f32_16x16x32_bf16 %0, %1, %2, %0" : "+v"(acc[5][3]) : "v"(W1[5]), "v"(X1[3]));
    asm volatile("s_waitcnt lgkmcnt(0)\n\ts_nop 6" ::: "memory");
    asm volatile("" : "+v"(sp_[0]), "+v"(sp_[1]), "+v"(sp_[2]), "+v"(sp_[3]), "+v"(sp_[4]), "+v"(sp_[5]), "+v"(bq[0]), "+v"(bq[1]), "+v"(bq[2]), "+v"(bq[3]), "+v"(bq[4]), "+v"(bq[5]));
    asm volatile("" : "+v"(rs[0]), "+v"(mr[0]), "+v"(rs[1]), "+v"(mr[1]), "+v"(rs[2]), "+v"(mr[2]), "+v"(rs[3]), "+v"(mr[3]));
    asm volatile("" : "+v"(acc[0][0]), "+v"(acc[0][1]), "+v"(acc[0][2]), "+v"(acc[0][3]), "+v"(acc[1][0]), "+v"(acc[1][1]), "+v"(acc[1][2]), "+v"(acc[1][3]), "+v"(acc[2][0]), "+v"(acc[2][1]), "+v"(acc[2][2]), "+v"(acc[2][3]));
    asm volatile("" : "+v"(acc[3][0]), "+v"(acc[3][1]), "+v"(acc[3][2]), "+v"(acc[3][3]), "+v"(acc[4][0]), "+v"(acc[4][1]), "+v"(acc[4][2]), "+v"(acc[4][3]), "+v"(acc[5][0]), "+v"(acc[5][1]), "+v"(acc[5][2]), "+v"(acc[5][3]));
    const unsigned flev = (lane >> 1) & 3, fo = 4096 + (c*16 + (flev < 3 ? 8 : 0) + 2*kq + (lane & 1))*128 + (flev < 3 ? 4*flev : 0);
    const bool fown = (lane & 1) == (j & 1) && flev < 3;
    unsigned f2v;
    const unsigned hv0 = vl16 + (4*wm + ((2*wn + kq) & 3))*1024, hv1 = vl16 + (4*wm + ((2*wn + 1 + kq) & 3))*1024, lh = lds0 + w*2048;
    f4 S[6], bi[6];
    _Pragma("unroll") for (int m = 0; m < 6; ++m) { S[m] = sp_[m]; bi[m] = unpk4(bq[m]); }
    const unsigned hso = 4194304 + ((c*88 + kso + 3*wn)*8 + 4*wm)*1024;
    unsigned hl[4]; _Pragma("unroll") for (int t = 0; t < 4; ++t) hl[t] = lds0 + 12288 + 3*wn*8192 + (4*wm + ((t - kq) & 3))*1024 + vl16;
    { f4 a0 = acc[0][0]*rs[0] + (bi[0] - S[0]*mr[0]), a1 = acc[1][0]*rs[0] + (bi[1] - S[1]*mr[0]);
      f4 z0 = gelu_z(a0), z1 = gelu_z(a1);
      z0 = exp4(z0); z1 = exp4(z1);
    asm volatile("" : "+v"(z0), "+v"(z1));
    asm volatile("buffer_load_dwordx4 %0, %1, %2 offen offset:3072 sc1 lds" :: "v"(vl16), "s"(rS), "s"(sw) : "memory");
      z0 = rcp4(z0 + 1.f); z1 = rcp4(z1 + 1.f);
      a0 *= z0; a1 *= z1;
      const u4 v = {cvtpk(a0.x, a0.y), cvtpk(a0.z, a0.w), cvtpk(a1.x, a1.y), cvtpk(a1.z, a1.w)};
      asm volatile("ds_write_b128 %0, %1 offset:0" :: "v"(hl[0]), "v"(v) : "memory");
      st128(v, rS, vl16 + 0, hso + 0, SC1); }
    __builtin_amdgcn_sched_barrier(0);
    { f4 a0 = acc[0][1]*rs[1] + (bi[0] - S[0]*mr[1]), a1 = acc[1][1]*rs[1] + (bi[1] - S[1]*mr[1]);
      f4 z0 = gelu_z(a0), z1 = gelu_z(a1);
      z0 = exp4(z0); z1 = exp4(z1);
    asm volatile("" : "+v"(z0), "+v"(z1));
    asm volatile("s_addk_i32 m0, 0x1000" ::: "scc", "memory");
    asm volatile("s_nop 0");
    asm volatile("buffer_load_dwordx4 %0, %1, %2 offen offset:0 sc1 lds" :: "v"(vl16h), "s"(rS), "s"(sw) : "memory");
      z0 = rcp4(z0 + 1.f); z1 = rcp4(z1 + 1.f);
      a0 *= z0; a1 *= z1;
      const u4 v = {cvtpk(a0.x, a0.y), cvtpk(a0.z, a0.w), cvtpk(a1.x, a1.y), cvtpk(a1.z, a1.w)};
      asm volatile("ds_write_b128 %0, %1 offset:0" :: "v"(hl[1]), "v"(v) : "memory");
      st128(v, rS, vl16 + 1024, hso + 0, SC1); }
    __builtin_amdgcn_sched_barrier(0);
    { f4 a0 = acc[0][2]*rs[2] + (bi[0] - S[0]*mr[2]), a1 = acc[1][2]*rs[2] + (bi[1] - S[1]*mr[2]);
      f4 z0 = gelu_z(a0), z1 = gelu_z(a1);
      z0 = exp4(z0); z1 = exp4(z1);
    asm volatile("" : "+v"(z0), "+v"(z1));
    sw = swb + (ksp + 0 + wm*(3u))*40960;
    asm volatile("" : "+s"(sw));
    asm volatile("s_add_u32 m0, %0, 92160" :: "s"(lw2) : "scc", "memory");
    asm volatile("s_nop 0");
    asm volatile("buffer_load_dwordx4 %0, %1, %2 offen offset:0 sc1 lds" :: "v"(vl16), "s"(rS), "s"(sw) : "memory");
      z0 = rcp4(z0 + 1.f); z1 = rcp4(z1 + 1.f);
      a0 *= z0; a1 *= z1;
      const u4 v = {cvtpk(a0.x, a0.y), cvtpk(a0.z, a0.w), cvtpk(a1.x, a1.y), cvtpk(a1.z, a1.w)};
      asm volatile("ds_write_b128 %0, %1 offset:0" :: "v"(hl[2]), "v"(v) : "memory");
      st128(v, rS, vl16 + 2048, hso + 0, SC1); }
    __builtin_amdgcn_sched_barrier(0);
    { f4 a0 = acc[0][3]*rs[3] + (bi[0] - S[0]*mr[3]), a1 = acc[1][3]*rs[3] + (bi[1] - S[1]*mr[3]);
      f4 z0 = gelu_z(a0), z1 = gelu_z(a1);
      z0 = exp4(z0); z1 = exp4(z1);
    asm volatile("" : "+v"(z0), "+v"(z1));
    asm volatile("buffer_load_dwordx4 %0, %1, %2 offen offset:1024 sc1 lds" :: "v"(vl16), "s"(rS), "s"(sw) : "memory");
      z0 = rcp4(z0 + 1.f); z1 = rcp4(z1 + 1.f);
      a0 *= z0; a1 *= z1;
      const u4 v = {cvtpk(a0.x, a0.y), cvtpk(a0.z, a0.w), cvtpk(a1.x, a1.y), cvtpk(a1.z, a1.w)};
      asm volatile("ds_write_b128 %0, %1 offset:0" :: "v"(hl[3]), "v"(v) : "memory");
      st128(v, rS, vl16 + 3072, hso + 0, SC1); }
    __builtin_amdgcn_sched_barrier(0);
    { f4 a0 = acc[2][0]*rs[0] + (bi[2] - S[2]*mr[0]), a1 = acc[3][0]*rs[0] + (bi[3] - S[3]*mr[0]);
      f4 z0 = gelu_z(a0), z1 = gelu_z(a1);
      z0 = exp4(z0); z1 = exp4(z1);
    asm volatile("" : "+v"(z0), "+v"(z1));
    asm volatile("buffer_load_dwordx4 %0, %1, %2 offen offset:2048 sc1 lds" :: "v"(vl16), "s"(rS), "s"(sw) : "memory");
      z0 = rcp4(z0 + 1.f); z1 = rcp4(z1 + 1.f);
      a0 *= z0; a1 *= z1;
      const u4 v = {cvtpk(a0.x, a0.y), cvtpk(a0.z, a0.w), cvtpk(a1.x, a1.y), cvtpk(a1.z, a1.w)};
      asm volatile("ds_write_b128 %0, %1 offset:8192" :: "v"(hl[0]), "v"(v) : "memory");
      st128(v, rS, vl16 + 0, hso + 8192, SC1); }
    __builtin_amdgcn_sched_barrier(0);
    { f4 a0 = acc[2][1]*rs[1] + (bi[2] - S[2]*mr[1]), a1 = acc[3][1]*rs[1] + (bi[3] - S[3]*mr[1]);
      f4 z0 = gelu_z(a0), z1 = gelu_z(a1);
      z0 = exp4(z0); z1 = exp4(z1);
    asm volatile("" : "+v"(z0), "+v"(z1));
    asm volatile("buffer_load_dwordx4 %0, %1, %2 offen offset:3072 sc1 lds" :: "v"(vl16), "s"(rS), "s"(sw) : "memory");
      z0 = rcp4(z0 + 1.f); z1 = rcp4(z1 + 1.f);
      a0 *= z0; a1 *= z1;
      const u4 v = {cvtpk(a0.x, a0.y), cvtpk(a0.z, a0.w), cvtpk(a1.x, a1.y), cvtpk(a1.z, a1.w)};
      asm volatile("ds_write_b128 %0, %1 offset:8192" :: "v"(hl[1]), "v"(v) : "memory");
      st128(v, rS, vl16 + 1024, hso + 8192, SC1); }
    __builtin_amdgcn_sched_barrier(0);
    { f4 a0 = acc[2][2]*rs[2] + (bi[2] - S[2]*mr[2]), a1 = acc[3][2]*rs[2] + (bi[3] - S[3]*mr[2]);
      f4 z0 = gelu_z(a0), z1 = gelu_z(a1);
      z0 = exp4(z0); z1 = exp4(z1);
    asm volatile("" : "+v"(z0), "+v"(z1));
    asm volatile("s_addk_i32 m0, 0x1000" ::: "scc", "memory");
    asm volatile("s_nop 0");
    asm volatile("buffer_load_dwordx4 %0, %1, %2 offen offset:0 sc1 lds" :: "v"(vl16h), "s"(rS), "s"(sw) : "memory");
      z0 = rcp4(z0 + 1.f); z1 = rcp4(z1 + 1.f);
      a0 *= z0; a1 *= z1;
      const u4 v = {cvtpk(a0.x, a0.y), cvtpk(a0.z, a0.w), cvtpk(a1.x, a1.y), cvtpk(a1.z, a1.w)};
      asm volatile("ds_write_b128 %0, %1 offset:8192" :: "v"(hl[2]), "v"(v) : "memory");
      st128(v, rS, vl16 + 2048, hso + 8192, SC1); }
    __builtin_amdgcn_sched_barrier(0);
    asm volatile("s_waitcnt vmcnt(6)\n\ts_barrier" ::: "memory");
    __builtin_amdgcn_raw_buffer_store_b32(tag, rS, tid == 0 ? 4096 + (c*16 + 8 + j)*128 + 0 : OOB, 0, SC1);
    { f4 a0 = acc[2][3]*rs[3] + (bi[2] - S[2]*mr[3]), a1 = acc[3][3]*rs[3] + (bi[3] - S[3]*mr[3]);
      f4 z0 = gelu_z(a0), z1 = gelu_z(a1);
      z0 = exp4(z0); z1 = exp4(z1);
    asm volatile("" : "+v"(z0), "+v"(z1));
    sw = swb + (ksp + 1 + wm*(3u))*40960;
    asm volatile("" : "+s"(sw));
    asm volatile("s_add_u32 m0, %0, 71680" :: "s"(lw2) : "scc", "memory");
    asm volatile("s_nop 0");
    asm volatile("buffer_load_dwordx4 %0, %1, %2 offen offset:0 sc1 lds" :: "v"(vl16), "s"(rS), "s"(sw) : "memory");
      z0 = rcp4(z0 + 1.f); z1 = rcp4(z1 + 1.f);
      a0 *= z0; a1 *= z1;
      const u4 v = {cvtpk(a0.x, a0.y), cvtpk(a0.z, a0.w), cvtpk(a1.x, a1.y), cvtpk(a1.z, a1.w)};
      asm volatile("ds_write_b128 %0, %1 offset:8192" :: "v"(hl[3]), "v"(v) : "memory");
      st128(v, rS, vl16 + 3072, hso + 8192, SC1); }
    __builtin_amdgcn_sched_barrier(0);
    { f4 a0 = acc[4][0]*rs[0] + (bi[4] - S[4]*mr[0]), a1 = acc[5][0]*rs[0] + (bi[5] - S[5]*mr[0]);
      f4 z0 = gelu_z(a0), z1 = gelu_z(a1);
      z0 = exp4(z0); z1 = exp4(z1);
    asm volatile("" : "+v"(z0), "+v"(z1));
    asm volatile("buffer_load_dwordx4 %0, %1, %2 offen offset:1024 sc1 lds" :: "v"(vl16), "s"(rS), "s"(sw) : "memory");
      z0 = rcp4(z0 + 1.f); z1 = rcp4(z1 + 1.f);
      a0 *= z0; a1 *= z1;
      const u4 v = {cvtpk(a0.x, a0.y), cvtpk(a0.z, a0.w), cvtpk(a1.x, a1.y), cvtpk(a1.z, a1.w)};
      asm volatile("ds_write_b128 %0, %1 offset:16384" :: "v"(hl[0]), "v"(v) : "memory");
      st128(v, rS, vl16 + 0, hso + 16384, SC1); }
    __builtin_amdgcn_sched_barrier(0);
    { f4 a0 = acc[4][1]*rs[1] + (bi[4] - S[4]*mr[1]), a1 = acc[5][1]*rs[1] + (bi[5] - S[5]*mr[1]);
      f4 z0 = gelu_z(a0), z1 = gelu_z(a1);
      z0 = exp4(z0); z1 = exp4(z1);
    asm volatile("" : "+v"(z0), "+v"(z1));
    asm volatile("buffer_load_dwordx4 %0, %1, %2 offen offset:2048 sc1 lds" :: "v"(vl16), "s"(rS), "s"(sw) : "memory");
      z0 = rcp4(z0 + 1.f); z1 = rcp4(z1 + 1.f);
      a0 *= z0; a1 *= z1;
      const u4 v = {cvtpk(a0.x, a0.y), cvtpk(a0.z, a0.w), cvtpk(a1.x, a1.y), cvtpk(a1.z, a1.w)};
      asm volatile("ds_write_b128 %0, %1 offset:16384" :: "v"(hl[1]), "v"(v) : "memory");
      st128(v, rS, vl16 + 1024, hso + 16384, SC1); }
    __builtin_amdgcn_sched_barrier(0);
    { f4 a0 = acc[4][2]*rs[2] + (bi[4] - S[4]*mr[2]), a1 = acc[5][2]*rs[2] + (bi[5] - S[5]*mr[2]);
      f4 z0 = gelu_z(a0), z1 = gelu_z(a1);
      z0 = exp4(z0); z1 = exp4(z1);
    asm volatile("" : "+v"(z0), "+v"(z1));
    asm volatile("buffer_load_dwordx4 %0, %1, %2 offen offset:3072 sc1 lds" :: "v"(vl16), "s"(rS), "s"(sw) : "memory");
      z0 = rcp4(z0 + 1.f); z1 = rcp4(z1 + 1.f);
      a0 *= z0; a1 *= z1;
      const u4 v = {cvtpk(a0.x, a0.y), cvtpk(a0.z, a0.w), cvtpk(a1.x, a1.y), cvtpk(a1.z, a1.w)};
      asm volatile("ds_write_b128 %0, %1 offset:16384" :: "v"(hl[2]), "v"(v) : "memory");
      st128(v, rS, vl16 + 2048, hso + 16384, SC1); }
    __builtin_amdgcn_sched_barrier(0);
    { f4 a0 = acc[4][3]*rs[3] + (bi[4] - S[4]*mr[3]), a1 = acc[5][3]*rs[3] + (bi[5] - S[5]*mr[3]);
      f4 z0 = gelu_z(a0), z1 = gelu_z(a1);
      z0 = exp4(z0); z1 = exp4(z1);
    asm volatile("" : "+v"(z0), "+v"(z1));
    asm volatile("buffer_load_dword %0, %1, %2, 0 offen sc1" : "=v"(f2v) : "v"(fo), "s"(rS) : "memory");
      z0 = rcp4(z0 + 1.f); z1 = rcp4(z1 + 1.f);
      a0 *= z0; a1 *= z1;
      const u4 v = {cvtpk(a0.x, a0.y), cvtpk(a0.z, a0.w), cvtpk(a1.x, a1.y), cvtpk(a1.z, a1.w)};
      asm volatile("ds_write_b128 %0, %1 offset:16384" :: "v"(hl[3]), "v"(v) : "memory");
      st128(v, rS, vl16 + 3072, hso + 16384, SC1); }
    __builtin_amdgcn_sched_barrier(0);
    __builtin_amdgcn_sched_barrier(0);
    asm volatile("s_addk_i32 m0, 0x1000" ::: "scc", "memory");
    asm volatile("s_nop 0");
    asm volatile("buffer_load_dwordx4 %0, %1, %2 offen offset:0 sc1 lds" :: "v"(vl16h), "s"(rS), "s"(sw) : "memory");
    f4 o[5][4]; u4 HX0[4], HX1[4], WX0[5], WX1[5]; u2 tl[4], th[4];
    const unsigned hb = lds0 + 12288 + wm*4096 + vl16, wlo = lds0 + 61440 + wn*5120 + vl16, whi = wlo + 51200;
    unsigned ht[2]; _Pragma("unroll") for (int t = 0; t < 2; ++t) { const unsigned h = (r16 >> 1) & 1;
      ht[t] = lds0 + 12288 + wm*4096 + (32*h + ((8*g + 4*t + (r16 >> 2)) ^ (4*h)))*16 + 8*(r16 & 1); }
    __builtin_amdgcn_sched_barrier(0);
    u4 WA[22][5], HK0[22], HK1[22];
    u2 ex[5], es; const unsigned tok = 128*c + 64*wm + 16*kq + r16, xvo = tok*1280 + (160*cq + 80*wn + 4*g)*2;
    asm volatile("s_waitcnt vmcnt(9) lgkmcnt(0)\n\ts_barrier" ::: "memory");
    __builtin_amdgcn_raw_buffer_store_b32(tag, rS, tid == 0 ? 4096 + (c*16 + 8 + j)*128 + 4 : OOB, 0, SC1);
    asm volatile("ds_read_b128 %0, %1 offset:3072" : "=v"(HX0[3]) : "v"(hb) : "memory");
    asm volatile("ds_read_b128 %0, %1 offset:2048" : "=v"(HX0[2]) : "v"(hb) : "memory");
    asm volatile("ds_read_b128 %0, %1 offset:40960" : "=v"(WX0[0]) : "v"(whi) : "memory");
    asm volatile("ds_read_b128 %0, %1 offset:41984" : "=v"(WX0[1]) : "v"(whi) : "memory");
    asm volatile("ds_read_b128 %0, %1 offset:43008" : "=v"(WX0[2]) : "v"(whi) : "memory");
    asm volatile("ds_read_b128 %0, %1 offset:44032" : "=v"(WX0[3]) : "v"(whi) : "memory");
    asm volatile("ds_read_b128 %0, %1 offset:45056" : "=v"(WX0[4]) : "v"(whi) : "memory");
    asm volatile("ds_read_b128 %0, %1 offset:1024" : "=v"(HX0[1]) : "v"(hb) : "memory");
    asm volatile("ds_read_b128 %0, %1 offset:0" : "=v"(HX0[0]) : "v"(hb) : "memory");
    asm volatile("s_waitcnt lgkmcnt(0)" ::: "memory");
    asm volatile("v_mfma_f32_16x16x32_bf16 %0, %1, %2, 0" : "=&v"(o[0][3]) : "v"(WX0[0]), "v"(HX0[3]));
    asm volatile("ds_read_b128 %0, %1 offset:11264" : "=v"(HX1[3]) : "v"(hb) : "memory");
    asm volatile("v_mfma_f32_16x16x32_bf16 %0, %1, %2, 0" : "=&v"(o[0][2]) : "v"(WX0[0]), "v"(HX0[2]));
    asm volatile("ds_read_b128 %0, %1 offset:10240" : "=v"(HX1[2]) : "v"(hb) : "memory");
    asm volatile("v_mfma_f32_16x16x32_bf16 %0, %1, %2, 0" : "=&v"(o[0][1]) : "v"(WX0[0]), "v"(HX0[1]));
    asm volatile("ds_read_b128 %0, %1 offset:30720" : "=v"(WX1[0]) : "v"(whi) : "memory");
    asm volatile("v_mfma_f32_16x16x32_bf16 %0, %1, %2, 0" : "=&v"(o[0][0]) : "v"(WX0[0]), "v"(HX0[0]));
    asm volatile("ds_read_b128 %0, %1 offset:31744" : "=v"(WX1[1]) : "v"(whi) : "memory");
    asm volatile("v_mfma_f32_16x16x32_bf16 %0, %1, %2, 0" : "=&v"(o[1][3]) : "v"(WX0[1]), "v"(HX0[3]));
    asm volatile("ds_read_b128 %0, %1 offset:32768" : "=v"(WX1[2]) : "v"(whi) : "memory");
    asm volatile("v_mfma_f32_16x16x32_bf16 %0, %1, %2, 0" : "=&v"(o[1][2]) : "v"(WX0[1]), "v"(HX0[2]));
    asm volatile("ds_read_b128 %0, %1 offset:33792" : "=v"(WX1[3]) : "v"(whi) : "memory");
    asm volatile("v_mfma_f32_16x16x32_bf16 %0, %1, %2, 0" : "=&v"(o[1][1]) : "v"(WX0[1]), "v"(HX0[1]));
    asm volatile("ds_read_b128 %0, %1 offset:34816" : "=v"(WX1[4]) : "v"(whi) : "memory");
    asm volatile("v_mfma_f32_16x16x32_bf16 %0, %1, %2, 0" : "=&v"(o[1][0]) : "v"(WX0[1]), "v"(HX0[0]));
    asm volatile("ds_read_b128 %0, %1 offset:9216" : "=v"(HX1[1]) : "v"(hb) : "memory");
    asm volatile("v_mfma_f32_16x16x32_bf16 %0, %1, %2, 0" : "=&v"(o[2][3]) : "v"(WX0[2]), "v"(HX0[3]));
    asm volatile("ds_read_b128 %0, %1 offset:8192" : "=v"(HX1[0]) : "v"(hb) : "memory");
    asm volatile("v_mfma_f32_16x16x32_bf16 %0, %1, %2, 0" : "=&v"(o[2][2]) : "v"(WX0[2]), "v"(HX0[2]));
    asm volatile("v_mfma_f32_16x16x32_bf16 %0, %1, %2, 0" : "=&v"(o[2][1]) : "v"(WX0[2]), "v"(HX0[1]));
    asm volatile("v_mfma_f32_16x16x32_bf16 %0, %1, %2, 0" : "=&v"(o[2][0]) : "v"(WX0[2]), "v"(HX0[0]));
    asm volatile("v_mfma_f32_16x16x32_bf16 %0, %1, %2, 0" : "=&v"(o[3][3]) : "v"(WX0[3]), "v"(HX0[3]));
    asm volatile("v_mfma_f32_16x16x32_bf16 %0, %1, %2, 0" : "=&v"(o[3][2]) : "v"(WX0[3]), "v"(HX0[2]));
    asm volatile("v_mfma_f32_16x16x32_bf16 %0, %1, %2, 0" : "=&v"(o[3][1]) : "v"(WX0[3]), "v"(HX0[1]));
    asm volatile("v_mfma_f32_16x16x32_bf16 %0, %1, %2, 0" : "=&v"(o[3][0]) : "v"(WX0[3]), "v"(HX0[0]));
    asm volatile("v_mfma_f32_16x16x32_bf16 %0, %1, %2, 0" : "=&v"(o[4][3]) : "v"(WX0[4]), "v"(HX0[3]));
    asm volatile("v_mfma_f32_16x16x32_bf16 %0, %1, %2, 0" : "=&v"(o[4][2]) : "v"(WX0[4]), "v"(HX0[2]));
    asm volatile("v_mfma_f32_16x16x32_bf16 %0, %1, %2, 0" : "=&v"(o[4][1]) : "v"(WX0[4]), "v"(HX0[1]));
    asm volatile("v_mfma_f32_16x16x32_bf16 %0, %1, %2, 0" : "=&v"(o[4][0]) : "v"(WX0[4]), "v"(HX0[0]));
    asm volatile("s_waitcnt lgkmcnt(0)\n\ts_barrier" ::: "memory");
    __builtin_amdgcn_sched_barrier(0);
    asm volatile("s_waitcnt vmcnt(3)" ::: "memory");
    asm volatile("" : "+v"(f2v));
    if (!__all(flev > 0 || fown || f2v == tag)) spin1(rS, fo, flev > 0 || fown, tag);
    __builtin_amdgcn_sched_barrier(0);
    asm volatile("v_mfma_f32_16x16x32_bf16 %0, %1, %2, %0" : "+v"(o[0][3]) : "v"(WX1[0]), "v"(HX1[3]));
    asm volatile("ds_read_b128 %0, %1 offset:19456" : "=v"(HX0[3]) : "v"(hb) : "memory");
    sh = 4194304 + (c*88 + ksp + 0)*8192;
    asm volatile("" : "+s"(sh));
    asm volatile("s_add_u32 m0, %0, 12288" :: "s"(lh) : "scc", "memory");
    asm volatile("v_mfma_f32_16x16x32_bf16 %0, %1, %2, %0" : "+v"(o[0][2]) : "v"(WX1[0]), "v"(HX1[2]));
    asm volatile("ds_read_b128 %0, %1 offset:18432" : "=v"(HX0[2]) : "v"(hb) : "memory");
    asm volatile("buffer_load_dwordx4 %0, %1, %2 offen offset:0 sc1 lds" :: "v"(hv0), "s"(rS), "s"(sh) : "memory");
    asm volatile("v_mfma_f32_16x16x32_bf16 %0, %1, %2, %0" : "+v"(o[0][1]) : "v"(WX1[0]), "v"(HX1[1]));
    asm volatile("ds_read_b128 %0, %1 offset:20480" : "=v"(WX0[0]) : "v"(whi) : "memory");
    asm volatile("v_mfma_f32_16x16x32_bf16 %0, %1, %2, %0" : "+v"(o[0][0]) : "v"(WX1[0]), "v"(HX1[0]));
    asm volatile("ds_read_b128 %0, %1 offset:21504" : "=v"(WX0[1]) : "v"(whi) : "memory");
    asm volatile("s_addk_i32 m0, 0x400" ::: "scc", "memory");
    asm volatile("v_mfma_f32_16x16x32_bf16 %0, %1, %2, %0" : "+v"(o[1][3]) : "v"(WX1[1]), "v"(HX1[3]));
    asm volatile("ds_read_b128 %0, %1 offset:22528" : "=v"(WX0[2]) : "v"(whi) : "memory");
    asm volatile("buffer_load_dwordx4 %0, %1, %2 offen offset:0 sc1 lds" :: "v"(hv1), "s"(rS), "s"(sh) : "memory");
    asm volatile("v_mfma_f32_16x16x32_bf16 %0, %1, %2, %0" : "+v"(o[1][2]) : "v"(WX1[1]), "v"(HX1[2]));
    asm volatile("ds_read_b128 %0, %1 offset:23552" : "=v"(WX0[3]) : "v"(whi) : "memory");
    asm volatile("v_mfma_f32_16x16x32_bf16 %0, %1, %2, %0" : "+v"(o[1][1]) : "v"(WX1[1]), "v"(HX1[1]));
    asm volatile("ds_read_b128 %0, %1 offset:24576" : "=v"(WX0[4]) : "v"(whi) : "memory");
    sh = 4194304 + (c*88 + ksp + 3)*8192;
    asm volatile("" : "+s"(sh));
    asm volatile("s_add_u32 m0, %0, 20480" :: "s"(lh) : "scc", "memory");
    asm volatile("v_mfma_f32_16x16x32_bf16 %0, %1, %2, %0" : "+v"(o[1][0]) : "v"(WX1[1]), "v"(HX1[0]));
    asm volatile("ds_read_b128 %0, %1 offset:17408" : "=v"(HX0[1]) : "v"(hb) : "memory");
    asm volatile("buffer_load_dwordx4 %0, %1, %2 offen offset:0 sc1 lds" :: "v"(hv0), "s"(rS), "s"(sh) : "memory");
    asm volatile("v_mfma_f32_16x16x32_bf16 %0, %1, %2, %0" : "+v"(o[2][3]) : "v"(WX1[2]), "v"(HX1[3]));
    asm volatile("ds_read_b128 %0, %1 offset:16384" : "=v"(HX0[0]) : "v"(hb) : "memory");
    asm volatile("v_mfma_f32_16x16x32_bf16 %0, %1, %2, %0" : "+v"(o[2][2]) : "v"(WX1[2]), "v"(HX1[2]));
    asm volatile("s_addk_i32 m0, 0x400" ::: "scc", "memory");
    asm volatile("v_mfma_f32_16x16x32_bf16 %0, %1, %2, %0" : "+v"(o[2][1]) : "v"(WX1[2]), "v"(HX1[1]));
    asm volatile("buffer_load_dwordx4 %0, %1, %2 offen offset:0 sc1 lds" :: "v"(hv1), "s"(rS), "s"(sh) : "memory");
    asm volatile("v_mfma_f32_16x16x32_bf16 %0, %1, %2, %0" : "+v"(o[2][0]) : "v"(WX1[2]), "v"(HX1[0]));
    asm volatile("v_mfma_f32_16x16x32_bf16 %0, %1, %2, %0" : "+v"(o[3][3]) : "v"(WX1[3]), "v"(HX1[3]));
    sw = swb + (ksp + 2 + wm*(3u))*40960;
    asm volatile("" : "+s"(sw));
    asm volatile("s_add_u32 m0, %0, 153600" :: "s"(lw2) : "scc", "memory");
    asm volatile("v_mfma_f32_16x16x32_bf16 %0, %1, %2, %0" : "+v"(o[3][2]) : "v"(WX1[3]), "v"(HX1[2]));
    asm volatile("buffer_load_dwordx4 %0, %1, %2 offen offset:0 sc1 lds" :: "v"(vl16), "s"(rS), "s"(sw) : "memory");
    asm volatile("v_mfma_f32_16x16x32_bf16 %0, %1, %2, %0" : "+v"(o[3][1]) : "v"(WX1[3]), "v"(HX1[1]));
    asm volatile("v_mfma_f32_16x16x32_bf16 %0, %1, %2, %0" : "+v"(o[3][0]) : "v"(WX1[3]), "v"(HX1[0]));
    asm volatile("v_mfma_f32_16x16x32_bf16 %0, %1, %2, %0" : "+v"(o[4][3]) : "v"(WX1[4]), "v"(HX1[3]));
    asm volatile("buffer_load_dwordx4 %0, %1, %2 offen offset:1024 sc1 lds" :: "v"(vl16), "s"(rS), "s"(sw) : "memory");
    asm volatile("v_mfma_f32_16x16x32_bf16 %0, %1, %2, %0" : "+v"(o[4][2]) : "v"(WX1[4]), "v"(HX1[2]));
    asm volatile("v_mfma_f32_16x16x32_bf16 %0, %1, %2, %0" : "+v"(o[4][1]) : "v"(WX1[4]), "v"(HX1[1]));
    asm volatile("v_mfma_f32_16x16x32_bf16 %0, %1, %2, %0" : "+v"(o[4][0]) : "v"(WX1[4]), "v"(HX1[0]));
    asm volatile("buffer_load_dwordx4 %0, %1, %2 offen offset:2048 sc1 lds" :: "v"(vl16), "s"(rS), "s"(sw) : "memory");
    asm volatile("s_waitcnt lgkmcnt(0)" ::: "memory");
    asm volatile("v_mfma_f32_16x16x32_bf16 %0, %1, %2, %0" : "+v"(o[0][3]) : "v"(WX0[0]), "v"(HX0[3]));
    asm volatile("ds_read_b128 %0, %1 offset:27648" : "=v"(HX1[3]) : "v"(hb) : "memory");
    asm volatile("v_mfma_f32_16x16x32_bf16 %0, %1, %2, %0" : "+v"(o[0][2]) : "v"(WX0[0]), "v"(HX0[2]));
    asm volatile("ds_read_b128 %0, %1 offset:26624" : "=v"(HX1[2]) : "v"(hb) : "memory");
    asm volatile("v_mfma_f32_16x16x32_bf16 %0, %1, %2, %0" : "+v"(o[0][1]) : "v"(WX0[0]), "v"(HX0[1]));
    asm volatile("ds_read_b128 %0, %1 offset:10240" : "=v"(WX1[0]) : "v"(whi) : "memory");
    asm volatile("buffer_load_dwordx4 %0, %1, %2 offen offset:3072 sc1 lds" :: "v"(vl16), "s"(rS), "s"(sw) : "memory");
    asm volatile("v_mfma_f32_16x16x32_bf16 %0, %1, %2, %0" : "+v"(o[0][0]) : "v"(WX0[0]), "v"(HX0[0]));
    asm volatile("ds_read_b128 %0, %1 offset:11264" : "=v"(WX1[1]) : "v"(whi) : "memory");
    asm volatile("v_mfma_f32_16x16x32_bf16 %0, %1, %2, %0" : "+v"(o[1][3]) : "v"(WX0[1]), "v"(HX0[3]));
    asm volatile("ds_read_b128 %0, %1 offset:12288" : "=v"(WX1[2]) : "v"(whi) : "memory");
    asm volatile("s_addk_i32 m0, 0x1000" ::: "scc", "memory");
    asm volatile("v_mfma_f32_16x16x32_bf16 %0, %1, %2, %0" : "+v"(o[1][2]) : "v"(WX0[1]), "v"(HX0[2]));
    asm volatile("ds_read_b128 %0, %1 offset:13312" : "=v"(WX1[3]) : "v"(whi) : "memory");
    asm volatile("buffer_load_dwordx4 %0, %1, %2 offen offset:0 sc1 lds" :: "v"(vl16h), "s"(rS), "s"(sw) : "memory");
    asm volatile("v_mfma_f32_16x16x32_bf16 %0, %1, %2, %0" : "+v"(o[1][1]) : "v"(WX0[1]), "v"(HX0[1]));
    asm volatile("ds_read_b128 %0, %1 offset:14336" : "=v"(WX1[4]) : "v"(whi) : "memory");
    asm volatile("v_mfma_f32_16x16x32_bf16 %0, %1, %2, %0" : "+v"(o[1][0]) : "v"(WX0[1]), "v"(HX0[0]));
    asm volatile("ds_read_b128 %0, %1 offset:25600" : "=v"(HX1[1]) : "v"(hb) : "memory");
    asm volatile("buffer_load_dword %0, %1, %2, 0 offen sc1" : "=v"(f2v) : "v"(fo), "s"(rS) : "memory");
    asm volatile("v_mfma_f32_16x16x32_bf16 %0, %1, %2, %0" : "+v"(o[2][3]) : "v"(WX0[2]), "v"(HX0[3]));
    asm volatile("ds_read_b128 %0, %1 offset:24576" : "=v"(HX1[0]) : "v"(hb) : "memory");
    asm volatile("v_mfma_f32_16x16x32_bf16 %0, %1, %2, %0" : "+v"(o[2][2]) : "v"(WX0[2]), "v"(HX0[2]));
    asm volatile("v_mfma_f32_16x16x32_bf16 %0, %1, %2, %0" : "+v"(o[2][1]) : "v"(WX0[2]), "v"(HX0[1]));
    asm volatile("v_mfma_f32_16x16x32_bf16 %0, %1, %2, %0" : "+v"(o[2][0]) : "v"(WX0[2]), "v"(HX0[0]));
    asm volatile("v_mfma_f32_16x16x32_bf16 %0, %1, %2, %0" : "+v"(o[3][3]) : "v"(WX0[3]), "v"(HX0[3]));
    asm volatile("v_mfma_f32_16x16x32_bf16 %0, %1, %2, %0" : "+v"(o[3][2]) : "v"(WX0[3]), "v"(HX0[2]));
    asm volatile("v_mfma_f32_16x16x32_bf16 %0, %1, %2, %0" : "+v"(o[3][1]) : "v"(WX0[3]), "v"(HX0[1]));
    asm volatile("v_mfma_f32_16x16x32_bf16 %0, %1, %2, %0" : "+v"(o[3][0]) : "v"(WX0[3]), "v"(HX0[0]));
    asm volatile("v_mfma_f32_16x16x32_bf16 %0, %1, %2, %0" : "+v"(o[4][3]) : "v"(WX0[4]), "v"(HX0[3]));
    asm volatile("v_mfma_f32_16x16x32_bf16 %0, %1, %2, %0" : "+v"(o[4][2]) : "v"(WX0[4]), "v"(HX0[2]));
    asm volatile("v_mfma_f32_16x16x32_bf16 %0, %1, %2, %0" : "+v"(o[4][1]) : "v"(WX0[4]), "v"(HX0[1]));
    asm volatile("v_mfma_f32_16x16x32_bf16 %0, %1, %2, %0" : "+v"(o[4][0]) : "v"(WX0[4]), "v"(HX0[0]));
    asm volatile("s_waitcnt vmcnt(12) lgkmcnt(0)\n\ts_barrier" ::: "memory");
    __builtin_amdgcn_raw_buffer_store_b32(tag, rS, tid == 0 ? 4096 + (c*16 + 8 + j)*128 + 8 : OOB, 0, SC1);
    asm volatile("v_mfma_f32_16x16x32_bf16 %0, %1, %2, %0" : "+v"(o[0][3]) : "v"(WX1[0]), "v"(HX1[3]));
    asm volatile("ds_read_b128 %0, %1 offset:35840" : "=v"(HX0[3]) : "v"(hb) : "memory");
    sw = swb + (kss + 0 + wm*(1u))*40960;
    asm volatile("" : "+s"(sw));
    asm volatile("s_add_u32 m0, %0, 133120" :: "s"(lw2) : "scc", "memory");
    asm volatile("v_mfma_f32_16x16x32_bf16 %0, %1, %2, %0" : "+v"(o[0][2]) : "v"(WX1[0]), "v"(HX1[2]));
    asm volatile("ds_read_b128 %0, %1 offset:34816" : "=v"(HX0[2]) : "v"(hb) : "memory");
    asm volatile("buffer_load_dwordx4 %0, %1, %2 offen offset:0 sc1 lds" :: "v"(vl16), "s"(rS), "s"(sw) : "memory");
    asm volatile("v_mfma_f32_16x16x32_bf16 %0, %1, %2, %0" : "+v"(o[0][1]) : "v"(WX1[0]), "v"(HX1[1]));
    asm volatile("ds_read_b128 %0, %1 offset:0" : "=v"(WX0[0]) : "v"(whi) : "memory");
    asm volatile("v_mfma_f32_16x16x32_bf16 %0, %1, %2, %0" : "+v"(o[0][0]) : "v"(WX1[0]), "v"(HX1[0]));
    asm volatile("ds_read_b128 %0, %1 offset:1024" : "=v"(WX0[1]) : "v"(whi) : "memory");
    asm volatile("v_mfma_f32_16x16x32_bf16 %0, %1, %2, %0" : "+v"(o[1][3]) : "v"(WX1[1]), "v"(HX1[3]));
    asm volatile("ds_read_b128 %0, %1 offset:2048" : "=v"(WX0[2]) : "v"(whi) : "memory");
    asm volatile("buffer_load_dwordx4 %0, %1, %2 offen offset:1024 sc1 lds" :: "v"(vl16), "s"(rS), "s"(sw) : "memory");
    asm volatile("v_mfma_f32_16x16x32_bf16 %0, %1, %2, %0" : "+v"(o[1][2]) : "v"(WX1[1]), "v"(HX1[2]));
    asm volatile("ds_read_b128 %0, %1 offset:3072" : "=v"(WX0[3]) : "v"(whi) : "memory");
    asm volatile("v_mfma_f32_16x16x32_bf16 %0, %1, %2, %0" : "+v"(o[1][1]) : "v"(WX1[1]), "v"(HX1[1]));
    asm volatile("ds_read_b128 %0, %1 offset:4096" : "=v"(WX0[4]) : "v"(whi) : "memory");
    asm volatile("v_mfma_f32_16x16x32_bf16 %0, %1, %2, %0" : "+v"(o[1][0]) : "v"(WX1[1]), "v"(HX1[0]));
    asm volatile("ds_read_b128 %0, %1 offset:33792" : "=v"(HX0[1]) : "v"(hb) : "memory");
    asm volatile("buffer_load_dwordx4 %0, %1, %2 offen offset:2048 sc1 lds" :: "v"(vl16), "s"(rS), "s"(sw) : "memory");
    asm volatile("v_mfma_f32_16x16x32_bf16 %0, %1, %2, %0" : "+v"(o[2][3]) : "v"(WX1[2]), "v"(HX1[3]));
    asm volatile("ds_read_b128 %0, %1 offset:32768" : "=v"(HX0[0]) : "v"(hb) : "memory");
    asm volatile("v_mfma_f32_16x16x32_bf16 %0, %1, %2, %0" : "+v"(o[2][2]) : "v"(WX1[2]), "v"(HX1[2]));
    asm volatile("v_mfma_f32_16x16x32_bf16 %0, %1, %2, %0" : "+v"(o[2][1]) : "v"(WX1[2]), "v"(HX1[1]));
    asm volatile("buffer_load_dwordx4 %0, %1, %2 offen offset:3072 sc1 lds" :: "v"(vl16), "s"(rS), "s"(sw) : "memory");
    asm volatile("v_mfma_f32_16x16x32_bf16 %0, %1, %2, %0" : "+v"(o[2][0]) : "v"(WX1[2]), "v"(HX1[0]));
    asm volatile("v_mfma_f32_16x16x32_bf16 %0, %1, %2, %0" : "+v"(o[3][3]) : "v"(WX1[3]), "v"(HX1[3]));
    asm volatile("s_addk_i32 m0, 0x1000" ::: "scc", "memory");
    asm volatile("v_mfma_f32_16x16x32_bf16 %0, %1, %2, %0" : "+v"(o[3][2]) : "v"(WX1[3]), "v"(HX1[2]));
    asm volatile("buffer_load_dwordx4 %0, %1, %2 offen offset:0 sc1 lds" :: "v"(vl16h), "s"(rS), "s"(sw) : "memory");
    asm volatile("v_mfma_f32_16x16x32_bf16 %0, %1, %2, %0" : "+v"(o[3][1]) : "v"(WX1[3]), "v"(HX1[1]));
    asm volatile("v_mfma_f32_16x16x32_bf16 %0, %1, %2, %0" : "+v"(o[3][0]) : "v"(WX1[3]), "v"(HX1[0]));
    asm volatile("v_mfma_f32_16x16x32_bf16 %0, %1, %2, %0" : "+v"(o[4][3]) : "v"(WX1[4]), "v"(HX1[3]));
    asm volatile("v_mfma_f32_16x16x32_bf16 %0, %1, %2, %0" : "+v"(o[4][2]) : "v"(WX1[4]), "v"(HX1[2]));
    asm volatile("v_mfma_f32_16x16x32_bf16 %0, %1, %2, %0" : "+v"(o[4][1]) : "v"(WX1[4]), "v"(HX1[1]));
    asm volatile("v_mfma_f32_16x16x32_bf16 %0, %1, %2, %0" : "+v"(o[4][0]) : "v"(WX1[4]), "v"(HX1[0]));
    asm volatile("s_waitcnt lgkmcnt(0)" ::: "memory");
    asm volatile("v_mfma_f32_16x16x32_bf16 %0, %1, %2, %0" : "+v"(o[0][3]) : "v"(WX0[0]), "v"(HX0[3]));
    asm volatile("ds_read_b128 %0, %1 offset:44032" : "=v"(HX1[3]) : "v"(hb) : "memory");
    asm volatile("v_mfma_f32_16x16x32_bf16 %0, %1, %2, %0" : "+v"(o[0][2]) : "v"(WX0[0]), "v"(HX0[2]));
    asm volatile("ds_read_b128 %0, %1 offset:43008" : "=v"(HX1[2]) : "v"(hb) : "memory");
    asm volatile("v_mfma_f32_16x16x32_bf16 %0, %1, %2, %0" : "+v"(o[0][1]) : "v"(WX0[0]), "v"(HX0[1]));
    asm volatile("ds_read_b128 %0, %1 offset:40960" : "=v"(WX1[0]) : "v"(wlo) : "memory");
    asm volatile("v_mfma_f32_16x16x32_bf16 %0, %1, %2, %0" : "+v"(o[0][0]) : "v"(WX0[0]), "v"(HX0[0]));
    asm volatile("ds_read_b128 %0, %1 offset:41984" : "=v"(WX1[1]) : "v"(wlo) : "memory");
    __builtin_amdgcn_sched_barrier(0);
    asm volatile("s_waitcnt vmcnt(6)" ::: "memory");
    asm volatile("" : "+v"(f2v));
    if (!__all(flev > 1 || fown || f2v == tag)) spin1(rS, fo, flev > 1 || fown, tag);
    __builtin_amdgcn_sched_barrier(0);
    asm volatile("v_mfma_f32_16x16x32_bf16 %0, %1, %2, %0" : "+v"(o[1][3]) : "v"(WX0[1]), "v"(HX0[3]));
    asm volatile("ds_read_b128 %0, %1 offset:43008" : "=v"(WX1[2]) : "v"(wlo) : "memory");
    sh = 4194304 + (c*88 + ksp + 1)*8192;
    asm volatile("" : "+s"(sh));
    asm volatile("s_add_u32 m0, %0, 28672" :: "s"(lh) : "scc", "memory");
    asm volatile("v_mfma_f32_16x16x32_bf16 %0, %1, %2, %0" : "+v"(o[1][2]) : "v"(WX0[1]), "v"(HX0[2]));
    asm volatile("ds_read_b128 %0, %1 offset:44032" : "=v"(WX1[3]) : "v"(wlo) : "memory");
    asm volatile("buffer_load_dwordx4 %0, %1, %2 offen offset:0 sc1 lds" :: "v"(hv0), "s"(rS), "s"(sh) : "memory");
    asm volatile("v_mfma_f32_16x16x32_bf16 %0, %1, %2, %0" : "+v"(o[1][1]) : "v"(WX0[1]), "v"(HX0[1]));
    asm volatile("ds_read_b128 %0, %1 offset:45056" : "=v"(WX1[4]) : "v"(wlo) : "memory");
    asm volatile("v_mfma_f32_16x16x32_bf16 %0, %1, %2, %0" : "+v"(o[1][0]) : "v"(WX0[1]), "v"(HX0[0]));
    asm volatile("ds_read_b128 %0, %1 offset:41984" : "=v"(HX1[1]) : "v"(hb) : "memory");
    asm volatile("s_addk_i32 m0, 0x400" ::: "scc", "memory");
    asm volatile("v_mfma_f32_16x16x32_bf16 %0, %1, %2, %0" : "+v"(o[2][3]) : "v"(WX0[2]), "v"(HX0[3]));
    asm volatile("ds_read_b128 %0, %1 offset:40960" : "=v"(HX1[0]) : "v"(hb) : "memory");
    asm volatile("buffer_load_dwordx4 %0, %1, %2 offen offset:0 sc1 lds" :: "v"(hv1), "s"(rS), "s"(sh) : "memory");
    asm volatile("v_mfma_f32_16x16x32_bf16 %0, %1, %2, %0" : "+v"(o[2][2]) : "v"(WX0[2]), "v"(HX0[2]));
    asm volatile("v_mfma_f32_16x16x32_bf16 %0, %1, %2, %0" : "+v"(o[2][1]) : "v"(WX0[2]), "v"(HX0[1]));
    sh = 4194304 + (c*88 + ksp + 4)*8192;
    asm volatile("" : "+s"(sh));
    asm volatile("s_add_u32 m0, %0, 36864" :: "s"(lh) : "scc", "memory");
    asm volatile("v_mfma_f32_16x16x32_bf16 %0, %1, %2, %0" : "+v"(o[2][0]) : "v"(WX0[2]), "v"(HX0[0]));
    asm volatile("buffer_load_dwordx4 %0, %1, %2 offen offset:0 sc1 lds" :: "v"(hv0), "s"(rS), "s"(sh) : "memory");
    asm volatile("v_mfma_f32_16x16x32_bf16 %0, %1, %2, %0" : "+v"(o[3][3]) : "v"(WX0[3]), "v"(HX0[3]));
    asm volatile("v_mfma_f32_16x16x32_bf16 %0, %1, %2, %0" : "+v"(o[3][2]) : "v"(WX0[3]), "v"(HX0[2]));
    asm volatile("s_addk_i32 m0, 0x400" ::: "scc", "memory");
    asm volatile("v_mfma_f32_16x16x32_bf16 %0, %1, %2, %0" : "+v"(o[3][1]) : "v"(WX0[3]), "v"(HX0[1]));
    asm volatile("buffer_load_dwordx4 %0, %1, %2 offen offset:0 sc1 lds" :: "v"(hv1), "s"(rS), "s"(sh) : "memory");
    asm volatile("v_mfma_f32_16x16x32_bf16 %0, %1, %2, %0" : "+v"(o[3][0]) : "v"(WX0[3]), "v"(HX0[0]));
    asm volatile("v_mfma_f32_16x16x32_bf16 %0, %1, %2, %0" : "+v"(o[4][3]) : "v"(WX0[4]), "v"(HX0[3]));
    asm volatile("v_mfma_f32_16x16x32_bf16 %0, %1, %2, %0" : "+v"(o[4][2]) : "v"(WX0[4]), "v"(HX0[2]));
    asm volatile("v_mfma_f32_16x16x32_bf16 %0, %1, %2, %0" : "+v"(o[4][1]) : "v"(WX0[4]), "v"(HX0[1]));
    asm volatile("v_mfma_f32_16x16x32_bf16 %0, %1, %2, %0" : "+v"(o[4][0]) : "v"(WX0[4]), "v"(HX0[0]));
    asm volatile("s_waitcnt lgkmcnt(0)\n\ts_barrier" ::: "memory");
    asm volatile("v_mfma_f32_16x16x32_bf16 %0, %1, %2, %0" : "+v"(o[0][3]) : "v"(WX1[0]), "v"(HX1[3]));
    asm volatile("ds_read_b128 %0, %1 offset:3072" : "=v"(HX0[3]) : "v"(hb) : "memory");
    sw = swb + (kss + 2 + wm*(1u))*40960;
    asm volatile("" : "+s"(sw));
    asm volatile("s_add_u32 m0, %0, 112640" :: "s"(lw2) : "scc", "memory");
    asm volatile("v_mfma_f32_16x16x32_bf16 %0, %1, %2, %0" : "+v"(o[0][2]) : "v"(WX1[0]), "v"(HX1[2]));
    asm volatile("ds_read_b128 %0, %1 offset:2048" : "=v"(HX0[2]) : "v"(hb) : "memory");
    asm volatile("buffer_load_dwordx4 %0, %1, %2 offen offset:0 sc1 lds" :: "v"(vl16), "s"(rS), "s"(sw) : "memory");
    asm volatile("v_mfma_f32_16x16x32_bf16 %0, %1, %2, %0" : "+v"(o[0][1]) : "v"(WX1[0]), "v"(HX1[1]));
    asm volatile("ds_read_b128 %0, %1 offset:30720" : "=v"(WX0[0]) : "v"(wlo) : "memory");
    asm volatile("v_mfma_f32_16x16x32_bf16 %0, %1, %2, %0" : "+v"(o[0][0]) : "v"(WX1[0]), "v"(HX1[0]));
    asm volatile("ds_read_b128 %0, %1 offset:31744" : "=v"(WX0[1]) : "v"(wlo) : "memory");
    asm volatile("v_mfma_f32_16x16x32_bf16 %0, %1, %2, %0" : "+v"(o[1][3]) : "v"(WX1[1]), "v"(HX1[3]));
    asm volatile("ds_read_b128 %0, %1 offset:32768" : "=v"(WX0[2]) : "v"(wlo) : "memory");
    asm volatile("buffer_load_dwordx4 %0, %1, %2 offen offset:1024 sc1 lds" :: "v"(vl16), "s"(rS), "s"(sw) : "memory");
    asm volatile("v_mfma_f32_16x16x32_bf16 %0, %1, %2, %0" : "+v"(o[1][2]) : "v"(WX1[1]), "v"(HX1[2]));
    asm volatile("ds_read_b128 %0, %1 offset:33792" : "=v"(WX0[3]) : "v"(wlo) : "memory");
    asm volatile("v_mfma_f32_16x16x32_bf16 %0, %1, %2, %0" : "+v"(o[1][1]) : "v"(WX1[1]), "v"(HX1[1]));
    asm volatile("ds_read_b128 %0, %1 offset:34816" : "=v"(WX0[4]) : "v"(wlo) : "memory");
    asm volatile("v_mfma_f32_16x16x32_bf16 %0, %1, %2, %0" : "+v"(o[1][0]) : "v"(WX1[1]), "v"(HX1[0]));
    asm volatile("ds_read_b128 %0, %1 offset:1024" : "=v"(HX0[1]) : "v"(hb) : "memory");
    asm volatile("buffer_load_dwordx4 %0, %1, %2 offen offset:2048 sc1 lds" :: "v"(vl16), "s"(rS), "s"(sw) : "memory");
    asm volatile("v_mfma_f32_16x16x32_bf16 %0, %1, %2, %0" : "+v"(o[2][3]) : "v"(WX1[2]), "v"(HX1[3]));
    asm volatile("ds_read_b128 %0, %1 offset:0" : "=v"(HX0[0]) : "v"(hb) : "memory");
    asm volatile("v_mfma_f32_16x16x32_bf16 %0, %1, %2, %0" : "+v"(o[2][2]) : "v"(WX1[2]), "v"(HX1[2]));
    asm volatile("v_mfma_f32_16x16x32_bf16 %0, %1, %2, %0" : "+v"(o[2][1]) : "v"(WX1[2]), "v"(HX1[1]));
    asm volatile("buffer_load_dwordx4 %0, %1, %2 offen offset:3072 sc1 lds" :: "v"(vl16), "s"(rS), "s"(sw) : "memory");
    asm volatile("v_mfma_f32_16x16x32_bf16 %0, %1, %2, %0" : "+v"(o[2][0]) : "v"(WX1[2]), "v"(HX1[0]));
    asm volatile("v_mfma_f32_16x16x32_bf16 %0, %1, %2, %0" : "+v"(o[3][3]) : "v"(WX1[3]), "v"(HX1[3]));
    asm volatile("s_addk_i32 m0, 0x1000" ::: "scc", "memory");
    asm volatile("v_mfma_f32_16x16x32_bf16 %0, %1, %2, %0" : "+v"(o[3][2]) : "v"(WX1[3]), "v"(HX1[2]));
    asm volatile("buffer_load_dwordx4 %0, %1, %2 offen offset:0 sc1 lds" :: "v"(vl16h), "s"(rS), "s"(sw) : "memory");
    asm volatile("v_mfma_f32_16x16x32_bf16 %0, %1, %2, %0" : "+v"(o[3][1]) : "v"(WX1[3]), "v"(HX1[1]));
    asm volatile("v_mfma_f32_16x16x32_bf16 %0, %1, %2, %0" : "+v"(o[3][0]) : "v"(WX1[3]), "v"(HX1[0]));
    asm volatile("v_mfma_f32_16x16x32_bf16 %0, %1, %2, %0" : "+v"(o[4][3]) : "v"(WX1[4]), "v"(HX1[3]));
    asm volatile("v_mfma_f32_16x16x32_bf16 %0, %1, %2, %0" : "+v"(o[4][2]) : "v"(WX1[4]), "v"(HX1[2]));
    asm volatile("v_mfma_f32_16x16x32_bf16 %0, %1, %2, %0" : "+v"(o[4][1]) : "v"(WX1[4]), "v"(HX1[1]));
    asm volatile("v_mfma_f32_16x16x32_bf16 %0, %1, %2, %0" : "+v"(o[4][0]) : "v"(WX1[4]), "v"(HX1[0]));
    asm volatile("s_waitcnt lgkmcnt(0)" ::: "memory");
    asm volatile("v_mfma_f32_16x16x32_bf16 %0, %1, %2, %0" : "+v"(o[0][3]) : "v"(WX0[0]), "v"(HX0[3]));
    asm volatile("ds_read_b128 %0, %1 offset:11264" : "=v"(HX1[3]) : "v"(hb) : "memory");
    asm volatile("v_mfma_f32_16x16x32_bf16 %0, %1, %2, %0" : "+v"(o[0][2]) : "v"(WX0[0]), "v"(HX0[2]));
    asm volatile("ds_read_b128 %0, %1 offset:10240" : "=v"(HX1[2]) : "v"(hb) : "memory");
    asm volatile("v_mfma_f32_16x16x32_bf16 %0, %1, %2, %0" : "+v"(o[0][1]) : "v"(WX0[0]), "v"(HX0[1]));
    asm volatile("ds_read_b128 %0, %1 offset:20480" : "=v"(WX1[0]) : "v"(wlo) : "memory");
    asm volatile("v_mfma_f32_16x16x32_bf16 %0, %1, %2, %0" : "+v"(o[0][0]) : "v"(WX0[0]), "v"(HX0[0]));
    asm volatile("ds_read_b128 %0, %1 offset:21504" : "=v"(WX1[1]) : "v"(wlo) : "memory");
    asm volatile("v_mfma_f32_16x16x32_bf16 %0, %1, %2, %0" : "+v"(o[1][3]) : "v"(WX0[1]), "v"(HX0[3]));
    asm volatile("ds_read_b128 %0, %1 offset:22528" : "=v"(WX1[2]) : "v"(wlo) : "memory");
    asm volatile("buffer_load_dword %0, %1, %2, 0 offen sc1" : "=v"(f2v) : "v"(fo), "s"(rS) : "memory");
    asm volatile("v_mfma_f32_16x16x32_bf16 %0, %1, %2, %0" : "+v"(o[1][2]) : "v"(WX0[1]), "v"(HX0[2]));
    asm volatile("ds_read_b128 %0, %1 offset:23552" : "=v"(WX1[3]) : "v"(wlo) : "memory");
    asm volatile("v_mfma_f32_16x16x32_bf16 %0, %1, %2, %0" : "+v"(o[1][1]) : "v"(WX0[1]), "v"(HX0[1]));
    asm volatile("ds_read_b128 %0, %1 offset:24576" : "=v"(WX1[4]) : "v"(wlo) : "memory");
    asm volatile("v_mfma_f32_16x16x32_bf16 %0, %1, %2, %0" : "+v"(o[1][0]) : "v"(WX0[1]), "v"(HX0[0]));
    asm volatile("ds_read_b128 %0, %1 offset:9216" : "=v"(HX1[1]) : "v"(hb) : "memory");
    asm volatile("v_mfma_f32_16x16x32_bf16 %0, %1, %2, %0" : "+v"(o[2][3]) : "v"(WX0[2]), "v"(HX0[3]));
    asm volatile("ds_read_b128 %0, %1 offset:8192" : "=v"(HX1[0]) : "v"(hb) : "memory");
    asm volatile("v_mfma_f32_16x16x32_bf16 %0, %1, %2, %0" : "+v"(o[2][2]) : "v"(WX0[2]), "v"(HX0[2]));
    asm volatile("v_mfma_f32_16x16x32_bf16 %0, %1, %2, %0" : "+v"(o[2][1]) : "v"(WX0[2]), "v"(HX0[1]));
    asm volatile("v_mfma_f32_16x16x32_bf16 %0, %1, %2, %0" : "+v"(o[2][0]) : "v"(WX0[2]), "v"(HX0[0]));
    asm volatile("v_mfma_f32_16x16x32_bf16 %0, %1, %2, %0" : "+v"(o[3][3]) : "v"(WX0[3]), "v"(HX0[3]));
    asm volatile("v_mfma_f32_16x16x32_bf16 %0, %1, %2, %0" : "+v"(o[3][2]) : "v"(WX0[3]), "v"(HX0[2]));
    asm volatile("v_mfma_f32_16x16x32_bf16 %0, %1, %2, %0" : "+v"(o[3][1]) : "v"(WX0[3]), "v"(HX0[1]));
    asm volatile("v_mfma_f32_16x16x32_bf16 %0, %1, %2, %0" : "+v"(o[3][0]) : "v"(WX0[3]), "v"(HX0[0]));
    asm volatile("v_mfma_f32_16x16x32_bf16 %0, %1, %2, %0" : "+v"(o[4][3]) : "v"(WX0[4]), "v"(HX0[3]));
    asm volatile("v_mfma_f32_16x16x32_bf16 %0, %1, %2, %0" : "+v"(o[4][2]) : "v"(WX0[4]), "v"(HX0[2]));
    asm volatile("v_mfma_f32_16x16x32_bf16 %0, %1, %2, %0" : "+v"(o[4][1]) : "v"(WX0[4]), "v"(HX0[1]));
    asm volatile("v_mfma_f32_16x16x32_bf16 %0, %1, %2, %0" : "+v"(o[4][0]) : "v"(WX0[4]), "v"(HX0[0]));
    asm volatile("s_waitcnt vmcnt(6) lgkmcnt(0)\n\ts_barrier" ::: "memory");
    asm volatile("v_mfma_f32_16x16x32_bf16 %0, %1, %2, %0" : "+v"(o[0][3]) : "v"(WX1[0]), "v"(HX1[3]));
    asm volatile("ds_read_b128 %0, %1 offset:19456" : "=v"(HX0[3]) : "v"(hb) : "memory");
    sw = swb + (kss + 4 + wm*(1u))*40960;
    asm volatile("" : "+s"(sw));
    asm volatile("s_add_u32 m0, %0, 92160" :: "s"(lw2) : "scc", "memory");
    asm volatile("v_mfma_f32_16x16x32_bf16 %0, %1, %2, %0" : "+v"(o[0][2]) : "v"(WX1[0]), "v"(HX1[2]));
    asm volatile("ds_read_b128 %0, %1 offset:18432" : "=v"(HX0[2]) : "v"(hb) : "memory");
    asm volatile("buffer_load_dwordx4 %0, %1, %2 offen offset:0 sc1 lds" :: "v"(vl16), "s"(rS), "s"(sw) : "memory");
    asm volatile("v_mfma_f32_16x16x32_bf16 %0, %1, %2, %0" : "+v"(o[0][1]) : "v"(WX1[0]), "v"(HX1[1]));
    asm volatile("ds_read_b128 %0, %1 offset:10240" : "=v"(WX0[0]) : "v"(wlo) : "memory");
    asm volatile("v_mfma_f32_16x16x32_bf16 %0, %1, %2, %0" : "+v"(o[0][0]) : "v"(WX1[0]), "v"(HX1[0]));
    asm volatile("ds_read_b128 %0, %1 offset:11264" : "=v"(WX0[1]) : "v"(wlo) : "memory");
    asm volatile("buffer_load_dwordx4 %0, %1, %2 offen offset:1024 sc1 lds" :: "v"(vl16), "s"(rS), "s"(sw) : "memory");
    asm volatile("v_mfma_f32_16x16x32_bf16 %0, %1, %2, %0" : "+v"(o[1][3]) : "v"(WX1[1]), "v"(HX1[3]));
    asm volatile("ds_read_b128 %0, %1 offset:12288" : "=v"(WX0[2]) : "v"(wlo) : "memory");
    asm volatile("v_mfma_f32_16x16x32_bf16 %0, %1, %2, %0" : "+v"(o[1][2]) : "v"(WX1[1]), "v"(HX1[2]));
    asm volatile("ds_read_b128 %0, %1 offset:13312" : "=v"(WX0[3]) : "v"(wlo) : "memory");
    asm volatile("buffer_load_dwordx4 %0, %1, %2 offen offset:2048 sc1 lds" :: "v"(vl16), "s"(rS), "s"(sw) : "memory");
    asm volatile("v_mfma_f32_16x16x32_bf16 %0, %1, %2, %0" : "+v"(o[1][1]) : "v"(WX1[1]), "v"(HX1[1]));
    asm volatile("ds_read_b128 %0, %1 offset:14336" : "=v"(WX0[4]) : "v"(wlo) : "memory");
    asm volatile("v_mfma_f32_16x16x32_bf16 %0, %1, %2, %0" : "+v"(o[1][0]) : "v"(WX1[1]), "v"(HX1[0]));
    asm volatile("ds_read_b128 %0, %1 offset:17408" : "=v"(HX0[1]) : "v"(hb) : "memory");
    asm volatile("buffer_load_dwordx4 %0, %1, %2 offen offset:3072 sc1 lds" :: "v"(vl16), "s"(rS), "s"(sw) : "memory");
    asm volatile("v_mfma_f32_16x16x32_bf16 %0, %1, %2, %0" : "+v"(o[2][3]) : "v"(WX1[2]), "v"(HX1[3]));
    asm volatile("ds_read_b128 %0, %1 offset:16384" : "=v"(HX0[0]) : "v"(hb) : "memory");
    asm volatile("s_addk_i32 m0, 0x1000" ::: "scc", "memory");
    asm volatile("v_mfma_f32_16x16x32_bf16 %0, %1, %2, %0" : "+v"(o[2][2]) : "v"(WX1[2]), "v"(HX1[2]));
    asm volatile("buffer_load_dwordx4 %0, %1, %2 offen offset:0 sc1 lds" :: "v"(vl16h), "s"(rS), "s"(sw) : "memory");
    asm volatile("v_mfma_f32_16x16x32_bf16 %0, %1, %2, %0" : "+v"(o[2][1]) : "v"(WX1[2]), "v"(HX1[1]));
    asm volatile("v_mfma_f32_16x16x32_bf16 %0, %1, %2, %0" : "+v"(o[2][0]) : "v"(WX1[2]), "v"(HX1[0]));
    asm volatile("v_mfma_f32_16x16x32_bf16 %0, %1, %2, %0" : "+v"(o[3][3]) : "v"(WX1[3]), "v"(HX1[3]));
    asm volatile("v_mfma_f32_16x16x32_bf16 %0, %1, %2, %0" : "+v"(o[3][2]) : "v"(WX1[3]), "v"(HX1[2]));
    asm volatile("v_mfma_f32_16x16x32_bf16 %0, %1, %2, %0" : "+v"(o[3][1]) : "v"(WX1[3]), "v"(HX1[1]));
    asm volatile("v_mfma_f32_16x16x32_bf16 %0, %1, %2, %0" : "+v"(o[3][0]) : "v"(WX1[3]), "v"(HX1[0]));
    asm volatile("v_mfma_f32_16x16x32_bf16 %0, %1, %2, %0" : "+v"(o[4][3]) : "v"(WX1[4]), "v"(HX1[3]));
    asm volatile("v_mfma_f32_16x16x32_bf16 %0, %1, %2, %0" : "+v"(o[4][2]) : "v"(WX1[4]), "v"(HX1[2]));
    asm volatile("v_mfma_f32_16x16x32_bf16 %0, %1, %2, %0" : "+v"(o[4][1]) : "v"(WX1[4]), "v"(HX1[1]));
    asm volatile("v_mfma_f32_16x16x32_bf16 %0, %1, %2, %0" : "+v"(o[4][0]) : "v"(WX1[4]), "v"(HX1[0]));
    asm volatile("s_waitcnt lgkmcnt(0)" ::: "memory");
    asm volatile("v_mfma_f32_16x16x32_bf16 %0, %1, %2, %0" : "+v"(o[0][3]) : "v"(WX0[0]), "v"(HX0[3]));
    asm volatile("ds_read_b128 %0, %1 offset:27648" : "=v"(HX1[3]) : "v"(hb) : "memory");
    asm volatile("v_mfma_f32_16x16x32_bf16 %0, %1, %2, %0" : "+v"(o[0][2]) : "v"(WX0[0]), "v"(HX0[2]));
    asm volatile("ds_read_b128 %0, %1 offset:26624" : "=v"(HX1[2]) : "v"(hb) : "memory");
    __builtin_amdgcn_sched_barrier(0);
    asm volatile("s_waitcnt vmcnt(5)" ::: "memory");
    asm volatile("" : "+v"(f2v));
    if (!__all(flev > 3 || fown || f2v == tag)) spin1(rS, fo, flev > 3 || fown, tag);
    __builtin_amdgcn_sched_barrier(0);
    asm volatile("v_mfma_f32_16x16x32_bf16 %0, %1, %2, %0" : "+v"(o[0][1]) : "v"(WX0[0]), "v"(HX0[1]));
    asm volatile("ds_read_b128 %0, %1 offset:0" : "=v"(WX1[0]) : "v"(wlo) : "memory");
    sh = 4194304 + (c*88 + ksp + 2)*8192;
    asm volatile("" : "+s"(sh));
    asm volatile("s_add_u32 m0, %0, 45056" :: "s"(lh) : "scc", "memory");
    asm volatile("v_mfma_f32_16x16x32_bf16 %0, %1, %2, %0" : "+v"(o[0][0]) : "v"(WX0[0]), "v"(HX0[0]));
    asm volatile("ds_read_b128 %0, %1 offset:1024" : "=v"(WX1[1]) : "v"(wlo) : "memory");
    asm volatile("buffer_load_dwordx4 %0, %1, %2 offen offset:0 sc1 lds" :: "v"(hv0), "s"(rS), "s"(sh) : "memory");
    asm volatile("v_mfma_f32_16x16x32_bf16 %0, %1, %2, %0" : "+v"(o[1][3]) : "v"(WX0[1]), "v"(HX0[3]));
    asm volatile("ds_read_b128 %0, %1 offset:2048" : "=v"(WX1[2]) : "v"(wlo) : "memory");
    asm volatile("s_addk_i32 m0, 0x400" ::: "scc", "memory");
    asm volatile("v_mfma_f32_16x16x32_bf16 %0, %1, %2, %0" : "+v"(o[1][2]) : "v"(WX0[1]), "v"(HX0[2]));
    asm volatile("ds_read_b128 %0, %1 offset:3072" : "=v"(WX1[3]) : "v"(wlo) : "memory");
    asm volatile("buffer_load_dwordx4 %0, %1, %2 offen offset:0 sc1 lds" :: "v"(hv1), "s"(rS), "s"(sh) : "memory");
    asm volatile("v_mfma_f32_16x16x32_bf16 %0, %1, %2, %0" : "+v"(o[1][1]) : "v"(WX0[1]), "v"(HX0[1]));
    asm volatile("ds_read_b128 %0, %1 offset:4096" : "=v"(WX1[4]) : "v"(wlo) : "memory");
    sh = 4194304 + (c*88 + ksp + 5)*8192;
    asm volatile("" : "+s"(sh));
    asm volatile("s_add_u32 m0, %0, 53248" :: "s"(lh) : "scc", "memory");
    asm volatile("v_mfma_f32_16x16x32_bf16 %0, %1, %2, %0" : "+v"(o[1][0]) : "v"(WX0[1]), "v"(HX0[0]));
    asm volatile("ds_read_b128 %0, %1 offset:25600" : "=v"(HX1[1]) : "v"(hb) : "memory");
    asm volatile("buffer_load_dwordx4 %0, %1, %2 offen offset:0 sc1 lds" :: "v"(hv0), "s"(rS), "s"(sh) : "memory");
    asm volatile("v_mfma_f32_16x16x32_bf16 %0, %1, %2, %0" : "+v"(o[2][3]) : "v"(WX0[2]), "v"(HX0[3]));
    asm volatile("ds_read_b128 %0, %1 offset:24576" : "=v"(HX1[0]) : "v"(hb) : "memory");
    asm volatile("s_addk_i32 m0, 0x400" ::: "scc", "memory");
    asm volatile("v_mfma_f32_16x16x32_bf16 %0, %1, %2, %0" : "+v"(o[2][2]) : "v"(WX0[2]), "v"(HX0[2]));
    asm volatile("buffer_load_dwordx4 %0, %1, %2 offen offset:0 sc1 lds" :: "v"(hv1), "s"(rS), "s"(sh) : "memory");
    asm volatile("v_mfma_f32_16x16x32_bf16 %0, %1, %2, %0" : "+v"(o[2][1]) : "v"(WX0[2]), "v"(HX0[1]));
    sh = 4194304 + (c*88 + kss + 0)*8192;
    asm volatile("" : "+s"(sh));
    asm volatile("s_add_u32 m0, %0, 12288" :: "s"(lh) : "scc", "memory");
    asm volatile("v_mfma_f32_16x16x32_bf16 %0, %1, %2, %0" : "+v"(o[2][0]) : "v"(WX0[2]), "v"(HX0[0]));
    asm volatile("buffer_load_dwordx4 %0, %1, %2 offen offset:0 sc1 lds" :: "v"(hv0), "s"(rS), "s"(sh) : "memory");
    asm volatile("v_mfma_f32_16x16x32_bf16 %0, %1, %2, %0" : "+v"(o[3][3]) : "v"(WX0[3]), "v"(HX0[3]));
    asm volatile("s_addk_i32 m0, 0x400" ::: "scc", "memory");
    asm volatile("v_mfma_f32_16x16x32_bf16 %0, %1, %2, %0" : "+v"(o[3][2]) : "v"(WX0[3]), "v"(HX0[2]));
    asm volatile("buffer_load_dwordx4 %0, %1, %2 offen offset:0 sc1 lds" :: "v"(hv1), "s"(rS), "s"(sh) : "memory");
    asm volatile("v_mfma_f32_16x16x32_bf16 %0, %1, %2, %0" : "+v"(o[3][1]) : "v"(WX0[3]), "v"(HX0[1]));
    sh = 4194304 + (c*88 + kss + 1)*8192;
    asm volatile("" : "+s"(sh));
    asm volatile("s_add_u32 m0, %0, 20480" :: "s"(lh) : "scc", "memory");
    asm volatile("v_mfma_f32_16x16x32_bf16 %0, %1, %2, %0" : "+v"(o[3][0]) : "v"(WX0[3]), "v"(HX0[0]));
    asm volatile("buffer_load_dwordx4 %0, %1, %2 offen offset:0 sc1 lds" :: "v"(hv0), "s"(rS), "s"(sh) : "memory");
    asm volatile("v_mfma_f32_16x16x32_bf16 %0, %1, %2, %0" : "+v"(o[4][3]) : "v"(WX0[4]), "v"(HX0[3]));
    asm volatile("s_addk_i32 m0, 0x400" ::: "scc", "memory");
    asm volatile("v_mfma_f32_16x16x32_bf16 %0, %1, %2, %0" : "+v"(o[4][2]) : "v"(WX0[4]), "v"(HX0[2]));
    asm volatile("buffer_load_dwordx4 %0, %1, %2 offen offset:0 sc1 lds" :: "v"(hv1), "s"(rS), "s"(sh) : "memory");
    asm volatile("v_mfma_f32_16x16x32_bf16 %0, %1, %2, %0" : "+v"(o[4][1]) : "v"(WX0[4]), "v"(HX0[1]));
    asm volatile("v_mfma_f32_16x16x32_bf16 %0, %1, %2, %0" : "+v"(o[4][0]) : "v"(WX0[4]), "v"(HX0[0]));
    asm volatile("s_waitcnt vmcnt(4) lgkmcnt(0)\n\ts_barrier" ::: "memory");
    asm volatile("v_mfma_f32_16x16x32_bf16 %0, %1, %2, %0" : "+v"(o[0][3]) : "v"(WX1[0]), "v"(HX1[3]));
    asm volatile("ds_read_b128 %0, %1 offset:35840" : "=v"(HX0[3]) : "v"(hb) : "memory");
    sh = 4194304 + (c*88 + kss + 2)*8192;
    asm volatile("" : "+s"(sh));
    asm volatile("s_add_u32 m0, %0, 28672" :: "s"(lh) : "scc", "memory");
    asm volatile("v_mfma_f32_16x16x32_bf16 %0, %1, %2, %0" : "+v"(o[0][2]) : "v"(WX1[0]), "v"(HX1[2]));
    asm volatile("ds_read_b128 %0, %1 offset:34816" : "=v"(HX0[2]) : "v"(hb) : "memory");
    asm volatile("buffer_load_dwordx4 %0, %1, %2 offen offset:0 sc1 lds" :: "v"(hv0), "s"(rS), "s"(sh) : "memory");
    asm volatile("v_mfma_f32_16x16x32_bf16 %0, %1, %2, %0" : "+v"(o[0][1]) : "v"(WX1[0]), "v"(HX1[1]));
    asm volatile("ds_read_b128 %0, %1 offset:40960" : "=a"(WA[10][0]) : "v"(whi) : "memory");
    asm volatile("s_addk_i32 m0, 0x400" ::: "scc", "memory");
    asm volatile("v_mfma_f32_16x16x32_bf16 %0, %1, %2, %0" : "+v"(o[0][0]) : "v"(WX1[0]), "v"(HX1[0]));
    asm volatile("ds_read_b128 %0, %1 offset:41984" : "=a"(WA[10][1]) : "v"(whi) : "memory");
    asm volatile("buffer_load_dwordx4 %0, %1, %2 offen offset:0 sc1 lds" :: "v"(hv1), "s"(rS), "s"(sh) : "memory");
    asm volatile("v_mfma_f32_16x16x32_bf16 %0, %1, %2, %0" : "+v"(o[1][3]) : "v"(WX1[1]), "v"(HX1[3]));
    asm volatile("ds_read_b128 %0, %1 offset:43008" : "=a"(WA[10][2]) : "v"(whi) : "memory");
    sh = 4194304 + (c*88 + kss + 3)*8192;
    asm volatile("" : "+s"(sh));
    asm volatile("s_add_u32 m0, %0, 36864" :: "s"(lh) : "scc", "memory");
    asm volatile("v_mfma_f32_16x16x32_bf16 %0, %1, %2, %0" : "+v"(o[1][2]) : "v"(WX1[1]), "v"(HX1[2]));
    asm volatile("ds_read_b128 %0, %1 offset:44032" : "=a"(WA[10][3]) : "v"(whi) : "memory");
    asm volatile("buffer_load_dwordx4 %0, %1, %2 offen offset:0 sc1 lds" :: "v"(hv0), "s"(rS), "s"(sh) : "memory");
    asm volatile("v_mfma_f32_16x16x32_bf16 %0, %1, %2, %0" : "+v"(o[1][1]) : "v"(WX1[1]), "v"(HX1[1]));
    asm volatile("ds_read_b128 %0, %1 offset:45056" : "=a"(WA[10][4]) : "v"(whi) : "memory");
    asm volatile("s_addk_i32 m0, 0x400" ::: "scc", "memory");
    asm volatile("v_mfma_f32_16x16x32_bf16 %0, %1, %2, %0" : "+v"(o[1][0]) : "v"(WX1[1]), "v"(HX1[0]));
    asm volatile("ds_read_b128 %0, %1 offset:33792" : "=v"(HK1[10]) : "v"(hb) : "memory");
    asm volatile("buffer_load_dwordx4 %0, %1, %2 offen offset:0 sc1 lds" :: "v"(hv1), "s"(rS), "s"(sh) : "memory");
    asm volatile("v_mfma_f32_16x16x32_bf16 %0, %1, %2, %0" : "+v"(o[2][3]) : "v"(WX1[2]), "v"(HX1[3]));
    asm volatile("ds_read_b128 %0, %1 offset:32768" : "=v"(HK0[10]) : "v"(hb) : "memory");
    sw = swb + (kss + 6 + wm*(1u))*40960;
    asm volatile("" : "+s"(sw));
    asm volatile("s_add_u32 m0, %0, 71680" :: "s"(lw2) : "scc", "memory");
    asm volatile("v_mfma_f32_16x16x32_bf16 %0, %1, %2, %0" : "+v"(o[2][2]) : "v"(WX1[2]), "v"(HX1[2]));
    asm volatile("buffer_load_dwordx4 %0, %1, %2 offen offset:0 sc1 lds" :: "v"(vl16), "s"(rS), "s"(sw) : "memory");
    asm volatile("v_mfma_f32_16x16x32_bf16 %0, %1, %2, %0" : "+v"(o[2][1]) : "v"(WX1[2]), "v"(HX1[1]));
    asm volatile("v_mfma_f32_16x16x32_bf16 %0, %1, %2, %0" : "+v"(o[2][0]) : "v"(WX1[2]), "v"(HX1[0]));
    asm volatile("buffer_load_dwordx4 %0, %1, %2 offen offset:1024 sc1 lds" :: "v"(vl16), "s"(rS), "s"(sw) : "memory");
    asm volatile("v_mfma_f32_16x16x32_bf16 %0, %1, %2, %0" : "+v"(o[3][3]) : "v"(WX1[3]), "v"(HX1[3]));
    asm volatile("v_mfma_f32_16x16x32_bf16 %0, %1, %2, %0" : "+v"(o[3][2]) : "v"(WX1[3]), "v"(HX1[2]));
    asm volatile("buffer_load_dwordx4 %0, %1, %2 offen offset:2048 sc1 lds" :: "v"(vl16), "s"(rS), "s"(sw) : "memory");
    asm volatile("v_mfma_f32_16x16x32_bf16 %0, %1, %2, %0" : "+v"(o[3][1]) : "v"(WX1[3]), "v"(HX1[1]));
    asm volatile("v_mfma_f32_16x16x32_bf16 %0, %1, %2, %0" : "+v"(o[3][0]) : "v"(WX1[3]), "v"(HX1[0]));
    asm volatile("buffer_load_dwordx4 %0, %1, %2 offen offset:3072 sc1 lds" :: "v"(vl16), "s"(rS), "s"(sw) : "memory");
    asm volatile("v_mfma_f32_16x16x32_bf16 %0, %1, %2, %0" : "+v"(o[4][3]) : "v"(WX1[4]), "v"(HX1[3]));
    asm volatile("s_addk_i32 m0, 0x1000" ::: "scc", "memory");
    asm volatile("v_mfma_f32_16x16x32_bf16 %0, %1, %2, %0" : "+v"(o[4][2]) : "v"(WX1[4]), "v"(HX1[2]));
    asm volatile("buffer_load_dwordx4 %0, %1, %2 offen offset:0 sc1 lds" :: "v"(vl16h), "s"(rS), "s"(sw) : "memory");
    asm volatile("v_mfma_f32_16x16x32_bf16 %0, %1, %2, %0" : "+v"(o[4][1]) : "v"(WX1[4]), "v"(HX1[1]));
    asm volatile("v_mfma_f32_16x16x32_bf16 %0, %1, %2, %0" : "+v"(o[4][0]) : "v"(WX1[4]), "v"(HX1[0]));
    asm volatile("s_waitcnt lgkmcnt(0)" ::: "memory");
    asm volatile("v_mfma_f32_16x16x32_bf16 %0, %1, %2, %0" : "+v"(o[0][3]) : "a"(WA[10][0]), "v"(HX0[3]));
    asm volatile("ds_read_b128 %0, %1 offset:44032" : "=v"(HX1[3]) : "v"(hb) : "memory");
    asm volatile("ds_read_b128 %0, %1 offset:43008" : "=v"(HX1[2]) : "v"(hb) : "memory");
    asm volatile("v_mfma_f32_16x16x32_bf16 %0, %1, %2, %0" : "+v"(o[0][2]) : "a"(WA[10][0]), "v"(HX0[2]));
    asm volatile("ds_read_b128 %0, %1 offset:30720" : "=a"(WA[11][0]) : "v"(whi) : "memory");
    asm volatile("v_mfma_f32_16x16x32_bf16 %0, %1, %2, %0" : "+v"(o[1][3]) : "a"(WA[10][1]), "v"(HX0[3]));
    asm volatile("ds_read_b128 %0, %1 offset:31744" : "=a"(WA[11][1]) : "v"(whi) : "memory");
    asm volatile("ds_read_b128 %0, %1 offset:32768" : "=a"(WA[11][2]) : "v"(whi) : "memory");
    asm volatile("v_mfma_f32_16x16x32_bf16 %0, %1, %2, %0" : "+v"(o[1][2]) : "a"(WA[10][1]), "v"(HX0[2]));
    asm volatile("ds_read_b128 %0, %1 offset:33792" : "=a"(WA[11][3]) : "v"(whi) : "memory");
    asm volatile("v_mfma_f32_16x16x32_bf16 %0, %1, %2, %0" : "+v"(o[2][3]) : "a"(WA[10][2]), "v"(HX0[3]));
    asm volatile("ds_read_b128 %0, %1 offset:34816" : "=a"(WA[11][4]) : "v"(whi) : "memory");
    asm volatile("ds_read_b128 %0, %1 offset:41984" : "=v"(HK1[11]) : "v"(hb) : "memory");
    asm volatile("v_mfma_f32_16x16x32_bf16 %0, %1, %2, %0" : "+v"(o[2][2]) : "a"(WA[10][2]), "v"(HX0[2]));
    asm volatile("ds_read_b128 %0, %1 offset:40960" : "=v"(HK0[11]) : "v"(hb) : "memory");
    asm volatile("v_mfma_f32_16x16x32_bf16 %0, %1, %2, %0" : "+v"(o[3][3]) : "a"(WA[10][3]), "v"(HX0[3]));
    asm volatile("v_mfma_f32_16x16x32_bf16 %0, %1, %2, %0" : "+v"(o[3][2]) : "a"(WA[10][3]), "v"(HX0[2]));
    asm volatile("v_mfma_f32_16x16x32_bf16 %0, %1, %2, %0" : "+v"(o[4][3]) : "a"(WA[10][4]), "v"(HX0[3]));
    asm volatile("v_mfma_f32_16x16x32_bf16 %0, %1, %2, %0" : "+v"(o[4][2]) : "a"(WA[10][4]), "v"(HX0[2]));
    asm volatile("s_waitcnt vmcnt(9) lgkmcnt(0)\n\ts_barrier" ::: "memory");
    asm volatile("v_mfma_f32_16x16x32_bf16 %0, %1, %2, %0" : "+v"(o[0][3]) : "a"(WA[11][0]), "v"(HX1[3]));
    asm volatile("ds_read_b64_tr_b16 %0, %1 offset:3072" : "=v"(tl[3]) : "v"(ht[0]) : "memory");
    asm volatile("ds_read_b64_tr_b16 %0, %1 offset:3072" : "=v"(th[3]) : "v"(ht[1]) : "memory");
    HX0[3] = cat(tl[3], th[3]);
    asm volatile("ds_read_b64_tr_b16 %0, %1 offset:2048" : "=v"(tl[2]) : "v"(ht[0]) : "memory");
    sh = 4194304 + (c*88 + kss + 4)*8192;
    asm volatile("" : "+s"(sh));
    asm volatile("s_add_u32 m0, %0, 45056" :: "s"(lh) : "scc", "memory");
    asm volatile("v_mfma_f32_16x16x32_bf16 %0, %1, %2, %0" : "+v"(o[0][2]) : "a"(WA[11][0]), "v"(HX1[2]));
    asm volatile("ds_read_b64_tr_b16 %0, %1 offset:2048" : "=v"(th[2]) : "v"(ht[1]) : "memory");
    HX0[2] = cat(tl[2], th[2]);
    asm volatile("ds_read_b128 %0, %1 offset:20480" : "=a"(WA[12][0]) : "v"(whi) : "memory");
    asm volatile("buffer_load_dwordx4 %0, %1, %2 offen offset:0 sc1 lds" :: "v"(hv0), "s"(rS), "s"(sh) : "memory");
    asm volatile("s_addk_i32 m0, 0x400" ::: "scc", "memory");
    asm volatile("v_mfma_f32_16x16x32_bf16 %0, %1, %2, %0" : "+v"(o[1][3]) : "a"(WA[11][1]), "v"(HX1[3]));
    asm volatile("ds_read_b128 %0, %1 offset:21504" : "=a"(WA[12][1]) : "v"(whi) : "memory");
    asm volatile("ds_read_b128 %0, %1 offset:22528" : "=a"(WA[12][2]) : "v"(whi) : "memory");
    asm volatile("buffer_load_dwordx4 %0, %1, %2 offen offset:0 sc1 lds" :: "v"(hv1), "s"(rS), "s"(sh) : "memory");
    sh = 4194304 + (c*88 + kss + 5)*8192;
    asm volatile("" : "+s"(sh));
    asm volatile("s_add_u32 m0, %0, 53248" :: "s"(lh) : "scc", "memory");
    asm volatile("v_mfma_f32_16x16x32_bf16 %0, %1, %2, %0" : "+v"(o[1][2]) : "a"(WA[11][1]), "v"(HX1[2]));
    asm volatile("ds_read_b128 %0, %1 offset:23552" : "=a"(WA[12][3]) : "v"(whi) : "memory");
    asm volatile("ds_read_b128 %0, %1 offset:24576" : "=a"(WA[12][4]) : "v"(whi) : "memory");
    asm volatile("buffer_load_dwordx4 %0, %1, %2 offen offset:0 sc1 lds" :: "v"(hv0), "s"(rS), "s"(sh) : "memory");
    asm volatile("s_addk_i32 m0, 0x400" ::: "scc", "memory");
    asm volatile("v_mfma_f32_16x16x32_bf16 %0, %1, %2, %0" : "+v"(o[2][3]) : "a"(WA[11][2]), "v"(HX1[3]));
    asm volatile("ds_read_b64_tr_b16 %0, %1 offset:1024" : "=v"(tl[1]) : "v"(ht[0]) : "memory");
    asm volatile("ds_read_b64_tr_b16 %0, %1 offset:1024" : "=v"(th[1]) : "v"(ht[1]) : "memory");
    HK1[12] = cat(tl[1], th[1]);
    asm volatile("buffer_load_dwordx4 %0, %1, %2 offen offset:0 sc1 lds" :: "v"(hv1), "s"(rS), "s"(sh) : "memory");
    sw = swb + (kss + 8 + wm*(1u))*40960;
    asm volatile("" : "+s"(sw));
    asm volatile("s_add_u32 m0, %0, 153600" :: "s"(lw2) : "scc", "memory");
    asm volatile("v_mfma_f32_16x16x32_bf16 %0, %1, %2, %0" : "+v"(o[2][2]) : "a"(WA[11][2]), "v"(HX1[2]));
    asm volatile("ds_read_b64_tr_b16 %0, %1 offset:0" : "=v"(tl[0]) : "v"(ht[0]) : "memory");
    asm volatile("ds_read_b64_tr_b16 %0, %1 offset:0" : "=v"(th[0]) : "v"(ht[1]) : "memory");
    HK0[12] = cat(tl[0], th[0]);
    asm volatile("buffer_load_dwordx4 %0, %1, %2 offen offset:0 sc1 lds" :: "v"(vl16), "s"(rS), "s"(sw) : "memory");
    asm volatile("v_mfma_f32_16x16x32_bf16 %0, %1, %2, %0" : "+v"(o[3][3]) : "a"(WA[11][3]), "v"(HX1[3]));
    asm volatile("buffer_load_dwordx4 %0, %1, %2 offen offset:1024 sc1 lds" :: "v"(vl16), "s"(rS), "s"(sw) : "memory");
    asm volatile("v_mfma_f32_16x16x32_bf16 %0, %1, %2, %0" : "+v"(o[3][2]) : "a"(WA[11][3]), "v"(HX1[2]));
    asm volatile("buffer_load_dwordx4 %0, %1, %2 offen offset:2048 sc1 lds" :: "v"(vl16), "s"(rS), "s"(sw) : "memory");
    asm volatile("v_mfma_f32_16x16x32_bf16 %0, %1, %2, %0" : "+v"(o[4][3]) : "a"(WA[11][4]), "v"(HX1[3]));
    asm volatile("buffer_load_dwordx4 %0, %1, %2 offen offset:3072 sc1 lds" :: "v"(vl16), "s"(rS), "s"(sw) : "memory");
    asm volatile("s_addk_i32 m0, 0x1000" ::: "scc", "memory");
    asm volatile("v_mfma_f32_16x16x32_bf16 %0, %1, %2, %0" : "+v"(o[4][2]) : "a"(WA[11][4]), "v"(HX1[2]));
    asm volatile("buffer_load_dwordx4 %0, %1, %2 offen offset:0 sc1 lds" :: "v"(vl16h), "s"(rS), "s"(sw) : "memory");
    asm volatile("s_waitcnt lgkmcnt(0)" ::: "memory");
    asm volatile("v_mfma_f32_16x16x32_bf16 %0, %1, %2, %0" : "+v"(o[0][3]) : "a"(WA[12][0]), "v"(HX0[3]));
    asm volatile("ds_read_b64_tr_b16 %0, %1 offset:11264" : "=v"(tl[3]) : "v"(ht[0]) : "memory");
    asm volatile("ds_read_b64_tr_b16 %0, %1 offset:11264" : "=v"(th[3]) : "v"(ht[1]) : "memory");
    HX1[3] = cat(tl[3], th[3]);
    asm volatile("ds_read_b64_tr_b16 %0, %1 offset:10240" : "=v"(tl[2]) : "v"(ht[0]) : "memory");
    asm volatile("v_mfma_f32_16x16x32_bf16 %0, %1, %2, %0" : "+v"(o[0][2]) : "a"(WA[12][0]), "v"(HX0[2]));
    asm volatile("ds_read_b64_tr_b16 %0, %1 offset:10240" : "=v"(th[2]) : "v"(ht[1]) : "memory");
    HX1[2] = cat(tl[2], th[2]);
    asm volatile("ds_read_b128 %0, %1 offset:10240" : "=a"(WA[13][0]) : "v"(whi) : "memory");
    asm volatile("v_mfma_f32_16x16x32_bf16 %0, %1, %2, %0" : "+v"(o[1][3]) : "a"(WA[12][1]), "v"(HX0[3]));
    asm volatile("ds_read_b128 %0, %1 offset:11264" : "=a"(WA[13][1]) : "v"(whi) : "memory");
    asm volatile("ds_read_b128 %0, %1 offset:12288" : "=a"(WA[13][2]) : "v"(whi) : "memory");
    asm volatile("v_mfma_f32_16x16x32_bf16 %0, %1, %2, %0" : "+v"(o[1][2]) : "a"(WA[12][1]), "v"(HX0[2]));
    asm volatile("ds_read_b128 %0, %1 offset:13312" : "=a"(WA[13][3]) : "v"(whi) : "memory");
    asm volatile("ds_read_b128 %0, %1 offset:14336" : "=a"(WA[13][4]) : "v"(whi) : "memory");
    asm volatile("v_mfma_f32_16x16x32_bf16 %0, %1, %2, %0" : "+v"(o[2][3]) : "a"(WA[12][2]), "v"(HX0[3]));
    asm volatile("ds_read_b64_tr_b16 %0, %1 offset:9216" : "=v"(tl[1]) : "v"(ht[0]) : "memory");
    asm volatile("ds_read_b64_tr_b16 %0, %1 offset:9216" : "=v"(th[1]) : "v"(ht[1]) : "memory");
    HK1[13] = cat(tl[1], th[1]);
    asm volatile("v_mfma_f32_16x16x32_bf16 %0, %1, %2, %0" : "+v"(o[2][2]) : "a"(WA[12][2]), "v"(HX0[2]));
    asm volatile("ds_read_b64_tr_b16 %0, %1 offset:8192" : "=v"(tl[0]) : "v"(ht[0]) : "memory");
    asm volatile("ds_read_b64_tr_b16 %0, %1 offset:8192" : "=v"(th[0]) : "v"(ht[1]) : "memory");
    HK0[13] = cat(tl[0], th[0]);
    asm volatile("v_mfma_f32_16x16x32_bf16 %0, %1, %2, %0" : "+v"(o[3][3]) : "a"(WA[12][3]), "v"(HX0[3]));
    asm volatile("v_mfma_f32_16x16x32_bf16 %0, %1, %2, %0" : "+v"(o[3][2]) : "a"(WA[12][3]), "v"(HX0[2]));
    asm volatile("v_mfma_f32_16x16x32_bf16 %0, %1, %2, %0" : "+v"(o[4][3]) : "a"(WA[12][4]), "v"(HX0[3]));
    asm volatile("v_mfma_f32_16x16x32_bf16 %0, %1, %2, %0" : "+v"(o[4][2]) : "a"(WA[12][4]), "v"(HX0[2]));
    asm volatile("s_waitcnt vmcnt(14) lgkmcnt(0)\n\ts_barrier" ::: "memory");
    asm volatile("v_mfma_f32_16x16x32_bf16 %0, %1, %2, %0" : "+v"(o[0][3]) : "a"(WA[13][0]), "v"(HX1[3]));
    asm volatile("ds_read_b64_tr_b16 %0, %1 offset:19456" : "=v"(tl[3]) : "v"(ht[0]) : "memory");
    asm volatile("ds_read_b64_tr_b16 %0, %1 offset:19456" : "=v"(th[3]) : "v"(ht[1]) : "memory");
    HX0[3] = cat(tl[3], th[3]);
    asm volatile("ds_read_b64_tr_b16 %0, %1 offset:18432" : "=v"(tl[2]) : "v"(ht[0]) : "memory");
    sh = 4194304 + (c*88 + kss + 6)*8192;
    asm volatile("" : "+s"(sh));
    asm volatile("s_add_u32 m0, %0, 12288" :: "s"(lh) : "scc", "memory");
    asm volatile("v_mfma_f32_16x16x32_bf16 %0, %1, %2, %0" : "+v"(o[0][2]) : "a"(WA[13][0]), "v"(HX1[2]));
    asm volatile("ds_read_b64_tr_b16 %0, %1 offset:18432" : "=v"(th[2]) : "v"(ht[1]) : "memory");
    HX0[2] = cat(tl[2], th[2]);
    asm volatile("ds_read_b128 %0, %1 offset:0" : "=a"(WA[14][0]) : "v"(whi) : "memory");
    asm volatile("buffer_load_dwordx4 %0, %1, %2 offen offset:0 sc1 lds" :: "v"(hv0), "s"(rS), "s"(sh) : "memory");
    asm volatile("v_mfma_f32_16x16x32_bf16 %0, %1, %2, %0" : "+v"(o[1][3]) : "a"(WA[13][1]), "v"(HX1[3]));
    asm volatile("ds_read_b128 %0, %1 offset:1024" : "=a"(WA[14][1]) : "v"(whi) : "memory");
    asm volatile("ds_read_b128 %0, %1 offset:2048" : "=a"(WA[14][2]) : "v"(whi) : "memory");
    asm volatile("v_mfma_f32_16x16x32_bf16 %0, %1, %2, %0" : "+v"(o[1][2]) : "a"(WA[13][1]), "v"(HX1[2]));
    asm volatile("ds_read_b128 %0, %1 offset:3072" : "=a"(WA[14][3]) : "v"(whi) : "memory");
    asm volatile("ds_read_b128 %0, %1 offset:4096" : "=a"(WA[14][4]) : "v"(whi) : "memory");
    asm volatile("s_addk_i32 m0, 0x400" ::: "scc", "memory");
    asm volatile("v_mfma_f32_16x16x32_bf16 %0, %1, %2, %0" : "+v"(o[2][3]) : "a"(WA[13][2]), "v"(HX1[3]));
    asm volatile("ds_read_b64_tr_b16 %0, %1 offset:17408" : "=v"(tl[1]) : "v"(ht[0]) : "memory");
    asm volatile("ds_read_b64_tr_b16 %0, %1 offset:17408" : "=v"(th[1]) : "v"(ht[1]) : "memory");
    HK1[14] = cat(tl[1], th[1]);
    asm volatile("buffer_load_dwordx4 %0, %1, %2 offen offset:0 sc1 lds" :: "v"(hv1), "s"(rS), "s"(sh) : "memory");
    asm volatile("v_mfma_f32_16x16x32_bf16 %0, %1, %2, %0" : "+v"(o[2][2]) : "a"(WA[13][2]), "v"(HX1[2]));
    asm volatile("ds_read_b64_tr_b16 %0, %1 offset:16384" : "=v"(tl[0]) : "v"(ht[0]) : "memory");
    asm volatile("ds_read_b64_tr_b16 %0, %1 offset:16384" : "=v"(th[0]) : "v"(ht[1]) : "memory");
    HK0[14] = cat(tl[0], th[0]);
    asm volatile("v_mfma_f32_16x16x32_bf16 %0, %1, %2, %0" : "+v"(o[3][3]) : "a"(WA[13][3]), "v"(HX1[3]));
    sh = 4194304 + (c*88 + kss + 7)*8192;
    asm volatile("" : "+s"(sh));
    asm volatile("s_add_u32 m0, %0, 20480" :: "s"(lh) : "scc", "memory");
    asm volatile("v_mfma_f32_16x16x32_bf16 %0, %1, %2, %0" : "+v"(o[3][2]) : "a"(WA[13][3]), "v"(HX1[2]));
    asm volatile("buffer_load_dwordx4 %0, %1, %2 offen offset:0 sc1 lds" :: "v"(hv0), "s"(rS), "s"(sh) : "memory");
    asm volatile("v_mfma_f32_16x16x32_bf16 %0, %1, %2, %0" : "+v"(o[4][3]) : "a"(WA[13][4]), "v"(HX1[3]));
    asm volatile("v_mfma_f32_16x16x32_bf16 %0, %1, %2, %0" : "+v"(o[4][2]) : "a"(WA[13][4]), "v"(HX1[2]));
    asm volatile("s_addk_i32 m0, 0x400" ::: "scc", "memory");
    asm volatile("s_waitcnt lgkmcnt(0)" ::: "memory");
    asm volatile("v_mfma_f32_16x16x32_bf16 %0, %1, %2, %0" : "+v"(o[0][3]) : "a"(WA[14][0]), "v"(HX0[3]));
    asm volatile("ds_read_b64_tr_b16 %0, %1 offset:27648" : "=v"(tl[3]) : "v"(ht[0]) : "memory");
    asm volatile("ds_read_b64_tr_b16 %0, %1 offset:27648" : "=v"(th[3]) : "v"(ht[1]) : "memory");
    HX1[3] = cat(tl[3], th[3]);
    asm volatile("ds_read_b64_tr_b16 %0, %1 offset:26624" : "=v"(tl[2]) : "v"(ht[0]) : "memory");
    asm volatile("buffer_load_dwordx4 %0, %1, %2 offen offset:0 sc1 lds" :: "v"(hv1), "s"(rS), "s"(sh) : "memory");
    asm volatile("v_mfma_f32_16x16x32_bf16 %0, %1, %2, %0" : "+v"(o[0][2]) : "a"(WA[14][0]), "v"(HX0[2]));
    asm volatile("ds_read_b64_tr_b16 %0, %1 offset:26624" : "=v"(th[2]) : "v"(ht[1]) : "memory");
    HX1[2] = cat(tl[2], th[2]);
    asm volatile("ds_read_b128 %0, %1 offset:40960" : "=a"(WA[15][0]) : "v"(wlo) : "memory");
    asm volatile("v_mfma_f32_16x16x32_bf16 %0, %1, %2, %0" : "+v"(o[1][3]) : "a"(WA[14][1]), "v"(HX0[3]));
    asm volatile("ds_read_b128 %0, %1 offset:41984" : "=a"(WA[15][1]) : "v"(wlo) : "memory");
    asm volatile("ds_read_b128 %0, %1 offset:43008" : "=a"(WA[15][2]) : "v"(wlo) : "memory");
    asm volatile("v_mfma_f32_16x16x32_bf16 %0, %1, %2, %0" : "+v"(o[1][2]) : "a"(WA[14][1]), "v"(HX0[2]));
    asm volatile("ds_read_b128 %0, %1 offset:44032" : "=a"(WA[15][3]) : "v"(wlo) : "memory");
    asm volatile("ds_read_b128 %0, %1 offset:45056" : "=a"(WA[15][4]) : "v"(wlo) : "memory");
    asm volatile("v_mfma_f32_16x16x32_bf16 %0, %1, %2, %0" : "+v"(o[2][3]) : "a"(WA[14][2]), "v"(HX0[3]));
    asm volatile("ds_read_b64_tr_b16 %0, %1 offset:25600" : "=v"(tl[1]) : "v"(ht[0]) : "memory");
    asm volatile("ds_read_b64_tr_b16 %0, %1 offset:25600" : "=v"(th[1]) : "v"(ht[1]) : "memory");
    HK1[15] = cat(tl[1], th[1]);
    asm volatile("v_mfma_f32_16x16x32_bf16 %0, %1, %2, %0" : "+v"(o[2][2]) : "a"(WA[14][2]), "v"(HX0[2]));
    asm volatile("ds_read_b64_tr_b16 %0, %1 offset:24576" : "=v"(tl[0]) : "v"(ht[0]) : "memory");
    asm volatile("ds_read_b64_tr_b16 %0, %1 offset:24576" : "=v"(th[0]) : "v"(ht[1]) : "memory");
    HK0[15] = cat(tl[0], th[0]);
    asm volatile("v_mfma_f32_16x16x32_bf16 %0, %1, %2, %0" : "+v"(o[3][3]) : "a"(WA[14][3]), "v"(HX0[3]));
    asm volatile("v_mfma_f32_16x16x32_bf16 %0, %1, %2, %0" : "+v"(o[3][2]) : "a"(WA[14][3]), "v"(HX0[2]));
    asm volatile("v_mfma_f32_16x16x32_bf16 %0, %1, %2, %0" : "+v"(o[4][3]) : "a"(WA[14][4]), "v"(HX0[3]));
    asm volatile("v_mfma_f32_16x16x32_bf16 %0, %1, %2, %0" : "+v"(o[4][2]) : "a"(WA[14][4]), "v"(HX0[2]));
    asm volatile("s_waitcnt vmcnt(9) lgkmcnt(0)\n\ts_barrier" ::: "memory");
    asm volatile("v_mfma_f32_16x16x32_bf16 %0, %1, %2, %0" : "+v"(o[0][3]) : "a"(WA[15][0]), "v"(HX1[3]));
    asm volatile("ds_read_b64_tr_b16 %0, %1 offset:35840" : "=v"(tl[3]) : "v"(ht[0]) : "memory");
    asm volatile("ds_read_b64_tr_b16 %0, %1 offset:35840" : "=v"(th[3]) : "v"(ht[1]) : "memory");
    HX0[3] = cat(tl[3], th[3]);
    asm volatile("ds_read_b64_tr_b16 %0, %1 offset:34816" : "=v"(tl[2]) : "v"(ht[0]) : "memory");
    sh = 4194304 + (c*88 + kss + 8)*8192;
    asm volatile("" : "+s"(sh));
    asm volatile("s_add_u32 m0, %0, 28672" :: "s"(lh) : "scc", "memory");
    asm volatile("v_mfma_f32_16x16x32_bf16 %0, %1, %2, %0" : "+v"(o[0][2]) : "a"(WA[15][0]), "v"(HX1[2]));
    asm volatile("ds_read_b64_tr_b16 %0, %1 offset:34816" : "=v"(th[2]) : "v"(ht[1]) : "memory");
    HX0[2] = cat(tl[2], th[2]);
    asm volatile("ds_read_b128 %0, %1 offset:30720" : "=a"(WA[16][0]) : "v"(wlo) : "memory");
    asm volatile("buffer_load_dwordx4 %0, %1, %2 offen offset:0 sc1 lds" :: "v"(hv0), "s"(rS), "s"(sh) : "memory");
    asm volatile("v_mfma_f32_16x16x32_bf16 %0, %1, %2, %0" : "+v"(o[1][3]) : "a"(WA[15][1]), "v"(HX1[3]));
    asm volatile("ds_read_b128 %0, %1 offset:31744" : "=a"(WA[16][1]) : "v"(wlo) : "memory");
    asm volatile("ds_read_b128 %0, %1 offset:32768" : "=a"(WA[16][2]) : "v"(wlo) : "memory");
    asm volatile("v_mfma_f32_16x16x32_bf16 %0, %1, %2, %0" : "+v"(o[1][2]) : "a"(WA[15][1]), "v"(HX1[2]));
    asm volatile("ds_read_b128 %0, %1 offset:33792" : "=a"(WA[16][3]) : "v"(wlo) : "memory");
    asm volatile("ds_read_b128 %0, %1 offset:34816" : "=a"(WA[16][4]) : "v"(wlo) : "memory");
    asm volatile("s_addk_i32 m0, 0x400" ::: "scc", "memory");
    asm volatile("v_mfma_f32_16x16x32_bf16 %0, %1, %2, %0" : "+v"(o[2][3]) : "a"(WA[15][2]), "v"(HX1[3]));
    asm volatile("ds_read_b64_tr_b16 %0, %1 offset:33792" : "=v"(tl[1]) : "v"(ht[0]) : "memory");
    asm volatile("ds_read_b64_tr_b16 %0, %1 offset:33792" : "=v"(th[1]) : "v"(ht[1]) : "memory");
    HK1[16] = cat(tl[1], th[1]);
    asm volatile("buffer_load_dwordx4 %0, %1, %2 offen offset:0 sc1 lds" :: "v"(hv1), "s"(rS), "s"(sh) : "memory");
    asm volatile("v_mfma_f32_16x16x32_bf16 %0, %1, %2, %0" : "+v"(o[2][2]) : "a"(WA[15][2]), "v"(HX1[2]));
    asm volatile("ds_read_b64_tr_b16 %0, %1 offset:32768" : "=v"(tl[0]) : "v"(ht[0]) : "memory");
    asm volatile("ds_read_b64_tr_b16 %0, %1 offset:32768" : "=v"(th[0]) : "v"(ht[1]) : "memory");
    HK0[16] = cat(tl[0], th[0]);
    asm volatile("v_mfma_f32_16x16x32_bf16 %0, %1, %2, %0" : "+v"(o[3][3]) : "a"(WA[15][3]), "v"(HX1[3]));
    sh = 4194304 + (c*88 + kss + 9)*8192;
    asm volatile("" : "+s"(sh));
    asm volatile("s_add_u32 m0, %0, 36864" :: "s"(lh) : "scc", "memory");
    asm volatile("v_mfma_f32_16x16x32_bf16 %0, %1, %2, %0" : "+v"(o[3][2]) : "a"(WA[15][3]), "v"(HX1[2]));
    asm volatile("buffer_load_dwordx4 %0, %1, %2 offen offset:0 sc1 lds" :: "v"(hv0), "s"(rS), "s"(sh) : "memory");
    asm volatile("v_mfma_f32_16x16x32_bf16 %0, %1, %2, %0" : "+v"(o[4][3]) : "a"(WA[15][4]), "v"(HX1[3]));
    asm volatile("v_mfma_f32_16x16x32_bf16 %0, %1, %2, %0" : "+v"(o[4][2]) : "a"(WA[15][4]), "v"(HX1[2]));
    asm volatile("s_addk_i32 m0, 0x400" ::: "scc", "memory");
    asm volatile("s_waitcnt lgkmcnt(0)" ::: "memory");
    asm volatile("v_mfma_f32_16x16x32_bf16 %0, %1, %2, %0" : "+v"(o[0][3]) : "a"(WA[16][0]), "v"(HX0[3]));
    asm volatile("ds_read_b64_tr_b16 %0, %1 offset:44032" : "=v"(tl[3]) : "v"(ht[0]) : "memory");
    asm volatile("ds_read_b64_tr_b16 %0, %1 offset:44032" : "=v"(th[3]) : "v"(ht[1]) : "memory");
    HX1[3] = cat(tl[3], th[3]);
    asm volatile("ds_read_b64_tr_b16 %0, %1 offset:43008" : "=v"(tl[2]) : "v"(ht[0]) : "memory");
    asm volatile("buffer_load_dwordx4 %0, %1, %2 offen offset:0 sc1 lds" :: "v"(hv1), "s"(rS), "s"(sh) : "memory");
    asm volatile("v_mfma_f32_16x16x32_bf16 %0, %1, %2, %0" : "+v"(o[0][2]) : "a"(WA[16][0]), "v"(HX0[2]));
    asm volatile("ds_read_b64_tr_b16 %0, %1 offset:43008" : "=v"(th[2]) : "v"(ht[1]) : "memory");
    HX1[2] = cat(tl[2], th[2]);
    asm volatile("ds_read_b128 %0, %1 offset:20480" : "=a"(WA[17][0]) : "v"(wlo) : "memory");
    asm volatile("v_mfma_f32_16x16x32_bf16 %0, %1, %2, %0" : "+v"(o[1][3]) : "a"(WA[16][1]), "v"(HX0[3]));
    asm volatile("ds_read_b128 %0, %1 offset:21504" : "=a"(WA[17][1]) : "v"(wlo) : "memory");
    asm volatile("ds_read_b128 %0, %1 offset:22528" : "=a"(WA[17][2]) : "v"(wlo) : "memory");
    asm volatile("v_mfma_f32_16x16x32_bf16 %0, %1, %2, %0" : "+v"(o[1][2]) : "a"(WA[16][1]), "v"(HX0[2]));
    asm volatile("ds_read_b128 %0, %1 offset:23552" : "=a"(WA[17][3]) : "v"(wlo) : "memory");
    asm volatile("ds_read_b128 %0, %1 offset:24576" : "=a"(WA[17][4]) : "v"(wlo) : "memory");
    asm volatile("v_mfma_f32_16x16x32_bf16 %0, %1, %2, %0" : "+v"(o[2][3]) : "a"(WA[16][2]), "v"(HX0[3]));
    asm volatile("ds_read_b64_tr_b16 %0, %1 offset:41984" : "=v"(tl[1]) : "v"(ht[0]) : "memory");
    asm volatile("ds_read_b64_tr_b16 %0, %1 offset:41984" : "=v"(th[1]) : "v"(ht[1]) : "memory");
    HK1[17] = cat(tl[1], th[1]);
    asm volatile("v_mfma_f32_16x16x32_bf16 %0, %1, %2, %0" : "+v"(o[2][2]) : "a"(WA[16][2]), "v"(HX0[2]));
    asm volatile("ds_read_b64_tr_b16 %0, %1 offset:40960" : "=v"(tl[0]) : "v"(ht[0]) : "memory");
    asm volatile("ds_read_b64_tr_b16 %0, %1 offset:40960" : "=v"(th[0]) : "v"(ht[1]) : "memory");
    HK0[17] = cat(tl[0], th[0]);
    asm volatile("v_mfma_f32_16x16x32_bf16 %0, %1, %2, %0" : "+v"(o[3][3]) : "a"(WA[16][3]), "v"(HX0[3]));
    asm volatile("v_mfma_f32_16x16x32_bf16 %0, %1, %2, %0" : "+v"(o[3][2]) : "a"(WA[16][3]), "v"(HX0[2]));
    asm volatile("v_mfma_f32_16x16x32_bf16 %0, %1, %2, %0" : "+v"(o[4][3]) : "a"(WA[16][4]), "v"(HX0[3]));
    asm volatile("v_mfma_f32_16x16x32_bf16 %0, %1, %2, %0" : "+v"(o[4][2]) : "a"(WA[16][4]), "v"(HX0[2]));
    asm volatile("s_waitcnt vmcnt(4) lgkmcnt(0)\n\ts_barrier" ::: "memory");
    asm volatile("v_mfma_f32_16x16x32_bf16 %0, %1, %2, %0" : "+v"(o[0][3]) : "a"(WA[17][0]), "v"(HX1[3]));
    asm volatile("ds_read_b64_tr_b16 %0, %1 offset:3072" : "=v"(tl[3]) : "v"(ht[0]) : "memory");
    asm volatile("ds_read_b64_tr_b16 %0, %1 offset:3072" : "=v"(th[3]) : "v"(ht[1]) : "memory");
    HX0[3] = cat(tl[3], th[3]);
    asm volatile("ds_read_b64_tr_b16 %0, %1 offset:2048" : "=v"(tl[2]) : "v"(ht[0]) : "memory");
    asm volatile("v_mfma_f32_16x16x32_bf16 %0, %1, %2, %0" : "+v"(o[0][2]) : "a"(WA[17][0]), "v"(HX1[2]));
    asm volatile("ds_read_b64_tr_b16 %0, %1 offset:2048" : "=v"(th[2]) : "v"(ht[1]) : "memory");
    HX0[2] = cat(tl[2], th[2]);
    asm volatile("ds_read_b128 %0, %1 offset:10240" : "=a"(WA[18][0]) : "v"(wlo) : "memory");
    asm volatile("buffer_load_dwordx2 %0, %1, %2, 0 offen sc1" : "=v"(es) : "v"(40960 + tok*8), "s"(rS) : "memory");
    asm volatile("v_mfma_f32_16x16x32_bf16 %0, %1, %2, %0" : "+v"(o[1][3]) : "a"(WA[17][1]), "v"(HX1[3]));
    asm volatile("ds_read_b128 %0, %1 offset:11264" : "=a"(WA[18][1]) : "v"(wlo) : "memory");
    asm volatile("ds_read_b128 %0, %1 offset:12288" : "=a"(WA[18][2]) : "v"(wlo) : "memory");
    asm volatile("v_mfma_f32_16x16x32_bf16 %0, %1, %2, %0" : "+v"(o[1][2]) : "a"(WA[17][1]), "v"(HX1[2]));
    asm volatile("ds_read_b128 %0, %1 offset:13312" : "=a"(WA[18][3]) : "v"(wlo) : "memory");
    asm volatile("ds_read_b128 %0, %1 offset:14336" : "=a"(WA[18][4]) : "v"(wlo) : "memory");
    asm volatile("buffer_load_dwordx2 %0, %1, %2, 0 offen offset:0" : "=v"(ex[0]) : "v"(xvo), "s"(rX) : "memory");
    asm volatile("v_mfma_f32_16x16x32_bf16 %0, %1, %2, %0" : "+v"(o[2][3]) : "a"(WA[17][2]), "v"(HX1[3]));
    asm volatile("ds_read_b64_tr_b16 %0, %1 offset:1024" : "=v"(tl[1]) : "v"(ht[0]) : "memory");
    asm volatile("ds_read_b64_tr_b16 %0, %1 offset:1024" : "=v"(th[1]) : "v"(ht[1]) : "memory");
    HK1[18] = cat(tl[1], th[1]);
    asm volatile("v_mfma_f32_16x16x32_bf16 %0, %1, %2, %0" : "+v"(o[2][2]) : "a"(WA[17][2]), "v"(HX1[2]));
    asm volatile("ds_read_b64_tr_b16 %0, %1 offset:0" : "=v"(tl[0]) : "v"(ht[0]) : "memory");
    asm volatile("ds_read_b64_tr_b16 %0, %1 offset:0" : "=v"(th[0]) : "v"(ht[1]) : "memory");
    HK0[18] = cat(tl[0], th[0]);
    asm volatile("buffer_load_dwordx2 %0, %1, %2, 0 offen offset:32" : "=v"(ex[1]) : "v"(xvo), "s"(rX) : "memory");
    asm volatile("v_mfma_f32_16x16x32_bf16 %0, %1, %2, %0" : "+v"(o[3][3]) : "a"(WA[17][3]), "v"(HX1[3]));
    asm volatile("v_mfma_f32_16x16x32_bf16 %0, %1, %2, %0" : "+v"(o[3][2]) : "a"(WA[17][3]), "v"(HX1[2]));
    asm volatile("buffer_load_dwordx2 %0, %1, %2, 0 offen offset:64" : "=v"(ex[2]) : "v"(xvo), "s"(rX) : "memory");
    asm volatile("v_mfma_f32_16x16x32_bf16 %0, %1, %2, %0" : "+v"(o[4][3]) : "a"(WA[17][4]), "v"(HX1[3]));
    asm volatile("v_mfma_f32_16x16x32_bf16 %0, %1, %2, %0" : "+v"(o[4][2]) : "a"(WA[17][4]), "v"(HX1[2]));
    asm volatile("buffer_load_dwordx2 %0, %1, %2, 0 offen offset:96" : "=v"(ex[3]) : "v"(xvo), "s"(rX) : "memory");
    asm volatile("s_waitcnt lgkmcnt(0)" ::: "memory");
    asm volatile("v_mfma_f32_16x16x32_bf16 %0, %1, %2, %0" : "+v"(o[0][3]) : "a"(WA[18][0]), "v"(HX0[3]));
    asm volatile("ds_read_b64_tr_b16 %0, %1 offset:11264" : "=v"(tl[3]) : "v"(ht[0]) : "memory");
    asm volatile("ds_read_b64_tr_b16 %0, %1 offset:11264" : "=v"(th[3]) : "v"(ht[1]) : "memory");
    HX1[3] = cat(tl[3], th[3]);
    asm volatile("ds_read_b64_tr_b16 %0, %1 offset:10240" : "=v"(tl[2]) : "v"(ht[0]) : "memory");
    asm volatile("v_mfma_f32_16x16x32_bf16 %0, %1, %2, %0" : "+v"(o[0][2]) : "a"(WA[18][0]), "v"(HX0[2]));
    asm volatile("ds_read_b64_tr_b16 %0, %1 offset:10240" : "=v"(th[2]) : "v"(ht[1]) : "memory");
    HX1[2] = cat(tl[2], th[2]);
    asm volatile("ds_read_b128 %0, %1 offset:0" : "=a"(WA[19][0]) : "v"(wlo) : "memory");
    asm volatile("buffer_load_dwordx2 %0, %1, %2, 0 offen offset:128" : "=v"(ex[4]) : "v"(xvo), "s"(rX) : "memory");
    asm volatile("v_mfma_f32_16x16x32_bf16 %0, %1, %2, %0" : "+v"(o[1][3]) : "a"(WA[18][1]), "v"(HX0[3]));
    asm volatile("ds_read_b128 %0, %1 offset:1024" : "=a"(WA[19][1]) : "v"(wlo) : "memory");
    asm volatile("ds_read_b128 %0, %1 offset:2048" : "=a"(WA[19][2]) : "v"(wlo) : "memory");
    asm volatile("v_mfma_f32_16x16x32_bf16 %0, %1, %2, %0" : "+v"(o[1][2]) : "a"(WA[18][1]), "v"(HX0[2]));
    asm volatile("ds_read_b128 %0, %1 offset:3072" : "=a"(WA[19][3]) : "v"(wlo) : "memory");
    asm volatile("ds_read_b128 %0, %1 offset:4096" : "=a"(WA[19][4]) : "v"(wlo) : "memory");
    asm volatile("v_mfma_f32_16x16x32_bf16 %0, %1, %2, %0" : "+v"(o[2][3]) : "a"(WA[18][2]), "v"(HX0[3]));
    asm volatile("ds_read_b64_tr_b16 %0, %1 offset:9216" : "=v"(tl[1]) : "v"(ht[0]) : "memory");
    asm volatile("ds_read_b64_tr_b16 %0, %1 offset:9216" : "=v"(th[1]) : "v"(ht[1]) : "memory");
    HK1[19] = cat(tl[1], th[1]);
    asm volatile("v_mfma_f32_16x16x32_bf16 %0, %1, %2, %0" : "+v"(o[2][2]) : "a"(WA[18][2]), "v"(HX0[2]));
    asm volatile("ds_read_b64_tr_b16 %0, %1 offset:8192" : "=v"(tl[0]) : "v"(ht[0]) : "memory");
    asm volatile("ds_read_b64_tr_b16 %0, %1 offset:8192" : "=v"(th[0]) : "v"(ht[1]) : "memory");
    HK0[19] = cat(tl[0], th[0]);
    asm volatile("v_mfma_f32_16x16x32_bf16 %0, %1, %2, %0" : "+v"(o[3][3]) : "a"(WA[18][3]), "v"(HX0[3]));
    asm volatile("v_mfma_f32_16x16x32_bf16 %0, %1, %2, %0" : "+v"(o[3][2]) : "a"(WA[18][3]), "v"(HX0[2]));
    asm volatile("v_mfma_f32_16x16x32_bf16 %0, %1, %2, %0" : "+v"(o[4][3]) : "a"(WA[18][4]), "v"(HX0[3]));
    asm volatile("v_mfma_f32_16x16x32_bf16 %0, %1, %2, %0" : "+v"(o[4][2]) : "a"(WA[18][4]), "v"(HX0[2]));
    asm volatile("s_waitcnt vmcnt(6) lgkmcnt(0)\n\ts_barrier" ::: "memory");
    asm volatile("v_mfma_f32_16x16x32_bf16 %0, %1, %2, %0" : "+v"(o[0][3]) : "a"(WA[19][0]), "v"(HX1[3]));
    asm volatile("ds_read_b64_tr_b16 %0, %1 offset:19456" : "=v"(tl[3]) : "v"(ht[0]) : "memory");
    asm volatile("ds_read_b64_tr_b16 %0, %1 offset:19456" : "=v"(th[3]) : "v"(ht[1]) : "memory");
    HX0[3] = cat(tl[3], th[3]);
    asm volatile("ds_read_b64_tr_b16 %0, %1 offset:18432" : "=v"(tl[2]) : "v"(ht[0]) : "memory");
    asm volatile("v_mfma_f32_16x16x32_bf16 %0, %1, %2, %0" : "+v"(o[0][2]) : "a"(WA[19][0]), "v"(HX1[2]));
    asm volatile("ds_read_b64_tr_b16 %0, %1 offset:18432" : "=v"(th[2]) : "v"(ht[1]) : "memory");
    HX0[2] = cat(tl[2], th[2]);
    asm volatile("ds_read_b128 %0, %1 offset:40960" : "=a"(WA[20][0]) : "v"(whi) : "memory");
    asm volatile("v_mfma_f32_16x16x32_bf16 %0, %1, %2, %0" : "+v"(o[1][3]) : "a"(WA[19][1]), "v"(HX1[3]));
    asm volatile("ds_read_b128 %0, %1 offset:41984" : "=a"(WA[20][1]) : "v"(whi) : "memory");
    asm volatile("ds_read_b128 %0, %1 offset:43008" : "=a"(WA[20][2]) : "v"(whi) : "memory");
    asm volatile("v_mfma_f32_16x16x32_bf16 %0, %1, %2, %0" : "+v"(o[1][2]) : "a"(WA[19][1]), "v"(HX1[2]));
    asm volatile("ds_read_b128 %0, %1 offset:44032" : "=a"(WA[20][3]) : "v"(whi) : "memory");
    asm volatile("ds_read_b128 %0, %1 offset:45056" : "=a"(WA[20][4]) : "v"(whi) : "memory");
    asm volatile("v_mfma_f32_16x16x32_bf16 %0, %1, %2, %0" : "+v"(o[2][3]) : "a"(WA[19][2]), "v"(HX1[3]));
    asm volatile("ds_read_b64_tr_b16 %0, %1 offset:17408" : "=v"(tl[1]) : "v"(ht[0]) : "memory");
    asm volatile("ds_read_b64_tr_b16 %0, %1 offset:17408" : "=v"(th[1]) : "v"(ht[1]) : "memory");
    HK1[20] = cat(tl[1], th[1]);
    asm volatile("v_mfma_f32_16x16x32_bf16 %0, %1, %2, %0" : "+v"(o[2][2]) : "a"(WA[19][2]), "v"(HX1[2]));
    asm volatile("ds_read_b64_tr_b16 %0, %1 offset:16384" : "=v"(tl[0]) : "v"(ht[0]) : "memory");
    asm volatile("ds_read_b64_tr_b16 %0, %1 offset:16384" : "=v"(th[0]) : "v"(ht[1]) : "memory");
    HK0[20] = cat(tl[0], th[0]);
    asm volatile("v_mfma_f32_16x16x32_bf16 %0, %1, %2, %0" : "+v"(o[3][3]) : "a"(WA[19][3]), "v"(HX1[3]));
    asm volatile("v_mfma_f32_16x16x32_bf16 %0, %1, %2, %0" : "+v"(o[3][2]) : "a"(WA[19][3]), "v"(HX1[2]));
    asm volatile("v_mfma_f32_16x16x32_bf16 %0, %1, %2, %0" : "+v"(o[4][3]) : "a"(WA[19][4]), "v"(HX1[3]));
    asm volatile("v_mfma_f32_16x16x32_bf16 %0, %1, %2, %0" : "+v"(o[4][2]) : "a"(WA[19][4]), "v"(HX1[2]));
    asm volatile("s_waitcnt lgkmcnt(0)" ::: "memory");
    const unsigned pd = (idx << 3) | (xc & 1);
    asm volatile("v_mfma_f32_16x16x32_bf16 %0, %1, %2, %0" : "+v"(o[0][3]) : "a"(WA[20][0]), "v"(HX0[3]));
    asm volatile("ds_read_b64_tr_b16 %0, %1 offset:27648" : "=v"(tl[3]) : "v"(ht[0]) : "memory");
    asm volatile("ds_read_b64_tr_b16 %0, %1 offset:27648" : "=v"(th[3]) : "v"(ht[1]) : "memory");
    HX1[3] = cat(tl[3], th[3]);
    asm volatile("ds_read_b64_tr_b16 %0, %1 offset:26624" : "=v"(tl[2]) : "v"(ht[0]) : "memory");
    asm volatile("v_mfma_f32_16x16x32_bf16 %0, %1, %2, %0" : "+v"(o[1][3]) : "a"(WA[20][1]), "v"(HX0[3]));
    asm volatile("ds_read_b64_tr_b16 %0, %1 offset:26624" : "=v"(th[2]) : "v"(ht[1]) : "memory");
    HX1[2] = cat(tl[2], th[2]);
    asm volatile("ds_read_b128 %0, %1 offset:30720" : "=a"(WA[21][0]) : "v"(whi) : "memory");
    asm volatile("v_mfma_f32_16x16x32_bf16 %0, %1, %2, %0" : "+v"(o[2][3]) : "a"(WA[20][2]), "v"(HX0[3]));
    asm volatile("ds_read_b128 %0, %1 offset:31744" : "=a"(WA[21][1]) : "v"(whi) : "memory");
    asm volatile("ds_read_b128 %0, %1 offset:32768" : "=a"(WA[21][2]) : "v"(whi) : "memory");
    asm volatile("v_mfma_f32_16x16x32_bf16 %0, %1, %2, %0" : "+v"(o[3][3]) : "a"(WA[20][3]), "v"(HX0[3]));
    asm volatile("ds_read_b128 %0, %1 offset:33792" : "=a"(WA[21][3]) : "v"(whi) : "memory");
    asm volatile("ds_read_b128 %0, %1 offset:34816" : "=a"(WA[21][4]) : "v"(whi) : "memory");
    asm volatile("v_mfma_f32_16x16x32_bf16 %0, %1, %2, %0" : "+v"(o[4][3]) : "a"(WA[20][4]), "v"(HX0[3]));
    asm volatile("ds_read_b64_tr_b16 %0, %1 offset:25600" : "=v"(tl[1]) : "v"(ht[0]) : "memory");
    asm volatile("ds_read_b64_tr_b16 %0, %1 offset:25600" : "=v"(th[1]) : "v"(ht[1]) : "memory");
    HK1[21] = cat(tl[1], th[1]);
    asm volatile("v_mfma_f32_16x16x32_bf16 %0, %1, %2, %0" : "+v"(o[0][2]) : "a"(WA[20][0]), "v"(HX0[2]));
    asm volatile("ds_read_b64_tr_b16 %0, %1 offset:24576" : "=v"(tl[0]) : "v"(ht[0]) : "memory");
    asm volatile("ds_read_b64_tr_b16 %0, %1 offset:24576" : "=v"(th[0]) : "v"(ht[1]) : "memory");
    HK0[21] = cat(tl[0], th[0]);
    asm volatile("v_mfma_f32_16x16x32_bf16 %0, %1, %2, %0" : "+v"(o[1][2]) : "a"(WA[20][1]), "v"(HX0[2]));
    asm volatile("v_mfma_f32_16x16x32_bf16 %0, %1, %2, %0" : "+v"(o[2][2]) : "a"(WA[20][2]), "v"(HX0[2]));
    asm volatile("v_mfma_f32_16x16x32_bf16 %0, %1, %2, %0" : "+v"(o[3][2]) : "a"(WA[20][3]), "v"(HX0[2]));
    asm volatile("v_mfma_f32_16x16x32_bf16 %0, %1, %2, %0" : "+v"(o[4][2]) : "a"(WA[20][4]), "v"(HX0[2]));
    asm volatile("s_waitcnt lgkmcnt(0)" ::: "memory");
    unsigned f3v; const unsigned f3 = 3964928 + (b*4 + w)*128 + (lane & 3)*4;
    asm volatile("v_mfma_f32_16x16x32_bf16 %0, %1, %2, %0" : "+v"(o[0][3]) : "a"(WA[21][0]), "v"(HX1[3]));
    asm volatile("v_mfma_f32_16x16x32_bf16 %0, %1, %2, %0" : "+v"(o[1][3]) : "a"(WA[21][1]), "v"(HX1[3]));
    asm volatile("v_mfma_f32_16x16x32_bf16 %0, %1, %2, %0" : "+v"(o[2][3]) : "a"(WA[21][2]), "v"(HX1[3]));
    asm volatile("v_mfma_f32_16x16x32_bf16 %0, %1, %2, %0" : "+v"(o[3][3]) : "a"(WA[21][3]), "v"(HX1[3]));
    asm volatile("v_mfma_f32_16x16x32_bf16 %0, %1, %2, %0" : "+v"(o[4][3]) : "a"(WA[21][4]), "v"(HX1[3]));
    asm volatile("v_mfma_f32_16x16x32_bf16 %0, %1, %2, %0" : "+v"(o[0][2]) : "a"(WA[21][0]), "v"(HX1[2]));
    asm volatile("v_mfma_f32_16x16x32_bf16 %0, %1, %2, %0" : "+v"(o[1][2]) : "a"(WA[21][1]), "v"(HX1[2]));
    asm volatile("v_mfma_f32_16x16x32_bf16 %0, %1, %2, %0" : "+v"(o[2][2]) : "a"(WA[21][2]), "v"(HX1[2]));
    asm volatile("v_mfma_f32_16x16x32_bf16 %0, %1, %2, %0" : "+v"(o[3][2]) : "a"(WA[21][3]), "v"(HX1[2]));
    unsigned q3_0, q3_1, q3_2, q3_3, q3_4, q3_5, q3_6, q3_7, q3_8, q3_9; const unsigned ps3 = 15728640 + (((pd + 2*((kq + 3) & 3))*3 + 2)*4 + w)*2560;
    asm volatile("v_cvt_pk_bf16_f32 %0, %1, %2" : "=v"(q3_0) : "v"(o[0][3].x), "v"(o[0][3].y));
    asm volatile("v_mfma_f32_16x16x32_bf16 %0, %1, %2, %0" : "+v"(o[4][2]) : "a"(WA[21][4]), "v"(HX1[2]));
    asm volatile("v_cvt_pk_bf16_f32 %0, %1, %2" : "=v"(q3_1) : "v"(o[0][3].z), "v"(o[0][3].w));
    asm volatile("v_mfma_f32_16x16x32_bf16 %0, %1, %2, %0" : "+v"(o[0][1]) : "a"(WA[10][0]), "v"(HK1[10]));
    asm volatile("v_cvt_pk_bf16_f32 %0, %1, %2" : "=v"(q3_2) : "v"(o[1][3].x), "v"(o[1][3].y));
    asm volatile("v_mfma_f32_16x16x32_bf16 %0, %1, %2, %0" : "+v"(o[1][1]) : "a"(WA[10][1]), "v"(HK1[10]));
    asm volatile("v_cvt_pk_bf16_f32 %0, %1, %2" : "=v"(q3_3) : "v"(o[1][3].z), "v"(o[1][3].w));
    st128((u4{q3_0, q3_1, q3_2, q3_3}), rS, vl16, ps3, SC1);
    asm volatile("v_mfma_f32_16x16x32_bf16 %0, %1, %2, %0" : "+v"(o[2][1]) : "a"(WA[10][2]), "v"(HK1[10]));
    asm volatile("v_cvt_pk_bf16_f32 %0, %1, %2" : "=v"(q3_4) : "v"(o[2][3].x), "v"(o[2][3].y));
    asm volatile("v_mfma_f32_16x16x32_bf16 %0, %1, %2, %0" : "+v"(o[3][1]) : "a"(WA[10][3]), "v"(HK1[10]));
    asm volatile("v_cvt_pk_bf16_f32 %0, %1, %2" : "=v"(q3_5) : "v"(o[2][3].z), "v"(o[2][3].w));
    asm volatile("v_mfma_f32_16x16x32_bf16 %0, %1, %2, %0" : "+v"(o[4][1]) : "a"(WA[10][4]), "v"(HK1[10]));
    asm volatile("v_cvt_pk_bf16_f32 %0, %1, %2" : "=v"(q3_6) : "v"(o[3][3].x), "v"(o[3][3].y));
    asm volatile("v_mfma_f32_16x16x32_bf16 %0, %1, %2, %0" : "+v"(o[0][1]) : "a"(WA[11][0]), "v"(HK1[11]));
    asm volatile("v_cvt_pk_bf16_f32 %0, %1, %2" : "=v"(q3_7) : "v"(o[3][3].z), "v"(o[3][3].w));
    st128((u4{q3_4, q3_5, q3_6, q3_7}), rS, vl16 + 1024, ps3, SC1);
    asm volatile("v_mfma_f32_16x16x32_bf16 %0, %1, %2, %0" : "+v"(o[1][1]) : "a"(WA[11][1]), "v"(HK1[11]));
    asm volatile("v_cvt_pk_bf16_f32 %0, %1, %2" : "=v"(q3_8) : "v"(o[4][3].x), "v"(o[4][3].y));
    asm volatile("v_mfma_f32_16x16x32_bf16 %0, %1, %2, %0" : "+v"(o[2][1]) : "a"(WA[11][2]), "v"(HK1[11]));
    asm volatile("v_cvt_pk_bf16_f32 %0, %1, %2" : "=v"(q3_9) : "v"(o[4][3].z), "v"(o[4][3].w));
    __builtin_amdgcn_raw_buffer_store_b64(u2{q3_8, q3_9}, rS, lane*8 + 2048, ps3, SC1);
    asm volatile("v_mfma_f32_16x16x32_bf16 %0, %1, %2, %0" : "+v"(o[3][1]) : "a"(WA[11][3]), "v"(HK1[11]));
    unsigned q2_0, q2_1, q2_2, q2_3, q2_4, q2_5, q2_6, q2_7, q2_8, q2_9; const unsigned ps2 = 15728640 + (((pd + 2*((kq + 2) & 3))*3 + 1)*4 + w)*2560;
    asm volatile("v_cvt_pk_bf16_f32 %0, %1, %2" : "=v"(q2_0) : "v"(o[0][2].x), "v"(o[0][2].y));
    asm volatile("v_mfma_f32_16x16x32_bf16 %0, %1, %2, %0" : "+v"(o[4][1]) : "a"(WA[11][4]), "v"(HK1[11]));
    asm volatile("v_cvt_pk_bf16_f32 %0, %1, %2" : "=v"(q2_1) : "v"(o[0][2].z), "v"(o[0][2].w));
    asm volatile("v_mfma_f32_16x16x32_bf16 %0, %1, %2, %0" : "+v"(o[0][1]) : "a"(WA[12][0]), "v"(HK1[12]));
    asm volatile("v_cvt_pk_bf16_f32 %0, %1, %2" : "=v"(q2_2) : "v"(o[1][2].x), "v"(o[1][2].y));
    asm volatile("v_mfma_f32_16x16x32_bf16 %0, %1, %2, %0" : "+v"(o[1][1]) : "a"(WA[12][1]), "v"(HK1[12]));
    asm volatile("v_cvt_pk_bf16_f32 %0, %1, %2" : "=v"(q2_3) : "v"(o[1][2].z), "v"(o[1][2].w));
    st128((u4{q2_0, q2_1, q2_2, q2_3}), rS, vl16, ps2, SC1);
    asm volatile("v_mfma_f32_16x16x32_bf16 %0, %1, %2, %0" : "+v"(o[2][1]) : "a"(WA[12][2]), "v"(HK1[12]));
    asm volatile("v_cvt_pk_bf16_f32 %0, %1, %2" : "=v"(q2_4) : "v"(o[2][2].x), "v"(o[2][2].y));
    asm volatile("v_mfma_f32_16x16x32_bf16 %0, %1, %2, %0" : "+v"(o[3][1]) : "a"(WA[12][3]), "v"(HK1[12]));
    asm volatile("v_cvt_pk_bf16_f32 %0, %1, %2" : "=v"(q2_5) : "v"(o[2][2].z), "v"(o[2][2].w));
    asm volatile("v_mfma_f32_16x16x32_bf16 %0, %1, %2, %0" : "+v"(o[4][1]) : "a"(WA[12][4]), "v"(HK1[12]));
    asm volatile("v_cvt_pk_bf16_f32 %0, %1, %2" : "=v"(q2_6) : "v"(o[3][2].x), "v"(o[3][2].y));
    asm volatile("v_mfma_f32_16x16x32_bf16 %0, %1, %2, %0" : "+v"(o[0][1]) : "a"(WA[13][0]), "v"(HK1[13]));
    asm volatile("v_cvt_pk_bf16_f32 %0, %1, %2" : "=v"(q2_7) : "v"(o[3][2].z), "v"(o[3][2].w));
    st128((u4{q2_4, q2_5, q2_6, q2_7}), rS, vl16 + 1024, ps2, SC1);
    asm volatile("v_mfma_f32_16x16x32_bf16 %0, %1, %2, %0" : "+v"(o[1][1]) : "a"(WA[13][1]), "v"(HK1[13]));
    asm volatile("v_cvt_pk_bf16_f32 %0, %1, %2" : "=v"(q2_8) : "v"(o[4][2].x), "v"(o[4][2].y));
    asm volatile("v_mfma_f32_16x16x32_bf16 %0, %1, %2, %0" : "+v"(o[2][1]) : "a"(WA[13][2]), "v"(HK1[13]));
    asm volatile("v_cvt_pk_bf16_f32 %0, %1, %2" : "=v"(q2_9) : "v"(o[4][2].z), "v"(o[4][2].w));
    __builtin_amdgcn_raw_buffer_store_b64(u2{q2_8, q2_9}, rS, lane*8 + 2048, ps2, SC1);
    asm volatile("v_mfma_f32_16x16x32_bf16 %0, %1, %2, %0" : "+v"(o[3][1]) : "a"(WA[13][3]), "v"(HK1[13]));
    asm volatile("v_mfma_f32_16x16x32_bf16 %0, %1, %2, %0" : "+v"(o[4][1]) : "a"(WA[13][4]), "v"(HK1[13]));
    asm volatile("v_mfma_f32_16x16x32_bf16 %0, %1, %2, %0" : "+v"(o[0][1]) : "a"(WA[14][0]), "v"(HK1[14]));
    asm volatile("v_mfma_f32_16x16x32_bf16 %0, %1, %2, %0" : "+v"(o[1][1]) : "a"(WA[14][1]), "v"(HK1[14]));
    asm volatile("v_mfma_f32_16x16x32_bf16 %0, %1, %2, %0" : "+v"(o[2][1]) : "a"(WA[14][2]), "v"(HK1[14]));
    asm volatile("v_mfma_f32_16x16x32_bf16 %0, %1, %2, %0" : "+v"(o[3][1]) : "a"(WA[14][3]), "v"(HK1[14]));
    asm volatile("v_mfma_f32_16x16x32_bf16 %0, %1, %2, %0" : "+v"(o[4][1]) : "a"(WA[14][4]), "v"(HK1[14]));
    asm volatile("v_mfma_f32_16x16x32_bf16 %0, %1, %2, %0" : "+v"(o[0][1]) : "a"(WA[15][0]), "v"(HK1[15]));
    asm volatile("v_mfma_f32_16x16x32_bf16 %0, %1, %2, %0" : "+v"(o[1][1]) : "a"(WA[15][1]), "v"(HK1[15]));
    asm volatile("v_mfma_f32_16x16x32_bf16 %0, %1, %2, %0" : "+v"(o[2][1]) : "a"(WA[15][2]), "v"(HK1[15]));
    asm volatile("v_mfma_f32_16x16x32_bf16 %0, %1, %2, %0" : "+v"(o[3][1]) : "a"(WA[15][3]), "v"(HK1[15]));
    asm volatile("v_mfma_f32_16x16x32_bf16 %0, %1, %2, %0" : "+v"(o[4][1]) : "a"(WA[15][4]), "v"(HK1[15]));
    asm volatile("v_mfma_f32_16x16x32_bf16 %0, %1, %2, %0" : "+v"(o[0][1]) : "a"(WA[16][0]), "v"(HK1[16]));
    asm volatile("v_mfma_f32_16x16x32_bf16 %0, %1, %2, %0" : "+v"(o[1][1]) : "a"(WA[16][1]), "v"(HK1[16]));
    asm volatile("v_mfma_f32_16x16x32_bf16 %0, %1, %2, %0" : "+v"(o[2][1]) : "a"(WA[16][2]), "v"(HK1[16]));
    asm volatile("v_mfma_f32_16x16x32_bf16 %0, %1, %2, %0" : "+v"(o[3][1]) : "a"(WA[16][3]), "v"(HK1[16]));
    asm volatile("v_mfma_f32_16x16x32_bf16 %0, %1, %2, %0" : "+v"(o[4][1]) : "a"(WA[16][4]), "v"(HK1[16]));
    asm volatile("v_mfma_f32_16x16x32_bf16 %0, %1, %2, %0" : "+v"(o[0][1]) : "a"(WA[17][0]), "v"(HK1[17]));
    asm volatile("v_mfma_f32_16x16x32_bf16 %0, %1, %2, %0" : "+v"(o[1][1]) : "a"(WA[17][1]), "v"(HK1[17]));
    asm volatile("v_mfma_f32_16x16x32_bf16 %0, %1, %2, %0" : "+v"(o[2][1]) : "a"(WA[17][2]), "v"(HK1[17]));
    asm volatile("v_mfma_f32_16x16x32_bf16 %0, %1, %2, %0" : "+v"(o[3][1]) : "a"(WA[17][3]), "v"(HK1[17]));
    asm volatile("v_mfma_f32_16x16x32_bf16 %0, %1, %2, %0" : "+v"(o[4][1]) : "a"(WA[17][4]), "v"(HK1[17]));
    asm volatile("v_mfma_f32_16x16x32_bf16 %0, %1, %2, %0" : "+v"(o[0][1]) : "a"(WA[18][0]), "v"(HK1[18]));
    asm volatile("v_mfma_f32_16x16x32_bf16 %0, %1, %2, %0" : "+v"(o[1][1]) : "a"(WA[18][1]), "v"(HK1[18]));
    asm volatile("v_mfma_f32_16x16x32_bf16 %0, %1, %2, %0" : "+v"(o[2][1]) : "a"(WA[18][2]), "v"(HK1[18]));
    asm volatile("v_mfma_f32_16x16x32_bf16 %0, %1, %2, %0" : "+v"(o[3][1]) : "a"(WA[18][3]), "v"(HK1[18]));
    asm volatile("v_mfma_f32_16x16x32_bf16 %0, %1, %2, %0" : "+v"(o[4][1]) : "a"(WA[18][4]), "v"(HK1[18]));
    asm volatile("v_mfma_f32_16x16x32_bf16 %0, %1, %2, %0" : "+v"(o[0][1]) : "a"(WA[19][0]), "v"(HK1[19]));
    asm volatile("v_mfma_f32_16x16x32_bf16 %0, %1, %2, %0" : "+v"(o[1][1]) : "a"(WA[19][1]), "v"(HK1[19]));
    asm volatile("v_mfma_f32_16x16x32_bf16 %0, %1, %2, %0" : "+v"(o[2][1]) : "a"(WA[19][2]), "v"(HK1[19]));
    asm volatile("v_mfma_f32_16x16x32_bf16 %0, %1, %2, %0" : "+v"(o[3][1]) : "a"(WA[19][3]), "v"(HK1[19]));
    asm volatile("v_mfma_f32_16x16x32_bf16 %0, %1, %2, %0" : "+v"(o[4][1]) : "a"(WA[19][4]), "v"(HK1[19]));
    asm volatile("v_mfma_f32_16x16x32_bf16 %0, %1, %2, %0" : "+v"(o[0][1]) : "a"(WA[20][0]), "v"(HK1[20]));
    asm volatile("v_mfma_f32_16x16x32_bf16 %0, %1, %2, %0" : "+v"(o[1][1]) : "a"(WA[20][1]), "v"(HK1[20]));
    asm volatile("v_mfma_f32_16x16x32_bf16 %0, %1, %2, %0" : "+v"(o[2][1]) : "a"(WA[20][2]), "v"(HK1[20]));
    asm volatile("v_mfma_f32_16x16x32_bf16 %0, %1, %2, %0" : "+v"(o[3][1]) : "a"(WA[20][3]), "v"(HK1[20]));
    asm volatile("v_mfma_f32_16x16x32_bf16 %0, %1, %2, %0" : "+v"(o[4][1]) : "a"(WA[20][4]), "v"(HK1[20]));
    asm volatile("v_mfma_f32_16x16x32_bf16 %0, %1, %2, %0" : "+v"(o[0][1]) : "a"(WA[21][0]), "v"(HK1[21]));
    asm volatile("v_mfma_f32_16x16x32_bf16 %0, %1, %2, %0" : "+v"(o[1][1]) : "a"(WA[21][1]), "v"(HK1[21]));
    asm volatile("v_mfma_f32_16x16x32_bf16 %0, %1, %2, %0" : "+v"(o[2][1]) : "a"(WA[21][2]), "v"(HK1[21]));
    asm volatile("v_mfma_f32_16x16x32_bf16 %0, %1, %2, %0" : "+v"(o[3][1]) : "a"(WA[21][3]), "v"(HK1[21]));
    asm volatile("v_mfma_f32_16x16x32_bf16 %0, %1, %2, %0" : "+v"(o[4][1]) : "a"(WA[21][4]), "v"(HK1[21]));
    asm volatile("v_mfma_f32_16x16x32_bf16 %0, %1, %2, %0" : "+v"(o[0][0]) : "a"(WA[10][0]), "v"(HK0[10]));
    asm volatile("v_mfma_f32_16x16x32_bf16 %0, %1, %2, %0" : "+v"(o[1][0]) : "a"(WA[10][1]), "v"(HK0[10]));
    asm volatile("v_mfma_f32_16x16x32_bf16 %0, %1, %2, %0" : "+v"(o[2][0]) : "a"(WA[10][2]), "v"(HK0[10]));
    asm volatile("v_mfma_f32_16x16x32_bf16 %0, %1, %2, %0" : "+v"(o[3][0]) : "a"(WA[10][3]), "v"(HK0[10]));
    unsigned q1_0, q1_1, q1_2, q1_3, q1_4, q1_5, q1_6, q1_7, q1_8, q1_9; const unsigned ps1 = 15728640 + (((pd + 2*((kq + 1) & 3))*3 + 0)*4 + w)*2560;
    asm volatile("v_cvt_pk_bf16_f32 %0, %1, %2" : "=v"(q1_0) : "v"(o[0][1].x), "v"(o[0][1].y));
    asm volatile("v_mfma_f32_16x16x32_bf16 %0, %1, %2, %0" : "+v"(o[4][0]) : "a"(WA[10][4]), "v"(HK0[10]));
    asm volatile("v_cvt_pk_bf16_f32 %0, %1, %2" : "=v"(q1_1) : "v"(o[0][1].z), "v"(o[0][1].w));
    asm volatile("v_mfma_f32_16x16x32_bf16 %0, %1, %2, %0" : "+v"(o[0][0]) : "a"(WA[11][0]), "v"(HK0[11]));
    asm volatile("v_cvt_pk_bf16_f32 %0, %1, %2" : "=v"(q1_2) : "v"(o[1][1].x), "v"(o[1][1].y));
    asm volatile("v_mfma_f32_16x16x32_bf16 %0, %1, %2, %0" : "+v"(o[1][0]) : "a"(WA[11][1]), "v"(HK0[11]));
    asm volatile("v_cvt_pk_bf16_f32 %0, %1, %2" : "=v"(q1_3) : "v"(o[1][1].z), "v"(o[1][1].w));
    st128((u4{q1_0, q1_1, q1_2, q1_3}), rS, vl16, ps1, SC1);
    asm volatile("v_mfma_f32_16x16x32_bf16 %0, %1, %2, %0" : "+v"(o[2][0]) : "a"(WA[11][2]), "v"(HK0[11]));
    asm volatile("v_cvt_pk_bf16_f32 %0, %1, %2" : "=v"(q1_4) : "v"(o[2][1].x), "v"(o[2][1].y));
    asm volatile("v_mfma_f32_16x16x32_bf16 %0, %1, %2, %0" : "+v"(o[3][0]) : "a"(WA[11][3]), "v"(HK0[11]));
    asm volatile("v_cvt_pk_bf16_f32 %0, %1, %2" : "=v"(q1_5) : "v"(o[2][1].z), "v"(o[2][1].w));
    asm volatile("v_mfma_f32_16x16x32_bf16 %0, %1, %2, %0" : "+v"(o[4][0]) : "a"(WA[11][4]), "v"(HK0[11]));
    asm volatile("v_cvt_pk_bf16_f32 %0, %1, %2" : "=v"(q1_6) : "v"(o[3][1].x), "v"(o[3][1].y));
    asm volatile("v_mfma_f32_16x16x32_bf16 %0, %1, %2, %0" : "+v"(o[0][0]) : "a"(WA[12][0]), "v"(HK0[12]));
    asm volatile("v_cvt_pk_bf16_f32 %0, %1, %2" : "=v"(q1_7) : "v"(o[3][1].z), "v"(o[3][1].w));
    st128((u4{q1_4, q1_5, q1_6, q1_7}), rS, vl16 + 1024, ps1, SC1);
    asm volatile("s_waitcnt vmcnt(2)" ::: "memory");
    __builtin_amdgcn_raw_buffer_store_b32(tag, rS, (lane == 1 || lane == 2) ? 3964928 + ((pd + 2*((kq + 1 + lane) & 3))*4 + w)*128 + lane*4 : OOB, 0, SC1);
    asm volatile("v_mfma_f32_16x16x32_bf16 %0, %1, %2, %0" : "+v"(o[1][0]) : "a"(WA[12][1]), "v"(HK0[12]));
    asm volatile("v_cvt_pk_bf16_f32 %0, %1, %2" : "=v"(q1_8) : "v"(o[4][1].x), "v"(o[4][1].y));
    asm volatile("v_mfma_f32_16x16x32_bf16 %0, %1, %2, %0" : "+v"(o[2][0]) : "a"(WA[12][2]), "v"(HK0[12]));
    asm volatile("v_cvt_pk_bf16_f32 %0, %1, %2" : "=v"(q1_9) : "v"(o[4][1].z), "v"(o[4][1].w));
    __builtin_amdgcn_raw_buffer_store_b64(u2{q1_8, q1_9}, rS, lane*8 + 2048, ps1, SC1);
    asm volatile("v_mfma_f32_16x16x32_bf16 %0, %1, %2, %0" : "+v"(o[3][0]) : "a"(WA[12][3]), "v"(HK0[12]));
    asm volatile("v_mfma_f32_16x16x32_bf16 %0, %1, %2, %0" : "+v"(o[4][0]) : "a"(WA[12][4]), "v"(HK0[12]));
    asm volatile("v_mfma_f32_16x16x32_bf16 %0, %1, %2, %0" : "+v"(o[0][0]) : "a"(WA[13][0]), "v"(HK0[13]));
    asm volatile("v_mfma_f32_16x16x32_bf16 %0, %1, %2, %0" : "+v"(o[1][0]) : "a"(WA[13][1]), "v"(HK0[13]));
    asm volatile("v_mfma_f32_16x16x32_bf16 %0, %1, %2, %0" : "+v"(o[2][0]) : "a"(WA[13][2]), "v"(HK0[13]));
    asm volatile("v_mfma_f32_16x16x32_bf16 %0, %1, %2, %0" : "+v"(o[3][0]) : "a"(WA[13][3]), "v"(HK0[13]));
    asm volatile("v_mfma_f32_16x16x32_bf16 %0, %1, %2, %0" : "+v"(o[4][0]) : "a"(WA[13][4]), "v"(HK0[13]));
    asm volatile("v_mfma_f32_16x16x32_bf16 %0, %1, %2, %0" : "+v"(o[0][0]) : "a"(WA[14][0]), "v"(HK0[14]));
    asm volatile("v_mfma_f32_16x16x32_bf16 %0, %1, %2, %0" : "+v"(o[1][0]) : "a"(WA[14][1]), "v"(HK0[14]));
    asm volatile("v_mfma_f32_16x16x32_bf16 %0, %1, %2, %0" : "+v"(o[2][0]) : "a"(WA[14][2]), "v"(HK0[14]));
    asm volatile("v_mfma_f32_16x16x32_bf16 %0, %1, %2, %0" : "+v"(o[3][0]) : "a"(WA[14][3]), "v"(HK0[14]));
    asm volatile("v_mfma_f32_16x16x32_bf16 %0, %1, %2, %0" : "+v"(o[4][0]) : "a"(WA[14][4]), "v"(HK0[14]));
    asm volatile("v_mfma_f32_16x16x32_bf16 %0, %1, %2, %0" : "+v"(o[0][0]) : "a"(WA[15][0]), "v"(HK0[15]));
    asm volatile("v_mfma_f32_16x16x32_bf16 %0, %1, %2, %0" : "+v"(o[1][0]) : "a"(WA[15][1]), "v"(HK0[15]));
    asm volatile("v_mfma_f32_16x16x32_bf16 %0, %1, %2, %0" : "+v"(o[2][0]) : "a"(WA[15][2]), "v"(HK0[15]));
    asm volatile("v_mfma_f32_16x16x32_bf16 %0, %1, %2, %0" : "+v"(o[3][0]) : "a"(WA[15][3]), "v"(HK0[15]));
    asm volatile("v_mfma_f32_16x16x32_bf16 %0, %1, %2, %0" : "+v"(o[4][0]) : "a"(WA[15][4]), "v"(HK0[15]));
    asm volatile("v_mfma_f32_16x16x32_bf16 %0, %1, %2, %0" : "+v"(o[0][0]) : "a"(WA[16][0]), "v"(HK0[16]));
    asm volatile("v_mfma_f32_16x16x32_bf16 %0, %1, %2, %0" : "+v"(o[1][0]) : "a"(WA[16][1]), "v"(HK0[16]));
    asm volatile("v_mfma_f32_16x16x32_bf16 %0, %1, %2, %0" : "+v"(o[2][0]) : "a"(WA[16][2]), "v"(HK0[16]));
    asm volatile("v_mfma_f32_16x16x32_bf16 %0, %1, %2, %0" : "+v"(o[3][0]) : "a"(WA[16][3]), "v"(HK0[16]));
    asm volatile("v_mfma_f32_16x16x32_bf16 %0, %1, %2, %0" : "+v"(o[4][0]) : "a"(WA[16][4]), "v"(HK0[16]));
    asm volatile("v_mfma_f32_16x16x32_bf16 %0, %1, %2, %0" : "+v"(o[0][0]) : "a"(WA[17][0]), "v"(HK0[17]));
    asm volatile("v_mfma_f32_16x16x32_bf16 %0, %1, %2, %0" : "+v"(o[1][0]) : "a"(WA[17][1]), "v"(HK0[17]));
    asm volatile("v_mfma_f32_16x16x32_bf16 %0, %1, %2, %0" : "+v"(o[2][0]) : "a"(WA[17][2]), "v"(HK0[17]));
    asm volatile("v_mfma_f32_16x16x32_bf16 %0, %1, %2, %0" : "+v"(o[3][0]) : "a"(WA[17][3]), "v"(HK0[17]));
    asm volatile("v_mfma_f32_16x16x32_bf16 %0, %1, %2, %0" : "+v"(o[4][0]) : "a"(WA[17][4]), "v"(HK0[17]));
    asm volatile("v_mfma_f32_16x16x32_bf16 %0, %1, %2, %0" : "+v"(o[0][0]) : "a"(WA[18][0]), "v"(HK0[18]));
    asm volatile("buffer_load_dword %0, %1, %2, 0 offen sc1" : "=v"(f3v) : "v"(f3), "s"(rS) : "memory");
    asm volatile("v_mfma_f32_16x16x32_bf16 %0, %1, %2, %0" : "+v"(o[1][0]) : "a"(WA[18][1]), "v"(HK0[18]));
    asm volatile("v_mfma_f32_16x16x32_bf16 %0, %1, %2, %0" : "+v"(o[2][0]) : "a"(WA[18][2]), "v"(HK0[18]));
    asm volatile("v_mfma_f32_16x16x32_bf16 %0, %1, %2, %0" : "+v"(o[3][0]) : "a"(WA[18][3]), "v"(HK0[18]));
    asm volatile("v_mfma_f32_16x16x32_bf16 %0, %1, %2, %0" : "+v"(o[4][0]) : "a"(WA[18][4]), "v"(HK0[18]));
    asm volatile("v_mfma_f32_16x16x32_bf16 %0, %1, %2, %0" : "+v"(o[0][0]) : "a"(WA[19][0]), "v"(HK0[19]));
    asm volatile("v_mfma_f32_16x16x32_bf16 %0, %1, %2, %0" : "+v"(o[1][0]) : "a"(WA[19][1]), "v"(HK0[19]));
    asm volatile("v_mfma_f32_16x16x32_bf16 %0, %1, %2, %0" : "+v"(o[2][0]) : "a"(WA[19][2]), "v"(HK0[19]));
    asm volatile("v_mfma_f32_16x16x32_bf16 %0, %1, %2, %0" : "+v"(o[3][0]) : "a"(WA[19][3]), "v"(HK0[19]));
    asm volatile("v_mfma_f32_16x16x32_bf16 %0, %1, %2, %0" : "+v"(o[4][0]) : "a"(WA[19][4]), "v"(HK0[19]));
    asm volatile("v_mfma_f32_16x16x32_bf16 %0, %1, %2, %0" : "+v"(o[0][0]) : "a"(WA[20][0]), "v"(HK0[20]));
    asm volatile("v_mfma_f32_16x16x32_bf16 %0, %1, %2, %0" : "+v"(o[1][0]) : "a"(WA[20][1]), "v"(HK0[20]));
    asm volatile("v_mfma_f32_16x16x32_bf16 %0, %1, %2, %0" : "+v"(o[2][0]) : "a"(WA[20][2]), "v"(HK0[20]));
    asm volatile("v_mfma_f32_16x16x32_bf16 %0, %1, %2, %0" : "+v"(o[3][0]) : "a"(WA[20][3]), "v"(HK0[20]));
    asm volatile("v_mfma_f32_16x16x32_bf16 %0, %1, %2, %0" : "+v"(o[4][0]) : "a"(WA[20][4]), "v"(HK0[20]));
    asm volatile("v_mfma_f32_16x16x32_bf16 %0, %1, %2, %0" : "+v"(o[0][0]) : "a"(WA[21][0]), "v"(HK0[21]));
    asm volatile("v_mfma_f32_16x16x32_bf16 %0, %1, %2, %0" : "+v"(o[1][0]) : "a"(WA[21][1]), "v"(HK0[21]));
    asm volatile("v_mfma_f32_16x16x32_bf16 %0, %1, %2, %0" : "+v"(o[2][0]) : "a"(WA[21][2]), "v"(HK0[21]));
    asm volatile("v_mfma_f32_16x16x32_bf16 %0, %1, %2, %0" : "+v"(o[3][0]) : "a"(WA[21][3]), "v"(HK0[21]));
    asm volatile("v_mfma_f32_16x16x32_bf16 %0, %1, %2, %0" : "+v"(o[4][0]) : "a"(WA[21][4]), "v"(HK0[21]));
    __builtin_amdgcn_sched_barrier(0);
    asm volatile("s_waitcnt vmcnt(1)" ::: "memory");
    __builtin_amdgcn_raw_buffer_store_b32(tag, rS, lane == 0 ? 3964928 + ((pd + 2*((kq + 1 + lane) & 3))*4 + w)*128 + lane*4 : OOB, 0, SC1);
    { u4 ra[3], rb[3]; u2 rc[3]; const unsigned pb = 15728640 + (b*12 + w)*2560;
    asm volatile("s_waitcnt vmcnt(1)" ::: "memory");
    asm volatile("" : "+v"(f3v));
      if (!__all((lane & 3) == 0 || (lane & 3) == 3 || f3v == tag)) spin1(rS, f3, (lane & 3) == 0 || (lane & 3) == 3, tag);
      asm volatile("buffer_load_dwordx4 %0, %1, %2, %3 offen offset:0 sc1" : "=v"(ra[2]) : "v"(vl16), "s"(rS), "s"(pb + 20480) : "memory");
      asm volatile("buffer_load_dwordx4 %0, %1, %2, %3 offen offset:1024 sc1" : "=v"(rb[2]) : "v"(vl16), "s"(rS), "s"(pb + 20480) : "memory");
      asm volatile("buffer_load_dwordx2 %0, %1, %2, %3 offen offset:2048 sc1" : "=v"(rc[2]) : "v"(lane*8), "s"(rS), "s"(pb + 20480) : "memory");
      asm volatile("buffer_load_dwordx4 %0, %1, %2, %3 offen offset:0 sc1" : "=v"(ra[1]) : "v"(vl16), "s"(rS), "s"(pb + 10240) : "memory");
      asm volatile("buffer_load_dwordx4 %0, %1, %2, %3 offen offset:1024 sc1" : "=v"(rb[1]) : "v"(vl16), "s"(rS), "s"(pb + 10240) : "memory");
      asm volatile("buffer_load_dwordx2 %0, %1, %2, %3 offen offset:2048 sc1" : "=v"(rc[1]) : "v"(lane*8), "s"(rS), "s"(pb + 10240) : "memory");
    asm volatile("buffer_load_dword %0, %1, %2, 0 offen sc1" : "=v"(f3v) : "v"(f3), "s"(rS) : "memory");
    asm volatile("s_nop 7");
    asm volatile("" : "+v"(es), "+v"(ex[0]), "+v"(ex[1]), "+v"(ex[2]), "+v"(ex[3]), "+v"(ex[4]), "+v"(o[0][0]), "+v"(o[1][0]), "+v"(o[2][0]), "+v"(o[3][0]), "+v"(o[4][0]));
    { const float mean = rbf(__uint_as_float(es.x)), rs = ln_rstd(__uint_as_float(es.y));
      { const f4 xx = unpk4(ex[0]); o[0][0] += xx + (xx - mean)*rs*unpk4(LDS(u2, 10112 + (80*wn + 4*g)*2)) + unpk4(LDS(u2, 10496 + (80*wn + 4*g)*2)); }
      { const f4 xx = unpk4(ex[1]); o[1][0] += xx + (xx - mean)*rs*unpk4(LDS(u2, 10144 + (80*wn + 4*g)*2)) + unpk4(LDS(u2, 10528 + (80*wn + 4*g)*2)); }
      { const f4 xx = unpk4(ex[2]); o[2][0] += xx + (xx - mean)*rs*unpk4(LDS(u2, 10176 + (80*wn + 4*g)*2)) + unpk4(LDS(u2, 10560 + (80*wn + 4*g)*2)); }
      { const f4 xx = unpk4(ex[3]); o[3][0] += xx + (xx - mean)*rs*unpk4(LDS(u2, 10208 + (80*wn + 4*g)*2)) + unpk4(LDS(u2, 10592 + (80*wn + 4*g)*2)); }
      { const f4 xx = unpk4(ex[4]); o[4][0] += xx + (xx - mean)*rs*unpk4(LDS(u2, 10240 + (80*wn + 4*g)*2)) + unpk4(LDS(u2, 10624 + (80*wn + 4*g)*2)); }
    }
    asm volatile("" : "+v"(o[0][0]), "+v"(o[1][0]), "+v"(o[2][0]), "+v"(o[3][0]), "+v"(o[4][0]));
    asm volatile("s_waitcnt vmcnt(0)" ::: "memory");
    asm volatile("" : "+v"(f3v));
      if (!__all((lane & 3) != 0 || f3v == tag)) spin1(rS, f3, (lane & 3) != 0, tag);
      asm volatile("buffer_load_dwordx4 %0, %1, %2, %3 offen offset:0 sc1" : "=v"(ra[0]) : "v"(vl16), "s"(rS), "s"(pb + 0) : "memory");
      asm volatile("buffer_load_dwordx4 %0, %1, %2, %3 offen offset:1024 sc1" : "=v"(rb[0]) : "v"(vl16), "s"(rS), "s"(pb + 0) : "memory");
      asm volatile("buffer_load_dwordx2 %0, %1, %2, %3 offen offset:2048 sc1" : "=v"(rc[0]) : "v"(lane*8), "s"(rS), "s"(pb + 0) : "memory");
    asm volatile("" : "+v"(ra[1]), "+v"(ra[2]), "+v"(rb[1]), "+v"(rb[2]), "+v"(rc[1]), "+v"(rc[2]));
      o[0][0] += unpk4(u2{ra[2].x, ra[2].y}) + unpk4(u2{ra[1].x, ra[1].y});
      o[1][0] += unpk4(u2{ra[2].z, ra[2].w}) + unpk4(u2{ra[1].z, ra[1].w});
      o[2][0] += unpk4(u2{rb[2].x, rb[2].y}) + unpk4(u2{rb[1].x, rb[1].y});
      o[3][0] += unpk4(u2{rb[2].z, rb[2].w}) + unpk4(u2{rb[1].z, rb[1].w});
      o[4][0] += unpk4(rc[2]) + unpk4(rc[1]);
    asm volatile("" : "+v"(o[0][0]), "+v"(o[1][0]), "+v"(o[2][0]), "+v"(o[3][0]), "+v"(o[4][0]));
    asm volatile("s_waitcnt vmcnt(0)" ::: "memory");
    asm volatile("" : "+v"(ra[0]), "+v"(rb[0]), "+v"(rc[0]));
      const rsrc_t rO = mk(out, 2048*640*4);
      { const f4 a = o[0][0] + unpk4(u2{ra[0].x, ra[0].y}); st128(__builtin_bit_cast(u4, a), rO, 2*xvo + 0, 0, 16); }
      { const f4 a = o[1][0] + unpk4(u2{ra[0].z, ra[0].w}); st128(__builtin_bit_cast(u4, a), rO, 2*xvo + 64, 0, 16); }
      { const f4 a = o[2][0] + unpk4(u2{rb[0].x, rb[0].y}); st128(__builtin_bit_cast(u4, a), rO, 2*xvo + 128, 0, 16); }
      { const f4 a = o[3][0] + unpk4(u2{rb[0].z, rb[0].w}); st128(__builtin_bit_cast(u4, a), rO, 2*xvo + 192, 0, 16); }
      { const f4 a = o[4][0] + unpk4(rc[0]); st128(__builtin_bit_cast(u4, a), rO, 2*xvo + 256, 0, 16); }
    }
    asm volatile(".Lfk_end1:");
  }
}

extern "C" void kernel_launch(void* const* d_in, const int* in_sizes, int n_in, void* d_out, int out_size, void* d_ws, size_t ws_size,
                              hipStream_t stream) {
  s5_mixer_fused<<<256, 256, 0, stream>>>((const u16*)d_in[0], (char*)d_ws, (const u16*)d_in[3], (const u16*)d_in[4], (const u16*)d_in[10],
                                          (const u16*)d_in[12], (const u16*)d_in[11], (const u16*)d_in[8], (const u16*)d_in[13], (const u16*)d_in[1],
                                          (const u16*)d_in[2], (const u16*)d_in[7], (const u16*)d_in[5], (const u16*)d_in[6], (float*)d_out);
}
```
